```python
import torch

def _softmax(x, axis=-1):
    return torch.softmax(x, dim=axis)
def _gelu(x):
    return torch.nn.functional.gelu(x, approximate="tanh")
def _silu(x):
    return torch.nn.functional.silu(x)
def _sigmoid(x):
    return torch.sigmoid(x)
def _layernorm(x, axis=-1, eps=1e-5):
    m = x.mean(dim=axis, keepdim=True)
    v = x.var(dim=axis, keepdim=True, unbiased=False)
    return (x - m) / torch.sqrt(v + eps)
def _scatter_add(tgt, idx, src, axis=0):
    return tgt.clone().index_add_(axis, idx, src)
def _segment_sum(src, seg, n):
    out = torch.zeros((n,) + src.shape[1:], dtype=src.dtype)
    return out.index_add_(0, seg, src)
def _pad(x, pw):
    flat = tuple(v for pair in reversed(pw) for v in pair)
    return torch.nn.functional.pad(x, flat)

kernel_name = 'parallel_sum__grouped_kv_mqa__glu_gated_swiglu__prompt'

def setup_inputs():

    _w = (lambda a: (lambda t: t.to(torch.bfloat16) if t.is_floating_point() else t)(torch.as_tensor(a)))
    def _A_setup():
        B, S, D, H = 1, 2048, 640, 32
        Dh = D // H
        return {
            "x": _w((lambda *s: torch.randn(*s, dtype=torch.bfloat16) * 0.05)(B, S, D)),
            "wq": _w((lambda *s: torch.randn(*s, dtype=torch.bfloat16) * 0.05)(D, H * Dh)),
            "wk": _w((lambda *s: torch.randn(*s, dtype=torch.bfloat16) * 0.05)(D, Dh)),
            "wv": _w((lambda *s: torch.randn(*s, dtype=torch.bfloat16) * 0.05)(D, Dh)),
            "wo": _w((lambda *s: torch.randn(*s, dtype=torch.bfloat16) * 0.05)(H * Dh, D)),
        }
    _A_in = _A_setup()

    def _B_setup():
        B, S, D, F = 1, 2048, 640, 1536
        return {
            "x": _w((lambda *s: torch.randn(*s, dtype=torch.bfloat16) * 0.05)(B, S, D)),
            "w1": _w((lambda *s: torch.randn(*s, dtype=torch.bfloat16) * 0.4)(D, F) * (2.0 / D) ** 0.5),
            "w3": _w((lambda *s: torch.randn(*s, dtype=torch.bfloat16) * 0.4)(D, F) * (2.0 / D) ** 0.5),
            "w2": _w((lambda *s: torch.randn(*s, dtype=torch.bfloat16) * 0.4)(F, D) * (2.0 / F) ** 0.5),
        }
    _B_in = _B_setup()

    return {
        "A_x": _A_in["x"],
        "A_wq": _A_in["wq"],
        "A_wk": _A_in["wk"],
        "A_wv": _A_in["wv"],
        "A_wo": _A_in["wo"],
        "B_x": _B_in["x"],
        "B_w1": _B_in["w1"],
        "B_w3": _B_in["w3"],
        "B_w2": _B_in["w2"],
    }

def reference(A_x, A_wq, A_wk, A_wv, A_wo, B_x, B_w1, B_w3, B_w2):
    def _A_ref(x, wq, wk, wv, wo):
        B, S, D = x.shape
        H = 32
        Dh = D // H
        q = (lambda x, *d: x.permute(*d))((x @ wq).reshape(B, S, H, Dh), 0, 2, 1, 3)
        k = x @ wk
        v = x @ wv
        scores = torch.einsum("bhqd,bkd->bhqk", q, k) * (Dh ** -0.5)
        p = _softmax(scores, axis=-1)
        o = torch.einsum("bhqk,bkd->bhqd", p, v)
        return (lambda x, *d: x.permute(*d))(o, 0, 2, 1, 3).reshape(B, S, H * Dh) @ wo

    def _B_ref(x, w1, w3, w2):
        gate = torch.einsum("bsd,df->bsf", x, w1)
        up = torch.einsum("bsd,df->bsf", x, w3)
        h = _silu(gate) * up
        out = torch.einsum("bsf,fd->bsd", h, w2)
        return out

    x = A_x
    xn = _layernorm(x, axis=-1)
    return x + _A_ref(xn, A_wq, A_wk, A_wv, A_wo) + _B_ref(xn, B_w1, B_w3, B_w2)
```

```cpp
#include <hip/hip_runtime.h>
#include <hip/hip_bf16.h>
#include <stdint.h>

typedef short bf16x8 __attribute__((ext_vector_type(8)));
typedef short s16x4 __attribute__((ext_vector_type(4)));
typedef float f32x2 __attribute__((ext_vector_type(2)));
typedef float f32x4 __attribute__((ext_vector_type(4)));
typedef float f32x16 __attribute__((ext_vector_type(16)));
typedef unsigned u32x4 __attribute__((ext_vector_type(4)));
typedef unsigned u32x2 __attribute__((ext_vector_type(2)));
typedef __bf16 bf16x2_t __attribute__((ext_vector_type(2)));
typedef unsigned short u16;
#define LAS __attribute__((address_space(3)))
#define DI __device__ __forceinline__
#define SBAR() __builtin_amdgcn_sched_barrier(0)
#define PIN(x) asm volatile("" : "+v"(x))

constexpr int S = 2048, D = 640, H = 32, DH = 20, F = 1536;
constexpr int KV_TILE = 2640, KV_K2 = 1024, KV_C = 1280, KV_V = 1296;
constexpr int KV_CHUNK = 8 * KV_TILE;
constexpr int KV_SLOT = 21 * 1024;
constexpr size_t WS_XN = 0;
constexpr size_t WS_Q = WS_XN + (size_t)S * D * 2;
constexpr size_t WS_O = WS_Q + (size_t)S * D * 2;
constexpr size_t WS_H = WS_O + (size_t)S * D * 2;
constexpr size_t WS_KV = WS_H + (size_t)S * F * 2;
constexpr size_t WS_KMAX = WS_KV + 64 * KV_TILE + 4096;
constexpr float QSCALE = 0.22360679774997896f * 1.4426950408889634f;

DI unsigned cvtpk(float a, float b) { f32x2 v = {a, b}; bf16x2_t r = __builtin_convertvector(v, bf16x2_t); return __builtin_bit_cast(unsigned, r); }
DI float bflo(unsigned u) { return __uint_as_float(u << 16); }
DI float bfhi(unsigned u) { return __uint_as_float(u & 0xffff0000u); }
DI float rbf(float a) { return bflo(cvtpk(a, a)); }
DI s16x4 trread(const LAS char* p) { return __builtin_bit_cast(s16x4, __builtin_amdgcn_ds_read_tr16_b64_v4i16((LAS s16x4*)p)); }
DI bf16x8 cat8(s16x4 a, s16x4 b) { return __builtin_shufflevector(a, b, 0, 1, 2, 3, 4, 5, 6, 7); }
DI void glds16(const void* g, unsigned lds_base) {
  unsigned sv;
  asm volatile("s_mov_b32 %0, m0\n\ts_mov_b32 m0, %2\n\ts_nop 0\n\tglobal_load_lds_dwordx4 %1, off\n\ts_mov_b32 m0, %0" : "=&s"(sv) : "v"(g), "s"(lds_base) : "memory");
}
#define MFMA32(a, b, c) __builtin_amdgcn_mfma_f32_32x32x16_bf16(a, b, c, 0, 0, 0)
#define MFMA32K8(a, b, c) __builtin_amdgcn_mfma_f32_32x32x8bf16_1k(a, b, c, 0, 0, 0)
#define MFMA16(a, b, c) __builtin_amdgcn_mfma_f32_16x16x32_bf16(a, b, c, 0, 0, 0)

constexpr int K1_XT = 102400, K1_LDS = 122880;
__global__ __launch_bounds__(512) void k_ln_qkv(const u16* __restrict__ x, const u16* __restrict__ wq, const u16* __restrict__ wk,
                                                 const u16* __restrict__ wv, char* __restrict__ ws) {
  extern __shared__ __attribute__((aligned(16))) char lds[];
  const int tid = threadIdx.x, lane = tid & 63, w = __builtin_amdgcn_readfirstlane(tid >> 6);
  const int b = blockIdx.x, bi = b >> 3, tt = (b & 7) * 4 + (bi & 3), g = bi >> 2;
  const int ts = w & 3, ch = w >> 2, r16 = lane & 15, gq = lane >> 4;
  const unsigned lds0 = (unsigned)(uintptr_t)(LAS char*)lds;
  const int token = tt * 64 + ts * 16 + r16;
  u32x4 xf[20];
  const u16* xp = x + (size_t)token * D + gq * 8;
#pragma unroll
  for (int s = 0; s < 20; ++s) xf[s] = *(const u32x4*)(xp + s * 32);
#pragma unroll
  for (int j = 0; j < 13; ++j) {
    const int piece = w + 8 * j;
    if (piece < 100) {
      const int ci = piece * 64 + lane, p2 = ci / 80, rem = ci - p2 * 80;
      const int ct = rem >> 4, hb = (rem >> 3) & 1, kk = (rem >> 1) & 3, c8 = rem & 1;
      const int k = ((p2 >> 1) * 4 + hb * 2 + (p2 & 1)) * 4 + kk;
      glds16(wq + (size_t)k * D + g * 80 + ct * 16 + c8 * 8, lds0 + piece * 1024);
    }
  }
  if (g < 4) {
    const u16* wsrc = (g < 2) ? wk : wv;
    if ((g & 1) == 0) {
#pragma unroll
      for (int j = 0; j < 5; ++j) {
        const int i = tid + 512 * j, k = i >> 2, c4 = i & 3, kb = k >> 2;
        const u32x2 v = *(const u32x2*)(wsrc + k * DH + c4 * 4);
        *(LAS u32x2*)((LAS char*)lds + K1_XT + ((kb >> 2) * 2 + (kb & 1)) * 256 + ((kb >> 1) & 1) * 128 + (k & 3) * 32 + c4 * 8) = v;
      }
    } else {
#pragma unroll
      for (int j = 0; j < 5; ++j) {
        const int i = tid + 512 * j, k = i >> 2, c4 = i & 3, kb = k >> 2;
        u32x2 v = {0u, 0u};
        if (c4 == 0) v = *(const u32x2*)(wsrc + k * DH + 16);
        *(LAS u32x2*)((LAS char*)lds + K1_XT + ((kb >> 2) * 2 + (kb & 1)) * 256 + ((kb >> 1) & 1) * 128 + (k & 3) * 32 + c4 * 8) = v;
      }
    }
  }
  float sum = 0.f;
#pragma unroll
  for (int s = 0; s < 20; ++s)
#pragma unroll
    for (int j = 0; j < 4; ++j) sum += bflo(xf[s][j]) + bfhi(xf[s][j]);
  sum += __shfl_xor(sum, 16); sum += __shfl_xor(sum, 32);
  const float mean = sum * (1.f / D);
  float sq = 0.f;
#pragma unroll
  for (int s = 0; s < 20; ++s)
#pragma unroll
    for (int j = 0; j < 4; ++j) { const float a = bflo(xf[s][j]) - mean, c = bfhi(xf[s][j]) - mean; sq += a * a; sq += c * c; }
  sq += __shfl_xor(sq, 16); sq += __shfl_xor(sq, 32);
  const float rstd = __builtin_amdgcn_rsqf(sq * (1.f / D) + 1e-5f);
  const float nmr = -mean * rstd;
#pragma unroll
  for (int s = 0; s < 20; ++s)
#pragma unroll
    for (int j = 0; j < 4; ++j) xf[s][j] = cvtpk(__builtin_fmaf(bflo(xf[s][j]), rstd, nmr), __builtin_fmaf(bfhi(xf[s][j]), rstd, nmr));
  if (g >= 4 && ch == 0) {
    u16* xn = (u16*)(ws + WS_XN) + (size_t)token * D + gq * 8;
#pragma unroll
    for (int s = 0; s < 20; ++s) if (s / 5 == g - 4) *(u32x4*)(xn + s * 32) = xf[s];
  }
  asm volatile("s_waitcnt vmcnt(0)" ::: "memory");
  __syncthreads();
  const int q4 = (lane & 15) >> 2, p = lane & 3;
  const LAS char* wb = (const LAS char*)lds + (gq >> 1) * 2560 + (gq & 1) * 128 + q4 * 32 + p * 8 + ch * 768;
  const LAS char* xb = (const LAS char*)lds + K1_XT + (gq >> 1) * 512 + (gq & 1) * 128 + q4 * 32 + p * 8;
  f32x4 acc[3] = {};
  if (ch == 0) {
#pragma unroll
    for (int s = 0; s < 20; ++s) {
      const bf16x8 xa = __builtin_bit_cast(bf16x8, xf[s]);
#pragma unroll
      for (int c = 0; c < 3; ++c) {
        const bf16x8 wf = cat8(trread(wb + s * 5120 + c * 256), trread(wb + s * 5120 + 1280 + c * 256));
        acc[c] = MFMA16(wf, xa, acc[c]);
      }
    }
  } else {
#pragma unroll
    for (int s = 0; s < 20; ++s) {
      const bf16x8 xa = __builtin_bit_cast(bf16x8, xf[s]);
#pragma unroll
      for (int c = 0; c < 2; ++c) {
        const bf16x8 wf = cat8(trread(wb + s * 5120 + c * 256), trread(wb + s * 5120 + 1280 + c * 256));
        acc[c] = MFMA16(wf, xa, acc[c]);
      }
    }
    if (g < 2) {
#pragma unroll
      for (int s = 0; s < 20; ++s) {
        const bf16x8 wf = cat8(trread(xb + s * 1024), trread(xb + s * 1024 + 256));
        acc[2] = MFMA16(wf, __builtin_bit_cast(bf16x8, xf[s]), acc[2]);
      }
    } else if (g < 4) {
#pragma unroll
      for (int s = 0; s < 20; ++s) {
        const bf16x8 wf = cat8(trread(xb + s * 1024), trread(xb + s * 1024 + 256));
        acc[2] = MFMA16(__builtin_bit_cast(bf16x8, xf[s]), wf, acc[2]);
      }
    }
  }
  {
    u16* qp = (u16*)(ws + WS_Q) + (size_t)token * D + g * 80 + ch * 48 + gq * 4;
#pragma unroll
    for (int c = 0; c < 3; ++c)
      if (ch == 0 || c < 2) {
        u32x2 v = {cvtpk(acc[c][0] * QSCALE, acc[c][1] * QSCALE), cvtpk(acc[c][2] * QSCALE, acc[c][3] * QSCALE)};
        *(u32x2*)(qp + c * 16) = v;
      }
  }
  if (ch == 1 && g < 4) {
    char* kv = ws + WS_KV;
    const u32x2 v = {cvtpk(acc[2][0], acc[2][1]), cvtpk(acc[2][2], acc[2][3])};
    if (g < 2) {
      const int kt = token >> 5, r = token & 31;
      float n2 = bflo(v[0]) * bflo(v[0]) + bfhi(v[0]) * bfhi(v[0]) + bflo(v[1]) * bflo(v[1]) + bfhi(v[1]) * bfhi(v[1]);
      if (g == 0) {
        *(u32x2*)(kv + kt * KV_TILE + (gq >> 1) * 512 + r * 16 + (gq & 1) * 8) = v;
      } else {
        if (gq == 0) *(u32x2*)(kv + kt * KV_TILE + KV_K2 + r * 8) = v;
        else n2 = 0.f;
        if (gq == 1 && r == 0) { const u32x4 c1 = {0x00003F80u, 0u, 0u, 0u}; *(u32x4*)(kv + kt * KV_TILE + KV_C) = c1; }
      }
      n2 += __shfl_xor(n2, 16); n2 += __shfl_xor(n2, 32);
#pragma unroll
      for (int o = 1; o < 16; o <<= 1) n2 = fmaxf(n2, __shfl_xor(n2, o));
      if (lane == 0) ((float*)(ws + WS_KMAX))[g * 128 + tt * 4 + ts] = n2;
    } else {
      const int kt = tt * 2 + (ts >> 1);
      char* vp = kv + kt * KV_TILE + KV_V + (((ts & 1) * 2 + (gq & 1)) * 21) * 16 + (gq >> 1) * 8;
      if (g == 2) *(u32x2*)(vp + r16 * 16) = v;
      else if (r16 < 4) *(u32x2*)(vp + (16 + r16) * 16) = v;
      else if (r16 == 4) { const u32x2 one = {0x3F803F80u, 0x3F803F80u}; *(u32x2*)(vp + 20 * 16) = one; }
    }
  }
}

constexpr int AT_LDS = 3 * KV_SLOT;
#define EXP2(v) __builtin_amdgcn_exp2f(v)

__global__ __launch_bounds__(512, 4) void k_attn(char* __restrict__ ws) {
  extern __shared__ __attribute__((aligned(16))) char lds[];
  const int tid = threadIdx.x, lane = tid & 63, w = __builtin_amdgcn_readfirstlane(tid >> 6);
  const int b = blockIdx.x, bi = b >> 3;
  const int tb = (b & 7) * 8 + (bi & 7), head = (bi >> 3) * 8 + w;
  const int r = lane & 31, h = lane >> 5;
  const unsigned lds0 = (unsigned)(uintptr_t)(LAS char*)lds;
  const char* kvg = ws + WS_KV + lane * 16;
  auto dma = [&](int chunk, int slot) {
#pragma unroll
    for (int j = 0; j < 3; ++j) {
      const int piece = w + 8 * j;
      if (piece < 21) glds16(kvg + chunk * KV_CHUNK + piece * 1024, lds0 + slot * KV_SLOT + piece * 1024);
    }
  };
  dma(0, 0); dma(1, 1);
  const int token = tb * 32 + r;
  const u16* qp = (const u16*)(ws + WS_Q) + (size_t)token * D + head * DH;
  u32x4 q1u; { const u32x2 a = *(const u32x2*)(qp + 8 * h), c = *(const u32x2*)(qp + 8 * h + 4); q1u = (u32x4){a[0], a[1], c[0], c[1]}; }
  u32x2 q2u = *(const u32x2*)(qp + 16);
  const f32x4 km4 = *(const f32x4*)((const float*)(ws + WS_KMAX) + lane * 4);
  float km = fmaxf(fmaxf(km4[0], km4[1]), fmaxf(km4[2], km4[3]));
#pragma unroll
  for (int o = 1; o < 32; o <<= 1) km = fmaxf(km, __shfl_xor(km, o));
  km += __shfl_xor(km, 32);
  float qn = 0.f;
#pragma unroll
  for (int j = 0; j < 4; ++j) qn += bflo(q1u[j]) * bflo(q1u[j]) + bfhi(q1u[j]) * bfhi(q1u[j]);
  if (h == 0) qn += bflo(q2u[0]) * bflo(q2u[0]) + bfhi(q2u[0]) * bfhi(q2u[0]) + bflo(q2u[1]) * bflo(q2u[1]) + bfhi(q2u[1]) * bfhi(q2u[1]);
  qn += __shfl_xor(qn, 32);
  const float bound = __builtin_sqrtf(qn * km) * 1.02f + 1e-3f;
  const bool fast = __all(bound <= 48.f);
  if (h == 1) { q2u[0] = fast ? (cvtpk(-bound, 0.f) & 0xffffu) : 0u; q2u[1] = 0u; }
  const bf16x8 q1 = __builtin_bit_cast(bf16x8, q1u);
  const s16x4 q2 = __builtin_bit_cast(s16x4, q2u);
  const int dv = r <= 20 ? r : (r < 28 ? 20 : 4);
  const LAS char* k1b = (const LAS char*)lds + lane * 16;
  const LAS char* k2b = (const LAS char*)lds + (h ? KV_C : KV_K2 + r * 8);
  const LAS char* vb = (const LAS char*)lds + KV_V + (h * 21 + dv) * 16;
  f32x16 o = {};
  const f32x16 zero16 = {};
  asm volatile("s_waitcnt vmcnt(0)" ::: "memory");
  __builtin_amdgcn_s_barrier();

  if (fast) {
    f32x16 sA, sB;
    bf16x8 k1f, vf0, vf1; s16x4 k2f;
    u32x4 pa, pb;
    float e14, e15;
    k1f = *(const LAS bf16x8*)(k1b); k2f = *(const LAS s16x4*)(k2b);
    sA = MFMA32(k1f, q1, zero16); sA = MFMA32K8(k2f, q2, sA);
    k1f = *(const LAS bf16x8*)(k1b + KV_TILE); k2f = *(const LAS s16x4*)(k2b + KV_TILE);
    vf1 = (bf16x8){0, 0, 0, 0, 0, 0, 0, 0}; pb = (u32x4){0u, 0u, 0u, 0u}; e14 = 0.f; e15 = 0.f;
    int cur = 0, nxt = KV_SLOT, fre = 2 * KV_SLOT;
#define TILE(C, N, i) do { \
      const int oV = (i) * KV_TILE, oK = ((i) + 2) * KV_TILE; \
      N = MFMA32(k1f, q1, zero16); SBAR(); \
      k1f = *(const LAS bf16x8*)((i) + 2 < 8 ? kc1 + oK : kn1 + (oK - 8 * KV_TILE)); vf0 = *(const LAS bf16x8*)(vc + oV); SBAR(); \
      C[0] = EXP2(C[0]); C[1] = EXP2(C[1]); SBAR(); pb[3] = cvtpk(e14, e15); PIN(pb); SBAR(); \
      C[2] = EXP2(C[2]); C[3] = EXP2(C[3]); SBAR(); pa[0] = cvtpk(C[0], C[1]); PIN(pa); SBAR(); \
      o = MFMA32(vf1, __builtin_bit_cast(bf16x8, pb), o); SBAR(); \
      vf1 = *(const LAS bf16x8*)(vc + oV + 672); SBAR(); \
      C[4] = EXP2(C[4]); C[5] = EXP2(C[5]); SBAR(); pa[1] = cvtpk(C[2], C[3]); PIN(pa); SBAR(); \
      C[6] = EXP2(C[6]); C[7] = EXP2(C[7]); SBAR(); pa[2] = cvtpk(C[4], C[5]); PIN(pa); SBAR(); \
      N = MFMA32K8(k2f, q2, N); SBAR(); \
      k2f = *(const LAS s16x4*)((i) + 2 < 8 ? kc2 + oK : kn2 + (oK - 8 * KV_TILE)); SBAR(); \
      C[8] = EXP2(C[8]); C[9] = EXP2(C[9]); SBAR(); pa[3] = cvtpk(C[6], C[7]); PIN(pa); SBAR(); \
      C[10] = EXP2(C[10]); C[11] = EXP2(C[11]); SBAR(); pb[0] = cvtpk(C[8], C[9]); PIN(pb); SBAR(); \
      o = MFMA32(vf0, __builtin_bit_cast(bf16x8, pa), o); SBAR(); \
      C[12] = EXP2(C[12]); C[13] = EXP2(C[13]); SBAR(); pb[1] = cvtpk(C[10], C[11]); PIN(pb); SBAR(); \
      e14 = EXP2(C[14]); e15 = EXP2(C[15]); SBAR(); pb[2] = cvtpk(C[12], C[13]); PIN(pb); PIN(e14); PIN(e15); SBAR(); \
    } while (0)
    for (int c = 0; c < 8; ++c) {
      const LAS char* kc1 = k1b + cur; const LAS char* kc2 = k2b + cur; const LAS char* vc = vb + cur;
      const LAS char* kn1 = k1b + nxt; const LAS char* kn2 = k2b + nxt;
      TILE(sA, sB, 0); TILE(sB, sA, 1); TILE(sA, sB, 2);
      asm volatile("s_waitcnt vmcnt(0)\n\ts_barrier" ::: "memory");
      if (c + 2 < 8) dma(c + 2, fre / KV_SLOT);
      TILE(sB, sA, 3); TILE(sA, sB, 4); TILE(sB, sA, 5); TILE(sA, sB, 6); TILE(sB, sA, 7);
      const int t = cur; cur = nxt; nxt = fre; fre = t;
    }
    pb[3] = cvtpk(e14, e15);
    o = MFMA32(vf1, __builtin_bit_cast(bf16x8, pb), o);
#undef TILE
  } else {
    float m = -INFINITY;
    int cur = 0, nxt = KV_SLOT, fre = 2 * KV_SLOT;
    for (int c = 0; c < 8; ++c) {
      for (int i = 0; i < 8; ++i) {
        if (i == 3) {
          asm volatile("s_waitcnt vmcnt(0)\n\ts_barrier" ::: "memory");
          if (c + 2 < 8) dma(c + 2, fre / KV_SLOT);
        }
        const int off = cur + i * KV_TILE;
        const bf16x8 k1f = *(const LAS bf16x8*)(k1b + off); const s16x4 k2f = *(const LAS s16x4*)(k2b + off);
        const bf16x8 vf0 = *(const LAS bf16x8*)(vb + off), vf1 = *(const LAS bf16x8*)(vb + off + 672);
        f32x16 sc = MFMA32(k1f, q1, zero16); sc = MFMA32K8(k2f, q2, sc);
        float mx = sc[0];
#pragma unroll
        for (int j = 1; j < 16; ++j) mx = fmaxf(mx, sc[j]);
        mx = fmaxf(mx, __shfl_xor(mx, 32));
        const float mn = fmaxf(m, mx);
        const float al = EXP2(m - mn);
        m = mn;
#pragma unroll
        for (int j = 0; j < 16; ++j) { o[j] *= al; sc[j] = EXP2(sc[j] - mn); }
        const u32x4 pa = {cvtpk(sc[0], sc[1]), cvtpk(sc[2], sc[3]), cvtpk(sc[4], sc[5]), cvtpk(sc[6], sc[7])};
        const u32x4 pb = {cvtpk(sc[8], sc[9]), cvtpk(sc[10], sc[11]), cvtpk(sc[12], sc[13]), cvtpk(sc[14], sc[15])};
        o = MFMA32(vf0, __builtin_bit_cast(bf16x8, pa), o);
        o = MFMA32(vf1, __builtin_bit_cast(bf16x8, pb), o);
      }
      const int t = cur; cur = nxt; nxt = fre; fre = t;
    }
  }
  const float l = __shfl(o[8], r + 32);
  const float il = 1.f / l;
  u16* op = (u16*)(ws + WS_O) + (size_t)token * D + head * DH + 4 * h;
  *(u32x2*)(op) = (u32x2){cvtpk(o[0] * il, o[1] * il), cvtpk(o[2] * il, o[3] * il)};
  *(u32x2*)(op + 8) = (u32x2){cvtpk(o[4] * il, o[5] * il), cvtpk(o[6] * il, o[7] * il)};
  if (h == 0) *(u32x2*)(op + 16) = (u32x2){cvtpk(o[8] * il, o[9] * il), cvtpk(o[10] * il, o[11] * il)};
}

constexpr int G1_X = 16384, G1_W = 24576, G1_STAGE = G1_X + G1_W, G1_LDS = 2 * G1_STAGE;
__global__ __launch_bounds__(512, 4) void k_gateup(const u16* __restrict__ w1, const u16* __restrict__ w3, char* __restrict__ ws) {
  extern __shared__ __attribute__((aligned(16))) char lds[];
  const int tid = threadIdx.x, lane = tid & 63, w = __builtin_amdgcn_readfirstlane(tid >> 6);
  const int b = blockIdx.x, bi = b >> 3;
  const int tp = (b & 7) * 2 + (bi & 1), cg = bi >> 1;
  const unsigned lds0 = (unsigned)(uintptr_t)(LAS char*)lds;
  const u16* xn = (const u16*)(ws + WS_XN);
  const u16* xs[2];
#pragma unroll
  for (int j = 0; j < 2; ++j) {
    const int row = (w + 8 * j) * 8 + (lane >> 3), cc = (lane & 7) ^ ((row >> 1) & 7);
    xs[j] = xn + (size_t)(tp * 128 + row) * D + cc * 8;
  }
  const u16* wsrc[3];
#pragma unroll
  for (int j = 0; j < 3; ++j) {
    const int ci = (w + 8 * j) * 64 + lane, kq = ci / 96, rem = ci - kq * 96;
    const int cgrp = rem >> 4, gu = (rem >> 3) & 1, kk = (rem >> 1) & 3, c8 = rem & 1;
    wsrc[j] = (gu ? w3 : w1) + (size_t)(kq * 4 + kk) * F + cg * 96 + cgrp * 16 + c8 * 8;
  }
  auto stage = [&](int kt, int buf) {
#pragma unroll
    for (int j = 0; j < 2; ++j) glds16(xs[j] + kt * 64, lds0 + buf * G1_STAGE + (w + 8 * j) * 1024);
#pragma unroll
    for (int j = 0; j < 3; ++j) glds16(wsrc[j] + (size_t)kt * 64 * F, lds0 + buf * G1_STAGE + G1_X + (w + 8 * j) * 1024);
  };
  const int tq = w & 3, hc = w >> 2, r = lane & 31, h = lane >> 5;
  const int xrow = tq * 32 + r;
  const LAS char* xb = (const LAS char*)lds + xrow * 128;
  const int xsw = (xrow >> 1) & 7;
  const int q4 = (lane & 15) >> 2, p = lane & 3, pp = ((p & 1) << 1) | (p >> 1);
  const LAS char* wb = (const LAS char*)lds + G1_X + (2 * h) * 1536 + hc * 768 + ((lane >> 4) & 1) * 128 + q4 * 32 + pp * 8;
  f32x16 acc[3] = {};
  stage(0, 0);
  for (int kt = 0; kt < 10; ++kt) {
    const int buf = kt & 1;
    asm volatile("s_waitcnt vmcnt(0)\n\ts_barrier" ::: "memory");
    if (kt + 1 < 10) stage(kt + 1, buf ^ 1);
#pragma unroll
    for (int s = 0; s < 4; ++s) {
      const bf16x8 xf = *(const LAS bf16x8*)(xb + buf * G1_STAGE + (((2 * s + h) ^ xsw) * 16));
#pragma unroll
      for (int c = 0; c < 3; ++c) {
        const LAS char* a = wb + buf * G1_STAGE + s * 4 * 1536 + c * 256;
        const bf16x8 wf = cat8(trread(a), trread(a + 1536));
        acc[c] = MFMA32(wf, xf, acc[c]);
      }
    }
    asm volatile("s_waitcnt lgkmcnt(0)" ::: "memory");
  }
  u16* hp = (u16*)(ws + WS_H) + (size_t)(tp * 128 + xrow) * F + cg * 96 + hc * 48 + 8 * h;
#pragma unroll
  for (int c = 0; c < 3; ++c) {
    float v[8];
#pragma unroll
    for (int j = 0; j < 8; ++j) {
      const float gt = acc[c][j], up = acc[c][j + 8];
      v[j] = gt * __builtin_amdgcn_rcpf(1.f + EXP2(-1.4426950408889634f * gt)) * up;
    }
    *(u32x4*)(hp + c * 16) = (u32x4){cvtpk(v[0], v[1]), cvtpk(v[2], v[3]), cvtpk(v[4], v[5]), cvtpk(v[6], v[7])};
  }
}

constexpr int K3_X = 8192, K3_W = 10240, K3_STAGE = K3_X + K3_W, K3_LDS = 3 * K3_STAGE;
__global__ __launch_bounds__(512) void k_out(const u16* __restrict__ x, const u16* __restrict__ wo, const u16* __restrict__ w2,
                                              const char* __restrict__ ws, u16* __restrict__ out) {
  extern __shared__ __attribute__((aligned(16))) char lds[];
  const int tid = threadIdx.x, lane = tid & 63, w = __builtin_amdgcn_readfirstlane(tid >> 6);
  const int b = blockIdx.x, bi = b >> 3;
  const int tt = (b & 7) * 4 + (bi & 3), cg = bi >> 2;
  const unsigned lds0 = (unsigned)(uintptr_t)(LAS char*)lds;
  const int srow = w * 8 + (lane >> 3), scc = (lane & 7) ^ ((srow >> 1) & 7);
  const u16* os = (const u16*)(ws + WS_O) + (size_t)(tt * 64 + srow) * D + scc * 8;
  const u16* hs = (const u16*)(ws + WS_H) + (size_t)(tt * 64 + srow) * F + scc * 8;
  int woff[2];
#pragma unroll
  for (int j = 0; j < 2; ++j) {
    const int ci = (w + 8 * j) * 64 + lane, p2 = ci / 80, rem = ci - p2 * 80;
    const int ct = rem >> 4, hb = (rem >> 3) & 1, kk = (rem >> 1) & 3, c8 = rem & 1;
    const int k = ((p2 >> 1) * 4 + hb * 2 + (p2 & 1)) * 4 + kk;
    woff[j] = k * D + cg * 80 + ct * 16 + c8 * 8;
  }
  auto stage = [&](int kt, int buf) {
    const u16* xsrc = kt < 10 ? os + kt * 64 : hs + (kt - 10) * 64;
    const u16* wsrc = kt < 10 ? wo + (size_t)kt * 64 * D : w2 + (size_t)(kt - 10) * 64 * D;
    glds16(xsrc, lds0 + buf * K3_STAGE + w * 1024);
    glds16(wsrc + woff[0], lds0 + buf * K3_STAGE + K3_X + w * 1024);
    if (w < 2) glds16(wsrc + woff[1], lds0 + buf * K3_STAGE + K3_X + (w + 8) * 1024);
  };
  const int ts = w & 3, kh = w >> 2, r16 = lane & 15, gq = lane >> 4, q4 = (lane & 15) >> 2, p = lane & 3;
  const int xrow = ts * 16 + r16;
  const LAS char* xb = (const LAS char*)lds + xrow * 128 + (((kh * 4 + gq) ^ ((xrow >> 1) & 7)) * 16);
  const LAS char* wb = (const LAS char*)lds + K3_X + kh * 5120 + (gq >> 1) * 2560 + (gq & 1) * 128 + q4 * 32 + p * 8;
  f32x4 acc[5] = {};
  stage(0, 0); stage(1, 1);
  int buf = 0;
  for (int kt = 0; kt < 34; ++kt) {
    if (kt + 1 < 34) { if (w < 2) asm volatile("s_waitcnt vmcnt(3)" ::: "memory"); else asm volatile("s_waitcnt vmcnt(2)" ::: "memory"); }
    else asm volatile("s_waitcnt vmcnt(0)" ::: "memory");
    __builtin_amdgcn_s_barrier();
    if (kt + 2 < 34) stage(kt + 2, buf == 0 ? 2 : buf - 1);
    const bf16x8 xf = *(const LAS bf16x8*)(xb + buf * K3_STAGE);
#pragma unroll
    for (int c = 0; c < 5; ++c) {
      const LAS char* a = wb + buf * K3_STAGE + c * 256;
      const bf16x8 wf = cat8(trread(a), trread(a + 1280));
      acc[c] = MFMA16(wf, xf, acc[c]);
    }
    asm volatile("s_waitcnt lgkmcnt(0)" ::: "memory");
    buf = buf == 2 ? 0 : buf + 1;
  }
  __syncthreads();
  LAS f32x4* red = (LAS f32x4*)lds + ts * 5 * 64 + lane;
  if (kh == 1) {
#pragma unroll
    for (int c = 0; c < 5; ++c) red[c * 64] = acc[c];
  }
  __syncthreads();
  if (kh == 0) {
    const size_t o0 = (size_t)(tt * 64 + xrow) * D + cg * 80 + gq * 4;
#pragma unroll
    for (int c = 0; c < 5; ++c) {
      const f32x4 a = acc[c] + red[c * 64];
      const u32x2 xv = *(const u32x2*)(x + o0 + c * 16);
      *(u32x2*)(out + o0 + c * 16) = (u32x2){cvtpk(a[0] + bflo(xv[0]), a[1] + bfhi(xv[0])), cvtpk(a[2] + bflo(xv[1]), a[3] + bfhi(xv[1]))};
    }
  }
}

extern "C" void kernel_launch(void* const* d_in, const int* in_sizes, int n_in, void* d_out, int out_size, void* d_ws, size_t ws_size,
                              hipStream_t stream) {
  const u16* x = (const u16*)d_in[0];
  const u16* wq = (const u16*)d_in[1];
  const u16* wk = (const u16*)d_in[2];
  const u16* wv = (const u16*)d_in[3];
  const u16* wo = (const u16*)d_in[4];
  const u16* w1 = (const u16*)d_in[6];
  const u16* w3 = (const u16*)d_in[7];
  const u16* w2 = (const u16*)d_in[8];
  char* ws = (char*)d_ws;
  (void)hipFuncSetAttribute((const void*)k_ln_qkv, hipFuncAttributeMaxDynamicSharedMemorySize, K1_LDS);
  (void)hipFuncSetAttribute((const void*)k_attn, hipFuncAttributeMaxDynamicSharedMemorySize, AT_LDS);
  (void)hipFuncSetAttribute((const void*)k_gateup, hipFuncAttributeMaxDynamicSharedMemorySize, G1_LDS);
  (void)hipFuncSetAttribute((const void*)k_out, hipFuncAttributeMaxDynamicSharedMemorySize, K3_LDS);
  k_ln_qkv<<<256, 512, K1_LDS, stream>>>(x, wq, wk, wv, ws);
  k_attn<<<256, 512, AT_LDS, stream>>>(ws);
  k_gateup<<<256, 512, G1_LDS, stream>>>(w1, w3, ws);
  k_out<<<256, 512, K3_LDS, stream>>>(x, wo, w2, ws, (u16*)d_out);
}
```

```cpp
#include <hip/hip_runtime.h>
#include <hip/hip_bf16.h>
#include <stdint.h>

typedef short bf16x8 __attribute__((ext_vector_type(8)));
typedef short s16x4 __attribute__((ext_vector_type(4)));
typedef float f32x2 __attribute__((ext_vector_type(2)));
typedef float f32x4 __attribute__((ext_vector_type(4)));
typedef float f32x16 __attribute__((ext_vector_type(16)));
typedef unsigned u32x4 __attribute__((ext_vector_type(4)));
typedef unsigned u32x2 __attribute__((ext_vector_type(2)));
typedef __bf16 bf16x2_t __attribute__((ext_vector_type(2)));
typedef unsigned short u16;
#define LAS __attribute__((address_space(3)))
#define DI __device__ __forceinline__
#define SBAR() __builtin_amdgcn_sched_barrier(0)
#define PIN(x) asm volatile("" : "+v"(x))

#define GPRIO 0
#define G1PRIO 2
#define APRIO 0
constexpr int S = 2048, D = 640, H = 32, DH = 20, F = 1536;
constexpr int VR = 22;
constexpr int KV_K2 = 1024, KV_C = 1280, KV_V = 1296, KV_TILE = KV_V + 4 * VR * 16;
constexpr int KV_CHUNK = 8 * KV_TILE;
constexpr int KV_PIECES = (KV_CHUNK + 1023) / 1024, KV_SLOT = KV_PIECES * 1024;
constexpr size_t WS_KMAX = 0;
constexpr size_t WS_KV = 4096;
constexpr size_t WS_Q = WS_KV + 192 * 1024;
constexpr size_t WS_O = WS_Q + (size_t)S * D * 2;
constexpr size_t WS_H = WS_O + (size_t)S * D * 2;
constexpr size_t WS_P = WS_H + (size_t)S * F * 2;
constexpr float QSCALE = 0.22360679774997896f * 1.4426950408889634f;
constexpr int LDS_BYTES = 81920, SLOT = 20480;

DI unsigned cvtpk(float a, float b) { f32x2 v = {a, b}; bf16x2_t r = __builtin_convertvector(v, bf16x2_t); return __builtin_bit_cast(unsigned, r); }
DI float bflo(unsigned u) { return __uint_as_float(u << 16); }
DI float bfhi(unsigned u) { return __uint_as_float(u & 0xffff0000u); }
DI float rbf(float a) { return bflo(cvtpk(a, a)); }
DI float dot2(unsigned a, unsigned b, float c) { return __builtin_amdgcn_fdot2_f32_bf16(__builtin_bit_cast(bf16x2_t, a), __builtin_bit_cast(bf16x2_t, b), c, false); }
DI s16x4 trread(const LAS char* p) { return __builtin_bit_cast(s16x4, __builtin_amdgcn_ds_read_tr16_b64_v4i16((LAS s16x4*)p)); }
DI bf16x8 cat8(s16x4 a, s16x4 b) { return __builtin_shufflevector(a, b, 0, 1, 2, 3, 4, 5, 6, 7); }
DI void glds16s(const void* sbase, unsigned voff, unsigned lds_base) {
  unsigned sv;
  asm volatile("s_mov_b32 %0, m0\n\ts_mov_b32 m0, %3\n\ts_nop 0\n\tglobal_load_lds_dwordx4 %1, %2\n\ts_mov_b32 m0, %0" : "=&s"(sv) : "v"(voff), "s"(sbase), "s"(lds_base) : "memory");
}
#define ST_DECL
#define ST()
#define ST_OUT(a, b)
#define MFMA32(a, b, c) __builtin_amdgcn_mfma_f32_32x32x16_bf16(a, b, c, 0, 0, 0)
#define MFMA32K8(a, b, c) __builtin_amdgcn_mfma_f32_32x32x8bf16_1k(a, b, c, 0, 0, 0)
#define MFMA16(a, b, c) __builtin_amdgcn_mfma_f32_16x16x32_bf16(a, b, c, 0, 0, 0)
#define EXP2(v) __builtin_amdgcn_exp2f(v)
#define RAWBAR() asm volatile("s_waitcnt lgkmcnt(0)\n\ts_barrier" ::: "memory")
template <int N> DI void vmwait() { asm volatile("s_waitcnt vmcnt(%0)" :: "n"(N) : "memory"); }
DI int wslot(int k) { return k ^ ((k >> 1) & 4); }
DI void ln_scalars(float sum, float sq, float& mean_b, float& rden) {
  const float mean = sum * (1.f / D);
  const float var = fmaxf(sq * (1.f / D) - mean * mean, 0.f);
  mean_b = rbf(mean);
  rden = __builtin_amdgcn_rcpf(rbf(__builtin_amdgcn_sqrtf(rbf(rbf(var) + 1e-5f))));
}

template <int NP>
DI void qkv_block(const u16* __restrict__ x, const u16* __restrict__ wq, const u16* __restrict__ wk, const u16* __restrict__ wv,
                  char* __restrict__ ws, char* lds, int b) {
  const int tid = threadIdx.x, lane = tid & 63, w = __builtin_amdgcn_readfirstlane(tid >> 6);
  const int bi = b >> 3, tt = (b & 7) * 4 + (bi & 3), g = bi >> 2;
  const int ts = w & 3, kh = w >> 2, r16 = lane & 15, gq = lane >> 4;
  const unsigned ldsw = (unsigned)(uintptr_t)(LAS char*)lds + w * 1024;
  const int token = tt * 64 + ts * 16 + r16;
  ST_DECL ST();
  u32x4 xf[10];
  const u16* xp = x + (size_t)token * D + kh * 320 + gq * 8;
#pragma unroll
  for (int s = 0; s < 10; ++s) asm volatile("global_load_dwordx4 %0, %1, off offset:%2" : "=v"(xf[s]) : "v"(xp), "i"(s * 64) : "memory");
  u32x2 ev[5];
#pragma unroll
  for (int j = 0; j < 5; ++j) ev[j] = (u32x2){0u, 0u};
  if (g < 4) {
    const u16* wsrc = ((g & 2) ? wv : wk) + (tid >> 2) * DH + ((g & 1) ? 16 : (tid & 3) * 4);
#pragma unroll
    for (int j = 0; j < 5; ++j) { const u16* pj = wsrc + j * 128 * DH; asm volatile("global_load_dwordx2 %0, %1, off" : "+v"(ev[j]) : "v"(pj) : "memory"); }
  }
  unsigned vo[3];
#pragma unroll
  for (int j = 0; j < 3; ++j) { const int ci = tid + 512 * j, row = ci / 10, c = ci - row * 10; vo[j] = (((row >> 6) & 1) * 320 + wslot(row & 63)) * (D * 2) + c * 16; }
  const u16* wqb = wq + g * 80;
  auto unit = [&](int u, int slot) {
#pragma unroll
    for (int j = 0; j < NP; ++j) glds16s(wqb + u * 64 * D, vo[j], ldsw + slot * SLOT + j * 8192);
  };
  unit(0, 0);
  ST();
  asm volatile("s_waitcnt vmcnt(%15)" : "+v"(xf[0]), "+v"(xf[1]), "+v"(xf[2]), "+v"(xf[3]), "+v"(xf[4]), "+v"(xf[5]), "+v"(xf[6]), "+v"(xf[7]), "+v"(xf[8]), "+v"(xf[9]),
               "+v"(ev[0]), "+v"(ev[1]), "+v"(ev[2]), "+v"(ev[3]), "+v"(ev[4]) : "n"(NP) : "memory");
  float sum = 0.f, sq = 0.f, sum2 = 0.f, sq2 = 0.f;
#pragma unroll
  for (int s = 0; s < 10; ++s)
#pragma unroll
    for (int j = 0; j < 4; j += 2) {
      sum = dot2(xf[s][j], 0x3F803F80u, sum); sq = dot2(xf[s][j], xf[s][j], sq);
      sum2 = dot2(xf[s][j + 1], 0x3F803F80u, sum2); sq2 = dot2(xf[s][j + 1], xf[s][j + 1], sq2);
    }
  sum += sum2; sq += sq2;
  unit(1, 1);
  sum += __shfl_xor(sum, 16); sq += __shfl_xor(sq, 16);
  sum += __shfl_xor(sum, 32); sq += __shfl_xor(sq, 32);
  LAS f32x2* stp = (LAS f32x2*)((LAS char*)lds + 3 * SLOT);
  if (gq == 0) stp[kh * 64 + ts * 16 + r16] = (f32x2){sum, sq};
  RAWBAR();
  { const f32x2 o = stp[(kh ^ 1) * 64 + ts * 16 + r16]; sum += o[0]; sq += o[1]; }
  float mean_b, rden;
  ln_scalars(sum, sq, mean_b, rden);
#pragma unroll
  for (int s = 0; s < 10; ++s) {
    float lo[4], hi[4];
#pragma unroll
    for (int j = 0; j < 4; ++j) { lo[j] = bflo(xf[s][j]); hi[j] = bfhi(xf[s][j]); }
    SBAR();
#pragma unroll
    for (int j = 0; j < 4; ++j) { lo[j] -= mean_b; hi[j] -= mean_b; }
    SBAR();
#pragma unroll
    for (int j = 0; j < 4; ++j) xf[s][j] = cvtpk(lo[j], hi[j]);
    SBAR();
#pragma unroll
    for (int j = 0; j < 4; ++j) { lo[j] = bflo(xf[s][j]); hi[j] = bfhi(xf[s][j]); }
    SBAR();
#pragma unroll
    for (int j = 0; j < 4; ++j) { lo[j] *= rden; hi[j] *= rden; }
    SBAR();
#pragma unroll
    for (int j = 0; j < 4; ++j) xf[s][j] = cvtpk(lo[j], hi[j]);
    PIN(xf[s]);
    if (s == 3) unit(2, 2);
    SBAR();
  }
  ST();
  const int q4 = (lane & 15) >> 2, p = lane & 3;
  const int r0 = (gq * 8 + q4) ^ ((gq & 1) << 2), r1 = (gq * 8 + 4 + q4) ^ ((gq & 1) << 2);
  const LAS char* wb0 = (const LAS char*)lds + (kh * 64 + r0) * 160 + p * 8;
  const LAS char* wb1 = (const LAS char*)lds + (kh * 64 + r1) * 160 + p * 8;
  f32x4 acc[6] = {};
  auto mm = [&](int u, int slot) {
#pragma unroll
    for (int s = 0; s < 2; ++s)
#pragma unroll
      for (int c = 0; c < 5; ++c)
        acc[c] = MFMA16(cat8(trread(wb0 + slot * SLOT + s * 5120 + c * 32), trread(wb1 + slot * SLOT + s * 5120 + c * 32)), __builtin_bit_cast(bf16x8, xf[2 * u + s]), acc[c]);
  };
  vmwait<2 * NP>(); RAWBAR(); unit(3, 3); mm(0, 0);
  vmwait<2 * NP>(); RAWBAR(); unit(4, 0); mm(1, 1);
  vmwait<2 * NP>(); RAWBAR();
  if (g < 4) {
#pragma unroll
    for (int j = 0; j < 5; ++j) *(LAS u32x2*)((LAS char*)lds + SLOT + wslot((tid >> 2) + j * 128) * 32 + (tid & 3) * 8) = ev[j];
  }
  mm(2, 2);
  vmwait<NP>(); RAWBAR();
  mm(3, 3);
  {
    const LAS char* eb0 = (const LAS char*)lds + SLOT + (kh * 320 + r0) * 32 + p * 8;
    const LAS char* eb1 = (const LAS char*)lds + SLOT + (kh * 320 + r1) * 32 + p * 8;
    if (g < 2) {
#pragma unroll
      for (int s = 0; s < 10; ++s) acc[5] = MFMA16(cat8(trread(eb0 + s * 1024), trread(eb1 + s * 1024)), __builtin_bit_cast(bf16x8, xf[s]), acc[5]);
    } else if (g < 4) {
#pragma unroll
      for (int s = 0; s < 10; ++s) acc[5] = MFMA16(__builtin_bit_cast(bf16x8, xf[s]), cat8(trread(eb0 + s * 1024), trread(eb1 + s * 1024)), acc[5]);
    }
  }
  vmwait<0>(); RAWBAR();
  mm(4, 0);
  ST();
  LAS f32x4* red = (LAS f32x4*)((LAS char*)lds + 2 * SLOT) + ts * 6 * 64 + lane;
  if (kh == 0) { red[3 * 64] = acc[3]; red[4 * 64] = acc[4]; red[5 * 64] = acc[5]; }
  else { red[0] = acc[0]; red[64] = acc[1]; red[128] = acc[2]; }
  RAWBAR();
  u16* qp = (u16*)(ws + WS_Q) + (size_t)token * D + g * 80 + gq * 4;
  if (kh == 0) {
#pragma unroll
    for (int c = 0; c < 3; ++c) {
      const f32x4 a = (acc[c] + red[c * 64]) * QSCALE;
      *(u32x2*)(qp + c * 16) = (u32x2){cvtpk(a[0], a[1]), cvtpk(a[2], a[3])};
    }
  } else {
#pragma unroll
    for (int c = 3; c < 5; ++c) {
      const f32x4 a = (acc[c] + red[c * 64]) * QSCALE;
      *(u32x2*)(qp + c * 16) = (u32x2){cvtpk(a[0], a[1]), cvtpk(a[2], a[3])};
    }
    if (g < 4) {
      char* kv = ws + WS_KV;
      const f32x4 a = acc[5] + red[5 * 64];
      const u32x2 v = {cvtpk(a[0], a[1]), cvtpk(a[2], a[3])};
      if (g < 2) {
        const int kt = token >> 5, r = token & 31;
        float n2 = dot2(v[0], v[0], dot2(v[1], v[1], 0.f));
        if (g == 0) {
          *(u32x2*)(kv + kt * KV_TILE + (gq >> 1) * 512 + r * 16 + (gq & 1) * 8) = v;
        } else {
          if (gq == 0) *(u32x2*)(kv + kt * KV_TILE + KV_K2 + r * 8) = v;
          else n2 = 0.f;
          if (gq == 1 && r == 0) { const u32x4 c1 = {0x00003F80u, 0u, 0u, 0u}; *(u32x4*)(kv + kt * KV_TILE + KV_C) = c1; }
        }
        n2 += __shfl_xor(n2, 16); n2 += __shfl_xor(n2, 32);
#pragma unroll
        for (int o = 1; o < 16; o <<= 1) n2 = fmaxf(n2, __shfl_xor(n2, o));
        if (lane == 0) ((float*)(ws + WS_KMAX))[g * 128 + tt * 4 + ts] = n2;
      } else {
        const int kt = tt * 2 + (ts >> 1);
        char* vp = kv + kt * KV_TILE + KV_V + (((ts & 1) * 2 + (gq & 1)) * VR) * 16 + (gq >> 1) * 8;
        if (g == 2) *(u32x2*)(vp + r16 * 16) = v;
        else if (r16 < 6) {
          const u32x2 one = {0x3F803F80u, 0x3F803F80u}, zer = {0u, 0u};
          *(u32x2*)(vp + (16 + r16) * 16) = r16 < 4 ? v : (r16 == 4 ? one : zer);
        }
      }
    }
  }
  ST(); ST_OUT(ws, 0);
}

struct Frag3 { bf16x8 x; s16x4 w[6]; };
template <int HC>
DI void gateup_block(const u16* __restrict__ x, const u16* __restrict__ w1, const u16* __restrict__ w3, char* __restrict__ ws, char* lds, int b) {
  constexpr int NP = HC ? 2 : 3;
  const int tid = threadIdx.x, lane = tid & 63, w = __builtin_amdgcn_readfirstlane(tid >> 6);
  const int bi = b >> 3;
  const int tp = ((b & 7) >> 2) * 8 + (bi & 7), cg = (b & 3) * 4 + (bi >> 3);
  const unsigned ldsw = (unsigned)(uintptr_t)(LAS char*)lds + w * 1024;
  ST_DECL ST();
  if (G1PRIO) __builtin_amdgcn_s_setprio(G1PRIO);
  const int srow = w * 16 + (lane >> 2);
  const u16* xsb = x + (size_t)tp * 128 * D;
  const unsigned xs = (srow * D + (((lane & 3) ^ ((srow >> 2) & 3)) * 8)) * 2;
  unsigned wsrc[2];
#pragma unroll
  for (int j = 0; j < 2; ++j) {
    const int pc = (w + 8 * j) % 12, gu = pc >= 6, rem = (pc - gu * 6) * 64 + lane, k = rem / 12, pos = rem - k * 12;
    wsrc[j] = (k * F + (pos ^ (gu << 1)) * 8) * 2;
  }
  const u16* wb0 = (w < 6 ? w1 : w3) + cg * 96;
  const u16* wb1 = w3 + cg * 96;
  auto piece = [&](int kt, int slot, int j) {
    if (j == 0) glds16s(xsb + kt * 32, xs, ldsw + slot * SLOT);
    if (j == 1) glds16s(wb0 + kt * 32 * F, wsrc[0], ldsw + slot * SLOT + 8192);
    if (j == 2 && NP == 3) glds16s(wb1 + kt * 32 * F, wsrc[1], ldsw + slot * SLOT + 16384);
  };
  auto stage = [&](int kt, int slot) { piece(kt, slot, 0); piece(kt, slot, 1); piece(kt, slot, 2); };
  stage(0, 0); stage(1, 1); stage(2, 2); stage(3, 3);
  const int tq = w & 3, r = lane & 31, h = lane >> 5;
  const int xrow = tq * 32 + r;
  const LAS char* xb0 = (const LAS char*)lds + xrow * 64 + ((h ^ ((xrow >> 2) & 3)) * 16);
  const LAS char* xb1 = (const LAS char*)lds + xrow * 64 + (((2 + h) ^ ((xrow >> 2) & 3)) * 16);
  const int gu = (lane >> 4) & 1, q4 = (lane & 15) >> 2, p = lane & 3, pp = ((p & 1) << 1) | (p >> 1);
  const LAS char* wb[3];
#pragma unroll
  for (int c = 0; c < 3; ++c) wb[c] = (const LAS char*)lds + 8192 + gu * 6144 + (8 * h + q4) * 192 + (((HC * 3 + c) ^ gu) * 32) + pp * 8;
  f32x16 acc[3] = {};
  float sum = 0.f, sq = 0.f, cs[3] = {0.f, 0.f, 0.f};
  auto rd = [&](Frag3& f, int slot, int s) {
    f.x = *(const LAS bf16x8*)((s ? xb1 : xb0) + slot * SLOT);
#pragma unroll
    for (int c = 0; c < 3; ++c) { f.w[2 * c] = trread(wb[c] + slot * SLOT + s * 3072); f.w[2 * c + 1] = trread(wb[c] + slot * SLOT + s * 3072 + 768); }
  };
  auto mm = [&](const Frag3& f, int slot, int s, int dk, int dsl) {
#pragma unroll
    for (int c = 0; c < 3; ++c) {
      acc[c] = MFMA32(cat8(f.w[2 * c], f.w[2 * c + 1]), f.x, acc[c]);
      SBAR();
      if (dk >= 0) { if (s == 1 && c == 0) piece(dk, dsl, 0); if (s == 1 && c == 2) piece(dk, dsl, 1); if (s == 0 && c == 0) piece(dk, dsl, 2); }
      SBAR();
    }
    if (s == HC) {
      const u32x4 xu = __builtin_bit_cast(u32x4, f.x);
#pragma unroll
      for (int j = 0; j < 4; ++j) { sum = dot2(xu[j], 0x3F803F80u, sum); sq = dot2(xu[j], xu[j], sq); }
    }
#pragma unroll
    for (int c = 0; c < 3; ++c)
      if (((slot * 6 + s * 3 + c) & 3) == tq) {
        const u32x2 a = __builtin_bit_cast(u32x2, f.w[2 * c]), bq = __builtin_bit_cast(u32x2, f.w[2 * c + 1]);
        cs[c] = dot2(a[0], 0x3F803F80u, dot2(a[1], 0x3F803F80u, dot2(bq[0], 0x3F803F80u, dot2(bq[1], 0x3F803F80u, cs[c]))));
      }
  };
  Frag3 f0, f1;
  vmwait<3 * NP>();
  RAWBAR();
  rd(f0, 0, 0);
#define IT(kt, sl, VM, PREV, DMA, NEXT) do { \
    rd(f1, sl, 1); SBAR(); mm(f0, sl, 0, PREV ? (kt) + 3 : -1, ((sl) + 3) & 3); SBAR(); \
    if (NEXT) { vmwait<VM>(); RAWBAR(); rd(f0, ((sl) + 1) & 3, 0); } \
    SBAR(); mm(f1, sl, 1, DMA ? (kt) + 4 : -1, sl); SBAR(); } while (0)
  IT(0, 0, 2 * NP, false, true, true); IT(1, 1, 2 * NP, true, true, true); IT(2, 2, 2 * NP, true, true, true); IT(3, 3, 2 * NP, true, true, true);
#pragma unroll 1
  for (int kt = 4; kt < 16; kt += 4) {
    IT(kt, 0, 2 * NP, true, true, true); IT(kt + 1, 1, 2 * NP, true, true, true); IT(kt + 2, 2, 2 * NP, true, true, true); IT(kt + 3, 3, 2 * NP, true, true, true);
  }
  IT(16, 0, 2 * NP, true, false, true); IT(17, 1, NP, false, false, true); IT(18, 2, 0, false, false, true); IT(19, 3, 0, false, false, false);
#undef IT
  PIN(acc[0]); PIN(acc[1]); PIN(acc[2]); ST();
  LAS f32x2* stp = (LAS f32x2*)lds;
  LAS float* csp = (LAS float*)((LAS char*)lds + 2048);
  LAS float* csf = (LAS float*)((LAS char*)lds + 6144);
  sum += __shfl_xor(sum, 32); sq += __shfl_xor(sq, 32);
#pragma unroll
  for (int c = 0; c < 3; ++c) cs[c] += __shfl_xor(cs[c], 32);
  if (h == 0) {
    stp[HC * 128 + xrow] = (f32x2){sum, sq};
#pragma unroll
    for (int c = 0; c < 3; ++c) csp[((tq * 2 + HC) * 3 + c) * 32 + r] = cs[c];
  }
  RAWBAR();
  if (tid < 192) csf[tid] = (csp[tid] + csp[192 + tid]) + (csp[384 + tid] + csp[576 + tid]);
  { const f32x2 o = stp[(HC ^ 1) * 128 + xrow]; sum += o[0]; sq += o[1]; }
  float mean_b, rden;
  ln_scalars(sum, sq, mean_b, rden);
  RAWBAR();
  u32x4 hv[3];
#pragma unroll
  for (int c = 0; c < 3; ++c) {
    float v[8];
#pragma unroll
    for (int jj = 0; jj < 2; ++jj) {
      const f32x4 cg4 = *(const LAS f32x4*)(csf + (HC * 3 + c) * 32 + 8 * jj + 4 * h), cu4 = *(const LAS f32x4*)(csf + (HC * 3 + c) * 32 + 16 + 8 * jj + 4 * h);
#pragma unroll
      for (int j = 0; j < 4; ++j) {
        const float gt = (acc[c][4 * jj + j] - mean_b * cg4[j]) * rden, up = (acc[c][8 + 4 * jj + j] - mean_b * cu4[j]) * rden;
        v[4 * jj + j] = gt * __builtin_amdgcn_rcpf(1.f + EXP2(-1.4426950408889634f * gt)) * up;
      }
    }
    hv[c] = (u32x4){cvtpk(v[0], v[1]), cvtpk(v[2], v[3]), cvtpk(v[4], v[5]), cvtpk(v[6], v[7])};
  }
#pragma unroll
  for (int c = 0; c < 3; ++c) *(LAS u32x4*)((LAS char*)lds + SLOT + xrow * 208 + HC * 96 + c * 32 + h * 16) = hv[c];
  RAWBAR();
  u16* hp = (u16*)(ws + WS_H) + (size_t)tp * 128 * F + cg * 96;
#pragma unroll
  for (int j = 0; j < 3; ++j) {
    const int ci = tid + 512 * j, row = ci / 12, pos = ci - row * 12;
    *(u32x4*)(hp + row * F + pos * 8) = *(const LAS u32x4*)((LAS char*)lds + SLOT + row * 208 + pos * 16);
  }
  ST(); ST_OUT(ws, 4);
}

__global__ __launch_bounds__(512, 4) void k_proj(const u16* __restrict__ x, const u16* __restrict__ wq, const u16* __restrict__ wk,
                                                  const u16* __restrict__ wv, const u16* __restrict__ w1, const u16* __restrict__ w3,
                                                  char* __restrict__ ws) {
  extern __shared__ __attribute__((aligned(16))) char lds[];
  const bool lo = __builtin_amdgcn_readfirstlane(threadIdx.x) < 256;
  if (blockIdx.x < 256) {
    if (lo) qkv_block<3>(x, wq, wk, wv, ws, lds, blockIdx.x); else qkv_block<2>(x, wq, wk, wv, ws, lds, blockIdx.x);
  } else {
    if (lo) gateup_block<0>(x, w1, w3, ws, lds, blockIdx.x - 256); else gateup_block<1>(x, w1, w3, ws, lds, blockIdx.x - 256);
  }
}

DI void attn_block(char* __restrict__ ws, char* lds, int b) {
  const int tid = threadIdx.x, lane = tid & 63, w = __builtin_amdgcn_readfirstlane(tid >> 6);
  const int bi = b >> 3;
  const int tb = (b & 7) * 8 + (bi & 7), head = (bi >> 3) * 8 + w;
  const int r = lane & 31, h = lane >> 5;
  const unsigned lds0 = (unsigned)(uintptr_t)(LAS char*)lds;
  const char* kvg = ws + WS_KV + w * 1024;
  const unsigned lane16 = lane * 16;
  auto dma1 = [&](int chunk, int slotoff, int j) {
    if (j < 2 || w < KV_PIECES - 16) glds16s(kvg + chunk * KV_CHUNK + j * 8192, lane16, lds0 + slotoff + (w + 8 * j) * 1024);
  };
  auto dma = [&](int chunk, int slotoff) { dma1(chunk, slotoff, 0); dma1(chunk, slotoff, 1); dma1(chunk, slotoff, 2); };
  ST_DECL ST();
  if (APRIO) __builtin_amdgcn_s_setprio(APRIO);
  const int token = tb * 32 + r;
  const u16* qp = (const u16*)(ws + WS_Q) + (size_t)token * D + head * DH + 8 * h;
  const u16* qp2 = (const u16*)(ws + WS_Q) + (size_t)token * D + head * DH + 16;
  const float* kmp = (const float*)(ws + WS_KMAX) + lane * 4;
  u32x4 q1u; u32x2 q2u; f32x4 km4;
  asm volatile("global_load_dwordx4 %0, %3, off\n\tglobal_load_dwordx2 %1, %4, off\n\tglobal_load_dwordx4 %2, %5, off"
               : "=&v"(q1u), "=&v"(q2u), "=&v"(km4) : "v"(qp), "v"(qp2), "v"(kmp) : "memory");
  dma(0, 0); dma(1, KV_SLOT);
  asm volatile("s_waitcnt vmcnt(4)" : "+v"(q1u), "+v"(q2u), "+v"(km4) :: "memory");
  float km = fmaxf(fmaxf(km4[0], km4[1]), fmaxf(km4[2], km4[3]));
#pragma unroll
  for (int o = 1; o < 32; o <<= 1) km = fmaxf(km, __shfl_xor(km, o));
  km += __shfl_xor(km, 32);
  float qn = dot2(q1u[0], q1u[0], dot2(q1u[1], q1u[1], dot2(q1u[2], q1u[2], dot2(q1u[3], q1u[3], 0.f))));
  if (h == 0) qn = dot2(q2u[0], q2u[0], dot2(q2u[1], q2u[1], qn));
  qn += __shfl_xor(qn, 32);
  const float bound = __builtin_amdgcn_sqrtf(qn * km) * 1.02f + 1e-3f;
  const bool fast = __all(bound <= 48.f);
  if (h == 1) { q2u[0] = fast ? (cvtpk(-bound, 0.f) & 0xffffu) : 0u; q2u[1] = 0u; }
  const bf16x8 q1 = __builtin_bit_cast(bf16x8, q1u);
  const s16x4 q2 = __builtin_bit_cast(s16x4, q2u);
  const int dv = r <= 20 ? r : 21;
  const LAS char* k1b = (const LAS char*)lds + lane * 16;
  const LAS char* k2b = (const LAS char*)lds + (h ? KV_C : KV_K2 + r * 8);
  const LAS char* vb = (const LAS char*)lds + KV_V + (h * VR + dv) * 16;
  f32x16 o = {};
  const f32x16 zero16 = {};
  vmwait<2>();
  __builtin_amdgcn_s_barrier();
  ST();
  if (fast) {
    f32x16 sA, sB;
    bf16x8 k1f, vf0, vf1; s16x4 k2f;
    u32x4 pa, pb;
    float e14, e15;
    k1f = *(const LAS bf16x8*)(k1b); k2f = *(const LAS s16x4*)(k2b);
    sA = MFMA32(k1f, q1, zero16); sA = MFMA32K8(k2f, q2, sA);
    k1f = *(const LAS bf16x8*)(k1b + KV_TILE); k2f = *(const LAS s16x4*)(k2b + KV_TILE);
    vf1 = (bf16x8){0, 0, 0, 0, 0, 0, 0, 0}; pb = (u32x4){0u, 0u, 0u, 0u}; e14 = 0.f; e15 = 0.f;
    int cur = 0, nxt = KV_SLOT, fre = 2 * KV_SLOT;
#define TILE(C, N, i, DM) do { \
      const int oV = (i) * KV_TILE, oK = ((i) + 2) * KV_TILE; \
      N = MFMA32(k1f, q1, zero16); SBAR(); \
      k1f = *(const LAS bf16x8*)((i) + 2 < 8 ? kc1 + oK : kn1 + (oK - 8 * KV_TILE)); vf0 = *(const LAS bf16x8*)(vc + oV); SBAR(); \
      C[0] = EXP2(C[0]); C[1] = EXP2(C[1]); SBAR(); pb[3] = cvtpk(e14, e15); PIN(pb); SBAR(); \
      C[2] = EXP2(C[2]); C[3] = EXP2(C[3]); SBAR(); pa[0] = cvtpk(C[0], C[1]); PIN(pa); SBAR(); \
      o = MFMA32(vf1, __builtin_bit_cast(bf16x8, pb), o); SBAR(); \
      vf1 = *(const LAS bf16x8*)(vc + oV + 2 * VR * 16); SBAR(); \
      C[4] = EXP2(C[4]); C[5] = EXP2(C[5]); SBAR(); pa[1] = cvtpk(C[2], C[3]); PIN(pa); SBAR(); \
      C[6] = EXP2(C[6]); C[7] = EXP2(C[7]); SBAR(); pa[2] = cvtpk(C[4], C[5]); PIN(pa); SBAR(); \
      N = MFMA32K8(k2f, q2, N); SBAR(); \
      k2f = *(const LAS s16x4*)((i) + 2 < 8 ? kc2 + oK : kn2 + (oK - 8 * KV_TILE)); SBAR(); \
      C[8] = EXP2(C[8]); C[9] = EXP2(C[9]); SBAR(); pa[3] = cvtpk(C[6], C[7]); PIN(pa); SBAR(); \
      C[10] = EXP2(C[10]); C[11] = EXP2(C[11]); SBAR(); pb[0] = cvtpk(C[8], C[9]); PIN(pb); SBAR(); \
      o = MFMA32(vf0, __builtin_bit_cast(bf16x8, pa), o); SBAR(); DM; SBAR(); \
      C[12] = EXP2(C[12]); C[13] = EXP2(C[13]); SBAR(); pb[1] = cvtpk(C[10], C[11]); PIN(pb); SBAR(); \
      e14 = EXP2(C[14]); e15 = EXP2(C[15]); SBAR(); pb[2] = cvtpk(C[12], C[13]); PIN(pb); PIN(e14); PIN(e15); SBAR(); \
    } while (0)
    for (int c = 0; c < 8; ++c) {
      const LAS char* kc1 = k1b + cur; const LAS char* kc2 = k2b + cur; const LAS char* vc = vb + cur;
      const LAS char* kn1 = k1b + nxt; const LAS char* kn2 = k2b + nxt;
      TILE(sA, sB, 0, (void)0); TILE(sB, sA, 1, (void)0); TILE(sA, sB, 2, (void)0);
      asm volatile("s_waitcnt vmcnt(0)\n\ts_barrier" ::: "memory");
      TILE(sB, sA, 3, if (c + 2 < 8) dma1(c + 2, fre, 0)); TILE(sA, sB, 4, if (c + 2 < 8) dma1(c + 2, fre, 1));
      TILE(sB, sA, 5, if (c + 2 < 8) dma1(c + 2, fre, 2)); TILE(sA, sB, 6, (void)0); TILE(sB, sA, 7, (void)0);
      const int t = cur; cur = nxt; nxt = fre; fre = t;
    }
    pb[3] = cvtpk(e14, e15);
    o = MFMA32(vf1, __builtin_bit_cast(bf16x8, pb), o);
#undef TILE
  } else {
    float m = -INFINITY;
    int cur = 0, nxt = KV_SLOT, fre = 2 * KV_SLOT;
#pragma unroll 1
    for (int c = 0; c < 8; ++c) {
#pragma unroll 1
      for (int i = 0; i < 8; ++i) {
        if (i == 3) {
          asm volatile("s_waitcnt vmcnt(0)\n\ts_barrier" ::: "memory");
          if (c + 2 < 8) dma(c + 2, fre);
        }
        const int off = cur + i * KV_TILE;
        const bf16x8 k1f = *(const LAS bf16x8*)(k1b + off); const s16x4 k2f = *(const LAS s16x4*)(k2b + off);
        const bf16x8 vf0 = *(const LAS bf16x8*)(vb + off), vf1 = *(const LAS bf16x8*)(vb + off + 2 * VR * 16);
        f32x16 sc = MFMA32(k1f, q1, zero16); sc = MFMA32K8(k2f, q2, sc);
        float mx = sc[0];
#pragma unroll
        for (int j = 1; j < 16; ++j) mx = fmaxf(mx, sc[j]);
        mx = fmaxf(mx, __shfl_xor(mx, 32));
        const float mn = fmaxf(m, mx);
        const float al = EXP2(m - mn);
        m = mn;
#pragma unroll
        for (int j = 0; j < 16; ++j) { o[j] *= al; sc[j] = EXP2(sc[j] - mn); }
        const u32x4 pa = {cvtpk(sc[0], sc[1]), cvtpk(sc[2], sc[3]), cvtpk(sc[4], sc[5]), cvtpk(sc[6], sc[7])};
        const u32x4 pb = {cvtpk(sc[8], sc[9]), cvtpk(sc[10], sc[11]), cvtpk(sc[12], sc[13]), cvtpk(sc[14], sc[15])};
        o = MFMA32(vf0, __builtin_bit_cast(bf16x8, pa), o);
        o = MFMA32(vf1, __builtin_bit_cast(bf16x8, pb), o);
      }
      const int t = cur; cur = nxt; nxt = fre; fre = t;
    }
  }
  PIN(o); ST();
  const float il = 1.f / __shfl(o[8], r + 32);
  u16* op = (u16*)(ws + WS_O) + (size_t)token * D + head * DH + 4 * h;
  *(u32x2*)(op) = (u32x2){cvtpk(o[0] * il, o[1] * il), cvtpk(o[2] * il, o[3] * il)};
  *(u32x2*)(op + 8) = (u32x2){cvtpk(o[4] * il, o[5] * il), cvtpk(o[6] * il, o[7] * il)};
  if (h == 0) *(u32x2*)(op + 16) = (u32x2){cvtpk(o[8] * il, o[9] * il), cvtpk(o[10] * il, o[11] * il)};
  ST(); ST_OUT(ws, 1);
}

DI void down_block(const u16* __restrict__ w2, char* __restrict__ ws, char* lds, int b) {
  const int tid = threadIdx.x, lane = tid & 63, w = __builtin_amdgcn_readfirstlane(tid >> 6);
  const int bi = b >> 3;
  const int p2 = (b & 7) * 2 + (bi & 1), u2 = bi >> 1, j2 = u2 / 5, ct = u2 - j2 * 5;
  const unsigned ldsw = (unsigned)(uintptr_t)(LAS char*)lds + w * 1024;
  ST_DECL ST();
  if (GPRIO) __builtin_amdgcn_s_setprio(GPRIO);
  const int srow = w * 16 + (lane >> 2);
  const u16* hb = (const u16*)(ws + WS_H) + (size_t)p2 * 128 * F + j2 * 512;
  const unsigned hs = (srow * F + (((lane & 3) ^ ((srow >> 2) & 3)) * 8)) * 2;
  const int sk = w * 4 + (lane >> 4);
  const u16* wbs = w2 + (size_t)j2 * 512 * D + ct * 128;
  const unsigned wsv = (sk * D + (((lane & 15) ^ ((sk & 3) << 2)) * 8)) * 2;
  auto stage = [&](int kt, int slot) {
    glds16s(hb + kt * 32, hs, ldsw + slot * 16384);
    glds16s(wbs + kt * 32 * D, wsv, ldsw + slot * 16384 + 8192);
  };
  stage(0, 0); stage(1, 1); stage(2, 2); stage(3, 3); stage(4, 4);
  const int th = w & 1, cq = w >> 1, r = lane & 31, h = lane >> 5;
  const LAS char* xb[2];
#pragma unroll
  for (int sx = 0; sx < 2; ++sx) { const int row = th * 64 + r; xb[sx] = (const LAS char*)lds + row * 64 + (((2 * sx + h) ^ ((row >> 2) & 3)) * 16); }
  const int q4 = (lane & 15) >> 2, gI = ((lane >> 4) & 1) * 4 + (lane & 3);
  const LAS char* wbp = (const LAS char*)lds + 8192 + (8 * h + q4) * 256 + ((cq ^ q4) * 64) + (4 * (gI & 1) + (gI >> 1)) * 8;
  struct Fr { bf16x8 x[2][2]; s16x4 w[2][2]; };
  auto rd = [&](Fr& f, int slot) {
#pragma unroll
    for (int sx = 0; sx < 2; ++sx) {
      f.w[sx][0] = trread(wbp + slot * 16384 + sx * 4096); f.w[sx][1] = trread(wbp + slot * 16384 + sx * 4096 + 1024);
      f.x[0][sx] = *(const LAS bf16x8*)(xb[sx] + slot * 16384); f.x[1][sx] = *(const LAS bf16x8*)(xb[sx] + slot * 16384 + 2048);
    }
  };
  f32x16 acc[2] = {};
  auto mm = [&](const Fr& f) {
#pragma unroll
    for (int sx = 0; sx < 2; ++sx) {
      acc[0] = MFMA32(cat8(f.w[sx][0], f.w[sx][1]), f.x[0][sx], acc[0]);
      acc[1] = MFMA32(cat8(f.w[sx][0], f.w[sx][1]), f.x[1][sx], acc[1]);
    }
  };
  Fr fa, fb;
  vmwait<8>();
  RAWBAR();
  rd(fa, 0);
#define IT(kt, FC, FN, VM, DMA) do { vmwait<VM>(); RAWBAR(); if (DMA) stage((kt) + 5, (kt) % 5); rd(FN, ((kt) + 1) % 5); SBAR(); mm(FC); SBAR(); } while (0)
  IT(0, fa, fb, 6, true); IT(1, fb, fa, 6, true); IT(2, fa, fb, 6, true); IT(3, fb, fa, 6, true); IT(4, fa, fb, 6, true);
  IT(5, fb, fa, 6, true); IT(6, fa, fb, 6, true); IT(7, fb, fa, 6, true); IT(8, fa, fb, 6, true); IT(9, fb, fa, 6, true);
  IT(10, fa, fb, 6, true); IT(11, fb, fa, 6, false); IT(12, fa, fb, 4, false); IT(13, fb, fa, 2, false); IT(14, fa, fb, 0, false);
  mm(fb);
#undef IT
  PIN(acc[0]); PIN(acc[1]); ST();
  u16* pp = (u16*)(ws + WS_P) + (size_t)j2 * S * D + (size_t)(p2 * 128 + th * 64 + r) * D + ct * 128 + cq * 32 + h * 16;
#pragma unroll
  for (int t = 0; t < 2; ++t) {
    *(u32x4*)(pp + t * 32 * D) = (u32x4){cvtpk(acc[t][0], acc[t][1]), cvtpk(acc[t][2], acc[t][3]), cvtpk(acc[t][4], acc[t][5]), cvtpk(acc[t][6], acc[t][7])};
    *(u32x4*)(pp + t * 32 * D + 8) = (u32x4){cvtpk(acc[t][8], acc[t][9]), cvtpk(acc[t][10], acc[t][11]), cvtpk(acc[t][12], acc[t][13]), cvtpk(acc[t][14], acc[t][15])};
  }
  ST(); ST_OUT(ws, 2);
}

__global__ __launch_bounds__(512, 4) void k_mid(const u16* __restrict__ w2, char* __restrict__ ws) {
  extern __shared__ __attribute__((aligned(16))) char lds[];
  if (blockIdx.x < 256) {
    attn_block(ws, lds, blockIdx.x);
  } else {
    down_block(w2, ws, lds, blockIdx.x - 256);
  }
}

struct Frag5 { bf16x8 x; s16x4 w[10]; };
template <int NP>
DI void out_body(const u16* __restrict__ a, unsigned as, const u16* __restrict__ bmat, unsigned bs0, unsigned bs1, unsigned ldsw,
                 const LAS char* xb, const LAS char* const (&wb)[2][2], f32x4 (&acc)[5]) {
  auto stage = [&](int kt, int slot) {
    glds16s(a + kt * 64, as, ldsw + slot * SLOT);
    glds16s(bmat + kt * 64 * D, bs0, ldsw + slot * SLOT + 8192);
    if (NP == 3) glds16s(bmat + kt * 64 * D, bs1, ldsw + slot * SLOT + 16384);
  };
  auto rd = [&](Frag5& f, int slot) {
    f.x = *(const LAS bf16x8*)(xb + slot * SLOT);
#pragma unroll
    for (int c = 0; c < 5; ++c) { f.w[2 * c] = trread(wb[0][c & 1] + slot * SLOT + (c >> 1) * 64); f.w[2 * c + 1] = trread(wb[1][c & 1] + slot * SLOT + (c >> 1) * 64); }
  };
  auto mm = [&](const Frag5& f) {
#pragma unroll
    for (int c = 0; c < 5; ++c) acc[c] = MFMA16(cat8(f.w[2 * c], f.w[2 * c + 1]), f.x, acc[c]);
  };
#pragma unroll
  for (int i = 0; i < 8; ++i) stage(i, i);
  Frag5 fa, fb;
  vmwait<7 * NP>();
  RAWBAR();
  rd(fa, 0);
#define IT(kt, sl, FC, FN, VM, DMA) do { vmwait<VM>(); RAWBAR(); if (DMA) stage((kt) + 8, sl); rd(FN, ((sl) + 1) % 8); SBAR(); mm(FC); SBAR(); } while (0)
  IT(0, 0, fa, fb, 6 * NP, true); IT(1, 1, fb, fa, 6 * NP, true); IT(2, 2, fa, fb, 6 * NP, false); IT(3, 3, fb, fa, 5 * NP, false);
  IT(4, 4, fa, fb, 4 * NP, false); IT(5, 5, fb, fa, 3 * NP, false); IT(6, 6, fa, fb, 2 * NP, false); IT(7, 7, fb, fa, NP, false);
  IT(8, 0, fa, fb, 0, false);
  mm(fb);
#undef IT
}
constexpr int K3_LDS = 8 * SLOT;
__global__ __launch_bounds__(512) void k_out(const u16* __restrict__ x, const u16* __restrict__ wo, const char* __restrict__ ws, u16* __restrict__ out) {
  extern __shared__ __attribute__((aligned(16))) char lds[];
  const int tid = threadIdx.x, lane = tid & 63, w = __builtin_amdgcn_readfirstlane(tid >> 6);
  const int b = blockIdx.x, bi = b >> 3;
  const int t0 = ((b & 7) * 4 + (bi & 3)) * 64, c0 = (bi >> 2) * 80;
  const int ts = w & 3, kh = w >> 2, r16 = lane & 15, gq = lane >> 4, q4 = (lane & 15) >> 2, p = lane & 3;
  const unsigned lds0 = (unsigned)(uintptr_t)(LAS char*)lds;
  ST_DECL ST();
  const int srow = w * 8 + (lane >> 3), scc = (lane & 7) ^ ((srow >> 1) & 7);
  const unsigned as = (srow * D + scc * 8) * 2;
  const int odd = (c0 >> 4) & 1;
  const u16* bmat = wo + (c0 - odd * 16);
  unsigned bs[2];
#pragma unroll
  for (int j = 0; j < 2; ++j) {
    const int ci = ((w + 8 * j) % 12) * 64 + lane, k = ci / 12, pos = ci - k * 12;
    int ch = pos ^ ((k >> 2) & 2);
    if (c0 - odd * 16 + ch * 8 >= D) ch = 0;
    bs[j] = (k * D + ch * 8) * 2;
  }
  const int xrow = ts * 16 + r16;
  const LAS char* xb = (const LAS char*)lds + xrow * 128 + (((kh * 4 + gq) ^ ((xrow >> 1) & 7)) * 16);
  const LAS char* wb[2][2];
#pragma unroll
  for (int t = 0; t < 2; ++t)
#pragma unroll
    for (int e = 0; e < 2; ++e) wb[t][e] = (const LAS char*)lds + 8192 + (kh * 32 + gq * 8 + t * 4 + q4) * 192 + (((e + odd) ^ (gq & 1)) * 32) + p * 8;
  f32x4 acc[5] = {};
  const u16* ob = (const u16*)(ws + WS_O) + (size_t)t0 * D;
  if (w < 4) out_body<3>(ob, as, bmat, bs[0], bs[1], lds0 + w * 1024, xb, wb, acc);
  else out_body<2>(ob, as, bmat, bs[0], bs[1], lds0 + w * 1024, xb, wb, acc);
  PIN(acc[0]); ST();
  RAWBAR();
#pragma unroll
  for (int c = 0; c < 5; ++c) *(LAS f32x4*)((LAS char*)lds + kh * 64 * 336 + xrow * 336 + c * 64 + gq * 16) = acc[c];
  u32x4 pv[4][2];
  const u16* pb = (const u16*)(ws + WS_P);
#pragma unroll
  for (int j = 0; j < 2; ++j) {
    const int ci = tid + 512 * j;
    if (ci < 640) {
      const int row = ci / 10, c = ci - row * 10;
      const size_t o0 = (size_t)(t0 + row) * D + c0 + c * 8;
      pv[0][j] = *(const u32x4*)(x + o0); pv[1][j] = *(const u32x4*)(pb + o0);
      pv[2][j] = *(const u32x4*)(pb + (size_t)S * D + o0); pv[3][j] = *(const u32x4*)(pb + (size_t)2 * S * D + o0);
    }
  }
  RAWBAR();
#pragma unroll
  for (int j = 0; j < 2; ++j) {
    const int ci = tid + 512 * j;
    if (ci < 640) {
      const int row = ci / 10, c = ci - row * 10;
      const LAS f32x4* a0 = (const LAS f32x4*)((LAS char*)lds + row * 336 + c * 32);
      const LAS f32x4* a1 = (const LAS f32x4*)((LAS char*)lds + 64 * 336 + row * 336 + c * 32);
      const f32x4 lo = a0[0] + a1[0], hi = a0[1] + a1[1];
      u32x4 res;
#pragma unroll
      for (int e = 0; e < 4; ++e) {
        const float s0 = (bflo(pv[0][j][e]) + bflo(pv[1][j][e])) + (bflo(pv[2][j][e]) + bflo(pv[3][j][e]));
        const float s1 = (bfhi(pv[0][j][e]) + bfhi(pv[1][j][e])) + (bfhi(pv[2][j][e]) + bfhi(pv[3][j][e]));
        const float g0 = e < 2 ? lo[2 * e] : hi[2 * e - 4], g1 = e < 2 ? lo[2 * e + 1] : hi[2 * e - 3];
        res[e] = cvtpk(g0 + s0, g1 + s1);
      }
      *(u32x4*)(out + (size_t)(t0 + row) * D + c0 + c * 8) = res;
    }
  }
  ST(); ST_OUT(ws, 3);
}

extern "C" void kernel_launch(void* const* d_in, const int* in_sizes, int n_in, void* d_out, int out_size, void* d_ws, size_t ws_size,
                              hipStream_t stream) {
  const u16* x = (const u16*)d_in[0];
  const u16* wq = (const u16*)d_in[1];
  const u16* wk = (const u16*)d_in[2];
  const u16* wv = (const u16*)d_in[3];
  const u16* wo = (const u16*)d_in[4];
  const u16* w1 = (const u16*)d_in[6];
  const u16* w3 = (const u16*)d_in[7];
  const u16* w2 = (const u16*)d_in[8];
  char* ws = (char*)d_ws;
  (void)hipFuncSetAttribute((const void*)k_proj, hipFuncAttributeMaxDynamicSharedMemorySize, LDS_BYTES);
  (void)hipFuncSetAttribute((const void*)k_mid, hipFuncAttributeMaxDynamicSharedMemorySize, LDS_BYTES);
  (void)hipFuncSetAttribute((const void*)k_out, hipFuncAttributeMaxDynamicSharedMemorySize, K3_LDS);
  k_proj<<<512, 512, LDS_BYTES, stream>>>(x, wq, wk, wv, w1, w3, ws);
  k_mid<<<496, 512, LDS_BYTES, stream>>>(w2, ws);
  k_out<<<256, 512, K3_LDS, stream>>>(x, wo, ws, (u16*)d_out);
}
```

```cpp
#include <hip/hip_runtime.h>
#include <hip/hip_bf16.h>
#include <stdint.h>

typedef short bf16x8 __attribute__((ext_vector_type(8)));
typedef short s16x4 __attribute__((ext_vector_type(4)));
typedef float f32x2 __attribute__((ext_vector_type(2)));
typedef float f32x4 __attribute__((ext_vector_type(4)));
typedef float f32x16 __attribute__((ext_vector_type(16)));
typedef unsigned u32x4 __attribute__((ext_vector_type(4)));
typedef unsigned u32x2 __attribute__((ext_vector_type(2)));
typedef __bf16 bf16x2_t __attribute__((ext_vector_type(2)));
typedef unsigned short u16;
#define LAS __attribute__((address_space(3)))
#define GAS __attribute__((address_space(1)))
#define RLX_AGENT __ATOMIC_RELAXED, __HIP_MEMORY_SCOPE_AGENT
#define DI __device__ __forceinline__
#define SBAR() __builtin_amdgcn_sched_barrier(0)
#define PIN(x) asm volatile("" : "+v"(x))

#define GPRIO 1
#define LADDER 1
#define APRIO 2
constexpr int S = 2048, D = 640, H = 32, DH = 20, F = 1536;
constexpr int VR = 22;
constexpr int KV_K2 = 1024, KV_C = 1280, KV_V = 1296, KV_TILE = KV_V + 4 * VR * 16;
constexpr int KV_CHUNK = 8 * KV_TILE;
constexpr int KV_PIECES = (KV_CHUNK + 1023) / 1024, KV_SLOT = KV_PIECES * 1024;
constexpr size_t WS_CNT = 0;
constexpr size_t WS_KMAX = 4096;
constexpr size_t WS_KV = 8192;
constexpr size_t WS_XN = WS_KV + 192 * 1024;
constexpr size_t WS_O = WS_XN + (size_t)S * D * 2;
constexpr size_t WS_H = WS_O + (size_t)S * D * 2;
constexpr size_t WS_WQ = WS_H + (size_t)S * F * 2;
constexpr size_t WS_WO = WS_WQ + (size_t)D * D * 2;
constexpr size_t WS_W13 = WS_WO + (size_t)D * D * 2;
constexpr size_t WS_W2 = WS_W13 + (size_t)2 * D * F * 2;
constexpr float QSCALE = 0.22360679774997896f * 1.4426950408889634f;
constexpr int LDS_BYTES = 81920, SLOT = 20480;

DI unsigned cvtpk(float a, float b) { f32x2 v = {a, b}; bf16x2_t r = __builtin_convertvector(v, bf16x2_t); return __builtin_bit_cast(unsigned, r); }
DI float bflo(unsigned u) { return __uint_as_float(u << 16); }
DI float bfhi(unsigned u) { return __uint_as_float(u & 0xffff0000u); }
DI float rbf(float a) { return bflo(cvtpk(a, a)); }
DI float dot2(unsigned a, unsigned b, float c) { return __builtin_amdgcn_fdot2_f32_bf16(__builtin_bit_cast(bf16x2_t, a), __builtin_bit_cast(bf16x2_t, b), c, false); }
DI s16x4 trread(const LAS char* p) { return __builtin_bit_cast(s16x4, __builtin_amdgcn_ds_read_tr16_b64_v4i16((LAS s16x4*)p)); }
DI bf16x8 cat8(s16x4 a, s16x4 b) { return __builtin_shufflevector(a, b, 0, 1, 2, 3, 4, 5, 6, 7); }
DI void glds16s(const void* sbase, unsigned voff, unsigned lds_base) {
  unsigned sv;
  asm volatile("s_mov_b32 %0, m0\n\ts_mov_b32 m0, %3\n\ts_nop 0\n\tglobal_load_lds_dwordx4 %1, %2\n\ts_mov_b32 m0, %0" : "=&s"(sv) : "v"(voff), "s"(sbase), "s"(lds_base) : "memory");
}
#define ST_DECL
#define ST()
#define ST_OUT(a, b)
#define MFMA32(a, b, c) __builtin_amdgcn_mfma_f32_32x32x16_bf16(a, b, c, 0, 0, 0)
#define MFMA32K8(a, b, c) __builtin_amdgcn_mfma_f32_32x32x8bf16_1k(a, b, c, 0, 0, 0)
#define MFMA16(a, b, c) __builtin_amdgcn_mfma_f32_16x16x32_bf16(a, b, c, 0, 0, 0)
#define EXP2(v) __builtin_amdgcn_exp2f(v)
#define RAWBAR() asm volatile("s_waitcnt lgkmcnt(0)\n\ts_barrier" ::: "memory")
DI void touch(const void* p, unsigned& d) { asm volatile("global_load_dword %0, %1, off" : "+v"(d) : "v"(p) : "memory"); }
template <int N> DI void vmwait() { asm volatile("s_waitcnt vmcnt(%0)" :: "n"(N) : "memory"); }
DI int wslot(int k) { return k ^ ((k >> 1) & 4); }
DI void ln_scalars(float sum, float sq, float& mean_b, float& rden) {
  const float mean = sum * (1.f / D);
  const float var = fmaxf(sq * (1.f / D) - mean * mean, 0.f);
  mean_b = rbf(mean);
  rden = __builtin_amdgcn_rcpf(rbf(__builtin_amdgcn_sqrtf(rbf(rbf(var) + 1e-5f))));
}

constexpr int RT_UNITS = 640 + 160 + 160 + 384;
DI void retile_wave(const u16* __restrict__ wq, const u16* __restrict__ wo, const u16* __restrict__ w1, const u16* __restrict__ w3,
                    const u16* __restrict__ w2, char* __restrict__ ws, char* lds, int u) {
  const int lane = threadIdx.x & 63, w = __builtin_amdgcn_readfirstlane(threadIdx.x >> 6);
  const unsigned ldsw = (unsigned)(uintptr_t)(LAS char*)lds + w * 6144;
  const LAS char* im = (const LAS char*)lds + w * 6144;
  const int h = lane >> 5, q4 = (lane & 15) >> 2, p = lane & 3, blk = (lane >> 4) & 1;
  ST_DECL ST();
  int rr[3], pos[3];
#pragma unroll
  for (int j = 0; j < 3; ++j) { const int ci = j * 64 + lane; rr[j] = ci / 12; pos[j] = ci - rr[j] * 12; }
  if (u < 640) {
    const int cg = u / 40, ks = u - cg * 40;
#pragma unroll
    for (int j = 0; j < 3; ++j) {
      glds16s(w1 + (size_t)ks * 16 * F + cg * 96, (rr[j] * F + pos[j] * 8) * 2, ldsw + j * 1024);
      glds16s(w3 + (size_t)ks * 16 * F + cg * 96, (rr[j] * F + pos[j] * 8) * 2, ldsw + 3072 + j * 1024);
    }
    vmwait<0>();
    const LAS char* a = im + blk * 3072 + (8 * h + q4) * 192 + (((p & 1) << 1) | (p >> 1)) * 8;
    char* dst = ws + WS_W13 + (size_t)u * 6144 + lane * 16;
#pragma unroll
    for (int c = 0; c < 6; ++c) *(bf16x8*)(dst + c * 1024) = cat8(trread(a + c * 32), trread(a + c * 32 + 768));
  } else {
    const int v = u - 640, kind = v < 160 ? 0 : v < 320 ? 1 : 2;
    const int i = v - kind * 160, nk = kind == 2 ? 48 : 20, cg = i / nk, ks = i - cg * nk;
    const int odd = cg & 1, c0 = cg * 80 - odd * 16;
    const u16* src = (kind == 0 ? wq : kind == 1 ? wo : w2) + (size_t)ks * 32 * D + c0;
#pragma unroll
    for (int j = 0; j < 3; ++j) {
      const unsigned vo = (rr[j] * D + (c0 + pos[j] * 8 < D ? pos[j] : 0) * 8) * 2;
      glds16s(src, vo, ldsw + j * 1024);
      glds16s(src + 16 * D, vo, ldsw + 3072 + j * 1024);
    }
    vmwait<0>();
    if (kind == 0) {
      const int gI = blk * 4 + p, dcol = gI < 4 ? ((gI & 1) * 8 + (gI >> 1) * 4) : 16, r = lane & 31;
      const LAS char* a = im + (8 * h + q4) * 192 + odd * 32 + dcol * 2;
      char* dst = ws + WS_WQ + (size_t)i * 5120 + (h * 20 + r) * 16;
#pragma unroll
      for (int s = 0; s < 2; ++s)
#pragma unroll
        for (int hd = 0; hd < 4; ++hd) {
          const bf16x8 f = cat8(trread(a + s * 3072 + hd * 40), trread(a + s * 3072 + hd * 40 + 768));
          if (r < 20) *(bf16x8*)(dst + (s * 4 + hd) * 640) = f;
        }
    } else {
      const LAS char* a = im + ((lane >> 4) * 8 + q4) * 192 + odd * 32 + p * 8;
      char* dst = ws + (kind == 1 ? WS_WO : WS_W2) + (size_t)i * 5120 + lane * 16;
#pragma unroll
      for (int c = 0; c < 5; ++c) *(bf16x8*)(dst + c * 1024) = cat8(trread(a + c * 32), trread(a + c * 32 + 768));
    }
  }
}

constexpr int PRE_W = 0, PRE_ST = 25600, PRE_RED = PRE_ST + 512, PRE_LDS = PRE_RED + 8192;
__global__ __launch_bounds__(256) void k_pre(const u16* __restrict__ x, const u16* __restrict__ wq, const u16* __restrict__ wk,
                                              const u16* __restrict__ wv, const u16* __restrict__ w1, const u16* __restrict__ w3,
                                              const u16* __restrict__ w2, const u16* __restrict__ wo, char* __restrict__ ws) {
  extern __shared__ __attribute__((aligned(16))) char lds[];
  const int tid = threadIdx.x, lane = tid & 63, w = __builtin_amdgcn_readfirstlane(tid >> 6);
  if (blockIdx.x >= 256) { retile_wave(wq, wo, w1, w3, w2, ws, lds, (blockIdx.x - 256) * 4 + w); return; }
  const int b = blockIdx.x, t16 = (b & 7) * 16 + (b >> 4), kv = (b >> 3) & 1;
  const int r16 = lane & 15, gq = lane >> 4;
  const unsigned lds0 = (unsigned)(uintptr_t)(LAS char*)lds;
  const int token = t16 * 16 + r16;
  ST_DECL ST();
  u32x4 xf[5];
  const u16* xp = x + (size_t)token * D + w * 160 + gq * 8;
#pragma unroll
  for (int s = 0; s < 5; ++s) asm volatile("global_load_dwordx4 %0, %1, off offset:%2" : "=v"(xf[s]) : "v"(xp), "i"(s * 64) : "memory");
  const u16* wsrc = kv ? wv : wk;
#pragma unroll
  for (int j = 0; j < 7; ++j)
    if (j < 6 || w == 0) glds16s(wsrc + (w + 4 * j) * 512, lane * 16, lds0 + PRE_W + (w + 4 * j) * 1024);
  ST();
  asm volatile("s_waitcnt vmcnt(0)" : "+v"(xf[0]), "+v"(xf[1]), "+v"(xf[2]), "+v"(xf[3]), "+v"(xf[4]) :: "memory");
  float sum = 0.f, sq = 0.f, sum2 = 0.f, sq2 = 0.f;
#pragma unroll
  for (int s = 0; s < 5; ++s)
#pragma unroll
    for (int j = 0; j < 4; j += 2) {
      sum = dot2(xf[s][j], 0x3F803F80u, sum); sq = dot2(xf[s][j], xf[s][j], sq);
      sum2 = dot2(xf[s][j + 1], 0x3F803F80u, sum2); sq2 = dot2(xf[s][j + 1], xf[s][j + 1], sq2);
    }
  sum += sum2; sq += sq2;
  sum += __shfl_xor(sum, 16); sq += __shfl_xor(sq, 16);
  sum += __shfl_xor(sum, 32); sq += __shfl_xor(sq, 32);
  if (b == 0 && tid < 48) ((unsigned*)(ws + WS_CNT))[tid * 16] = 0u;
  LAS f32x2* stp = (LAS f32x2*)((LAS char*)lds + PRE_ST);
  if (gq == 0) stp[w * 16 + r16] = (f32x2){sum, sq};
  RAWBAR();
  { const f32x2 a = stp[r16], c = stp[16 + r16], d = stp[32 + r16], e = stp[48 + r16]; sum = (a[0] + c[0]) + (d[0] + e[0]); sq = (a[1] + c[1]) + (d[1] + e[1]); }
  float mean_b, rden;
  ln_scalars(sum, sq, mean_b, rden);
  ST();
#pragma unroll
  for (int s = 0; s < 5; ++s)
#pragma unroll
    for (int j = 0; j < 4; ++j) {
      const unsigned d = cvtpk(bflo(xf[s][j]) - mean_b, bfhi(xf[s][j]) - mean_b);
      xf[s][j] = cvtpk(bflo(d) * rden, bfhi(d) * rden);
    }
  if ((w >> 1) == kv) {
    u16* xn = (u16*)(ws + WS_XN) + (size_t)token * D + w * 160 + gq * 8;
#pragma unroll
    for (int s = 0; s < 5; ++s) *(u32x4*)(xn + s * 32) = xf[s];
  }
  const int q4 = (lane & 15) >> 2, p = lane & 3;
  const LAS char* e0 = (const LAS char*)lds + PRE_W + (w * 160 + gq * 8 + q4) * 40 + p * 8;
  const LAS char* e1 = (const LAS char*)lds + PRE_W + (w * 160 + gq * 8 + q4) * 40 + 32;
  f32x4 acc[2] = {};
  if (kv == 0) {
#pragma unroll
    for (int s = 0; s < 5; ++s) {
      acc[0] = MFMA16(cat8(trread(e0 + s * 1280), trread(e0 + s * 1280 + 160)), __builtin_bit_cast(bf16x8, xf[s]), acc[0]);
      acc[1] = MFMA16(cat8(trread(e1 + s * 1280), trread(e1 + s * 1280 + 160)), __builtin_bit_cast(bf16x8, xf[s]), acc[1]);
    }
  } else {
#pragma unroll
    for (int s = 0; s < 5; ++s) {
      acc[0] = MFMA16(__builtin_bit_cast(bf16x8, xf[s]), cat8(trread(e0 + s * 1280), trread(e0 + s * 1280 + 160)), acc[0]);
      acc[1] = MFMA16(__builtin_bit_cast(bf16x8, xf[s]), cat8(trread(e1 + s * 1280), trread(e1 + s * 1280 + 160)), acc[1]);
    }
  }
  LAS f32x4* red = (LAS f32x4*)((LAS char*)lds + PRE_RED) + lane;
  red[(w * 2) * 64] = acc[0]; red[(w * 2 + 1) * 64] = acc[1];
  RAWBAR();
  ST();
  if (w < 2) {
    const f32x4 a = (red[w * 64] + red[(2 + w) * 64]) + (red[(4 + w) * 64] + red[(6 + w) * 64]);
    const u32x2 v = {cvtpk(a[0], a[1]), cvtpk(a[2], a[3])};
    char* kvp = ws + WS_KV + (t16 >> 1) * KV_TILE;
    if (kv == 0) {
      const int r = token & 31;
      float n2 = dot2(v[0], v[0], dot2(v[1], v[1], 0.f));
      if (w == 0) {
        *(u32x2*)(kvp + (gq >> 1) * 512 + r * 16 + (gq & 1) * 8) = v;
      } else {
        if (gq == 0) *(u32x2*)(kvp + KV_K2 + r * 8) = v;
        else n2 = 0.f;
        if (gq == 1 && r == 0) { const u32x4 c1 = {0x00003F80u, 0u, 0u, 0u}; *(u32x4*)(kvp + KV_C) = c1; }
      }
      n2 += __shfl_xor(n2, 16); n2 += __shfl_xor(n2, 32);
#pragma unroll
      for (int o = 1; o < 16; o <<= 1) n2 = fmaxf(n2, __shfl_xor(n2, o));
      if (lane == 0) ((float*)(ws + WS_KMAX))[w * 128 + t16] = n2;
    } else {
      char* vp = kvp + KV_V + (((t16 & 1) * 2 + (gq & 1)) * VR) * 16 + (gq >> 1) * 8;
      if (w == 0) *(u32x2*)(vp + r16 * 16) = v;
      else if (r16 < 6) {
        const u32x2 one = {0x3F803F80u, 0x3F803F80u}, zer = {0u, 0u};
        *(u32x2*)(vp + (16 + r16) * 16) = r16 < 4 ? v : (r16 == 4 ? one : zer);
      }
    }
  }
  ST(); ST_OUT(ws, 0);
}

template <int NL> DI void glds_lin(const char* src, unsigned lane16, unsigned dst) {
  unsigned sv;
  asm volatile("s_mov_b32 %0, m0\n\ts_mov_b32 m0, %3\n\ts_mov_b32 exec_hi, %4\n\tglobal_load_lds_dwordx4 %1, %2\n\ts_mov_b32 exec_hi, -1\n\ts_mov_b32 m0, %0"
               : "=&s"(sv) : "v"(lane16), "s"(src), "s"(dst), "n"((1u << (NL - 32)) - 1u) : "memory");
}

constexpr int ST64 = 18432;

constexpr int KV_A = LDS_BYTES - KV_SLOT, KV_B = 0, KV_Cs = KV_SLOT, O_STG = 2 * KV_SLOT;
struct FragQ { bf16x8 x[4]; bf16x8 w[4]; };

DI void attn_block(char* __restrict__ ws, char* lds, int b) {
  const int tid = threadIdx.x, lane = tid & 63, w = __builtin_amdgcn_readfirstlane(tid >> 6);
  const int bi = b >> 3;
  const int tt = (b & 7) * 4 + (bi & 3), t0 = tt * 64, hg = bi >> 2;
  const int th = w & 1, hd = w >> 1;
  const int r = lane & 31, h = lane >> 5;
  const unsigned lds0 = (unsigned)(uintptr_t)(LAS char*)lds;
  const char* kvg = ws + WS_KV + w * 1024;
  const unsigned lane16 = lane * 16;
  auto dma1 = [&](int chunk, int slotoff, int j) {
    if (j < 2 || w < KV_PIECES - 16) glds16s(kvg + chunk * KV_CHUNK + j * 8192, lane16, lds0 + slotoff + (w + 8 * j) * 1024);
  };
  auto dma = [&](int chunk, int slotoff) { dma1(chunk, slotoff, 0); dma1(chunk, slotoff, 1); dma1(chunk, slotoff, 2); };
  ST_DECL ST();
  if (APRIO) __builtin_amdgcn_s_setprio(APRIO);
  if (w == 0) glds16s(ws + WS_KMAX, lane16, lds0 + 4 * ST64);
  float km;
  f32x16 qa = {};
  {
    const int srow = w * 8 + (lane >> 3);
    const unsigned as = (srow * D + ((lane & 7) ^ ((srow >> 1) & 7)) * 8) * 2;
    const u16* ab = (const u16*)(ws + WS_XN) + (size_t)t0 * D;
    const char* wsrc = ws + WS_WQ + (size_t)hg * 102400 + w * 1280;
    auto piece = [&](int kt, int j) {
      const int slot = kt & 3;
      if (j == 0) glds16s(ab + kt * 64, as, lds0 + slot * ST64 + w * 1024);
      else glds_lin<40>(wsrc + kt * 10240 + (j - 1) * 640, lane16, lds0 + slot * ST64 + 8192 + w * 1280 + (j - 1) * 640);
    };
    const int xrow = th * 32 + r;
    const LAS char* xb[4];
#pragma unroll
    for (int s = 0; s < 4; ++s) xb[s] = (const LAS char*)lds + xrow * 128 + (((2 * s + h) ^ ((xrow >> 1) & 7)) * 16);
    const LAS char* wb = (const LAS char*)lds + 8192 + hd * 640 + (h * 20 + min(r, 19)) * 16;
    auto rd = [&](FragQ& f, int slot) {
#pragma unroll
      for (int s = 0; s < 4; ++s) { f.x[s] = *(const LAS bf16x8*)(xb[s] + slot * ST64); f.w[s] = *(const LAS bf16x8*)(wb + slot * ST64 + s * 2560); }
    };
    auto mm = [&](const FragQ& f, int dk) {
#pragma unroll
      for (int s = 0; s < 4; ++s) { qa = MFMA32(f.w[s], f.x[s], qa); SBAR(); if (dk >= 0 && s < 3) piece(dk, s); SBAR(); }
    };
#pragma unroll
    for (int i = 0; i < 4; ++i) { piece(i, 0); piece(i, 1); piece(i, 2); }
    FragQ fa, fb;
    vmwait<9>();
    RAWBAR();
    rd(fa, 0);
    {
      const f32x4 km4 = *(const LAS f32x4*)((LAS char*)lds + 4 * ST64 + lane * 16);
      km = fmaxf(fmaxf(km4[0], km4[1]), fmaxf(km4[2], km4[3]));
#pragma unroll
      for (int o = 1; o < 32; o <<= 1) km = fmaxf(km, __shfl_xor(km, o));
      km += __shfl_xor(km, 32);
      PIN(km);
    }
#define IT(kt, FC, FN, VM, DMA) do { vmwait<VM>(); RAWBAR(); rd(FN, ((kt) + 1) & 3); SBAR(); mm(FC, DMA ? (kt) + 4 : -1); SBAR(); } while (0)
    IT(0, fa, fb, 6, true); IT(1, fb, fa, 6, true); IT(2, fa, fb, 6, true); IT(3, fb, fa, 6, true);
    IT(4, fa, fb, 6, true); IT(5, fb, fa, 6, true); IT(6, fa, fb, 6, false);
    vmwait<3>(); RAWBAR(); dma(0, KV_A); rd(fa, 0); SBAR(); mm(fb, -1); SBAR();
    IT(8, fa, fb, 2, false);
    mm(fb, -1);
#undef IT
  }
  PIN(qa); ST();
  const u32x4 q1u = {cvtpk(qa[0] * QSCALE, qa[1] * QSCALE), cvtpk(qa[2] * QSCALE, qa[3] * QSCALE), cvtpk(qa[4] * QSCALE, qa[5] * QSCALE), cvtpk(qa[6] * QSCALE, qa[7] * QSCALE)};
  u32x2 q2u = {cvtpk(qa[8] * QSCALE, qa[9] * QSCALE), cvtpk(qa[10] * QSCALE, qa[11] * QSCALE)};
  float qn = dot2(q1u[0], q1u[0], dot2(q1u[1], q1u[1], dot2(q1u[2], q1u[2], dot2(q1u[3], q1u[3], 0.f))));
  if (h == 0) qn = dot2(q2u[0], q2u[0], dot2(q2u[1], q2u[1], qn));
  { const auto sw = __builtin_amdgcn_permlane32_swap(__float_as_uint(qn), __float_as_uint(qn), false, false); qn = __uint_as_float(sw[0]) + __uint_as_float(sw[1]); }
  const float bound = __builtin_amdgcn_sqrtf(qn * km) * 1.02f + 1e-3f;
  const bool fast = __all(bound <= 48.f);
  if (h == 1) { q2u[0] = fast ? (cvtpk(-bound, 0.f) & 0xffffu) : 0u; q2u[1] = 0u; }
  const bf16x8 q1 = __builtin_bit_cast(bf16x8, q1u);
  const s16x4 q2 = __builtin_bit_cast(s16x4, q2u);
  const int dv = r <= 20 ? r : 21;
  const LAS char* k1b = (const LAS char*)lds + lane * 16;
  const LAS char* k2b = (const LAS char*)lds + (h ? KV_C : KV_K2 + r * 8);
  const LAS char* vb = (const LAS char*)lds + KV_V + (h * VR + dv) * 16;
  f32x16 o = {};
  const f32x16 zero16 = {};
  vmwait<0>();
  RAWBAR();
  dma(1, KV_B);
  ST();
  if (fast) {
    f32x16 sA, sB;
    bf16x8 k1f, vf0, vf1; s16x4 k2f;
    u32x4 pa, pb;
    float e14, e15;
    k1f = *(const LAS bf16x8*)(k1b + KV_A); k2f = *(const LAS s16x4*)(k2b + KV_A);
    sA = MFMA32(k1f, q1, zero16); sA = MFMA32K8(k2f, q2, sA);
    k1f = *(const LAS bf16x8*)(k1b + KV_A + KV_TILE); k2f = *(const LAS s16x4*)(k2b + KV_A + KV_TILE);
    vf1 = (bf16x8){0, 0, 0, 0, 0, 0, 0, 0}; pb = (u32x4){0u, 0u, 0u, 0u}; e14 = 0.f; e15 = 0.f;
    int cur = KV_A, nxt = KV_B, fre = KV_Cs;
#define TILE(C, N, i, DM) do { \
      const int oV = (i) * KV_TILE, oK = ((i) + 2) * KV_TILE; \
      N = MFMA32(k1f, q1, zero16); SBAR(); \
      k1f = *(const LAS bf16x8*)((i) + 2 < 8 ? kc1 + oK : kn1 + (oK - 8 * KV_TILE)); vf0 = *(const LAS bf16x8*)(vc + oV); SBAR(); \
      C[0] = EXP2(C[0]); C[1] = EXP2(C[1]); SBAR(); pb[3] = cvtpk(e14, e15); PIN(pb); SBAR(); \
      C[2] = EXP2(C[2]); C[3] = EXP2(C[3]); SBAR(); pa[0] = cvtpk(C[0], C[1]); PIN(pa); SBAR(); \
      o = MFMA32(vf1, __builtin_bit_cast(bf16x8, pb), o); SBAR(); \
      vf1 = *(const LAS bf16x8*)(vc + oV + 2 * VR * 16); SBAR(); \
      C[4] = EXP2(C[4]); C[5] = EXP2(C[5]); SBAR(); pa[1] = cvtpk(C[2], C[3]); PIN(pa); SBAR(); \
      C[6] = EXP2(C[6]); C[7] = EXP2(C[7]); SBAR(); pa[2] = cvtpk(C[4], C[5]); PIN(pa); SBAR(); \
      N = MFMA32K8(k2f, q2, N); SBAR(); \
      k2f = *(const LAS s16x4*)((i) + 2 < 8 ? kc2 + oK : kn2 + (oK - 8 * KV_TILE)); SBAR(); \
      C[8] = EXP2(C[8]); C[9] = EXP2(C[9]); SBAR(); pa[3] = cvtpk(C[6], C[7]); PIN(pa); SBAR(); \
      C[10] = EXP2(C[10]); C[11] = EXP2(C[11]); SBAR(); pb[0] = cvtpk(C[8], C[9]); PIN(pb); SBAR(); \
      o = MFMA32(vf0, __builtin_bit_cast(bf16x8, pa), o); SBAR(); DM; SBAR(); \
      C[12] = EXP2(C[12]); C[13] = EXP2(C[13]); SBAR(); pb[1] = cvtpk(C[10], C[11]); PIN(pb); SBAR(); \
      e14 = EXP2(C[14]); e15 = EXP2(C[15]); SBAR(); pb[2] = cvtpk(C[12], C[13]); PIN(pb); PIN(e14); PIN(e15); SBAR(); \
    } while (0)
    for (int c = 0; c < 8; ++c) {
      const LAS char* kc1 = k1b + cur; const LAS char* kc2 = k2b + cur; const LAS char* vc = vb + cur;
      const LAS char* kn1 = k1b + nxt; const LAS char* kn2 = k2b + nxt;
      TILE(sA, sB, 0, (void)0); TILE(sB, sA, 1, (void)0); TILE(sA, sB, 2, (void)0);
      asm volatile("s_waitcnt vmcnt(0)\n\ts_barrier" ::: "memory");
      if (LADDER && w >= 4) __builtin_amdgcn_s_setprio(0);
      TILE(sB, sA, 3, if (c + 2 < 8) dma1(c + 2, fre, 0));
      if (LADDER == 1) __builtin_amdgcn_s_setprio(APRIO);
      TILE(sA, sB, 4, if (c + 2 < 8) dma1(c + 2, fre, 1));
      if (LADDER == 2) __builtin_amdgcn_s_setprio(APRIO);
      TILE(sB, sA, 5, if (c + 2 < 8) dma1(c + 2, fre, 2)); TILE(sA, sB, 6, (void)0); TILE(sB, sA, 7, (void)0);
      const int t = cur; cur = nxt; nxt = fre; fre = t;
    }
    pb[3] = cvtpk(e14, e15);
    o = MFMA32(vf1, __builtin_bit_cast(bf16x8, pb), o);
#undef TILE
  } else {
    float m = -INFINITY;
    int cur = KV_A, nxt = KV_B, fre = KV_Cs;
#pragma unroll 1
    for (int c = 0; c < 8; ++c) {
#pragma unroll 1
      for (int i = 0; i < 8; ++i) {
        if (i == 3) {
          asm volatile("s_waitcnt vmcnt(0)\n\ts_barrier" ::: "memory");
          if (c + 2 < 8) dma(c + 2, fre);
        }
        const int off = cur + i * KV_TILE;
        const bf16x8 k1f = *(const LAS bf16x8*)(k1b + off); const s16x4 k2f = *(const LAS s16x4*)(k2b + off);
        const bf16x8 vf0 = *(const LAS bf16x8*)(vb + off), vf1 = *(const LAS bf16x8*)(vb + off + 2 * VR * 16);
        f32x16 sc = MFMA32(k1f, q1, zero16); sc = MFMA32K8(k2f, q2, sc);
        float mx = sc[0];
#pragma unroll
        for (int j = 1; j < 16; ++j) mx = fmaxf(mx, sc[j]);
        mx = fmaxf(mx, __shfl_xor(mx, 32));
        const float mn = fmaxf(m, mx);
        const float al = EXP2(m - mn);
        m = mn;
#pragma unroll
        for (int j = 0; j < 16; ++j) { o[j] *= al; sc[j] = EXP2(sc[j] - mn); }
        const u32x4 pa = {cvtpk(sc[0], sc[1]), cvtpk(sc[2], sc[3]), cvtpk(sc[4], sc[5]), cvtpk(sc[6], sc[7])};
        const u32x4 pb = {cvtpk(sc[8], sc[9]), cvtpk(sc[10], sc[11]), cvtpk(sc[12], sc[13]), cvtpk(sc[14], sc[15])};
        o = MFMA32(vf0, __builtin_bit_cast(bf16x8, pa), o);
        o = MFMA32(vf1, __builtin_bit_cast(bf16x8, pb), o);
      }
      const int t = cur; cur = nxt; nxt = fre; fre = t;
    }
  }
  PIN(o); ST();
  const float il = 1.f / __shfl(o[8], r + 32);
  LAS char* os = (LAS char*)lds + O_STG + (th * 32 + r) * 176 + hd * 40 + 8 * h;
  *(LAS u32x2*)(os) = (u32x2){cvtpk(o[0] * il, o[1] * il), cvtpk(o[2] * il, o[3] * il)};
  *(LAS u32x2*)(os + 16) = (u32x2){cvtpk(o[4] * il, o[5] * il), cvtpk(o[6] * il, o[7] * il)};
  if (h == 0) *(LAS u32x2*)(os + 32) = (u32x2){cvtpk(o[8] * il, o[9] * il), cvtpk(o[10] * il, o[11] * il)};
  RAWBAR();
  const __amdgpu_buffer_rsrc_t orr = __builtin_amdgcn_make_buffer_rsrc(ws + WS_O, 0, S * D * 2, 0x00020000);
#pragma unroll
  for (int j = 0; j < 2; ++j) {
    const int ci = tid + 512 * j;
    if (ci < 640) {
      const int c = ci >> 6, cc = hg * 10 + c;
      const u32x4 v = *(const LAS u32x4*)((LAS char*)lds + O_STG + lane * 176 + c * 16);
      __builtin_amdgcn_raw_buffer_store_b128(v, orr, ((tt * 20 + (cc >> 2)) * 4 + (lane >> 4)) * 1024 + ((cc & 3) * 16 + (lane & 15)) * 16, 0, 16);
    }
  }
  vmwait<0>();
  RAWBAR();
  if (tid == 0) __hip_atomic_fetch_add((GAS unsigned*)(ws + WS_CNT) + (16 + tt) * 16, 1u, RLX_AGENT);
  ST(); ST_OUT(ws, 1);
}

struct Frag3 { bf16x8 x; bf16x8 w[3]; };
struct Frag5 { bf16x8 x; bf16x8 w[5]; };
DI void wait_count(GAS unsigned* cnt, unsigned want) {
  for (unsigned spins = 0; spins < (1u << 22); ++spins) {
    if ((unsigned)__builtin_amdgcn_readfirstlane(__hip_atomic_load(cnt, RLX_AGENT)) >= want) break;
    __builtin_amdgcn_s_sleep(2);
  }
}
DI void ffn_block(const u16* __restrict__ x, char* __restrict__ ws, u16* __restrict__ out, char* lds, int b) {
  const int tid = threadIdx.x, lane = tid & 63, w = __builtin_amdgcn_readfirstlane(tid >> 6);
  const int bi = b >> 3;
  const unsigned lds0 = (unsigned)(uintptr_t)(LAS char*)lds, lane16 = lane * 16;
  ST_DECL ST();
  if (GPRIO) __builtin_amdgcn_s_setprio(GPRIO);
  {
    const int tp = ((b & 7) >> 2) * 8 + (bi & 7), cg = (b & 3) * 4 + (bi >> 3);
    const int srow = w * 16 + (lane >> 2);
    const u16* xsb = (const u16*)(ws + WS_XN) + (size_t)tp * 128 * D;
    const unsigned xs = (srow * D + (((lane & 3) ^ ((srow >> 2) & 3)) * 8)) * 2;
    const char* wsrc = ws + WS_W13 + (size_t)cg * 245760 + w * 1536;
    auto piece = [&](int kt, int slot, int j) {
      if (j == 0) glds16s(xsb + kt * 32, xs, lds0 + slot * SLOT + w * 1024);
      else glds_lin<48>(wsrc + kt * 12288 + (j - 1) * 768, lane16, lds0 + slot * SLOT + 8192 + w * 1536 + (j - 1) * 768);
    };
#pragma unroll
    for (int i = 0; i < 4; ++i) { piece(i, i, 0); piece(i, i, 1); piece(i, i, 2); }
    const int tq = w & 3, hc = w >> 2, r = lane & 31, h = lane >> 5;
    const int xrow = tq * 32 + r;
    const LAS char* xb0 = (const LAS char*)lds + xrow * 64 + ((h ^ ((xrow >> 2) & 3)) * 16);
    const LAS char* xb1 = (const LAS char*)lds + xrow * 64 + (((2 + h) ^ ((xrow >> 2) & 3)) * 16);
    const LAS char* wb = (const LAS char*)lds + 8192 + hc * 3072 + lane * 16;
    f32x16 acc[3] = {};
    auto rd = [&](Frag3& f, int slot, int s) {
      f.x = *(const LAS bf16x8*)((s ? xb1 : xb0) + slot * SLOT);
#pragma unroll
      for (int c = 0; c < 3; ++c) f.w[c] = *(const LAS bf16x8*)(wb + slot * SLOT + s * 6144 + c * 1024);
    };
    auto mm = [&](const Frag3& f, int s, int dk, int dsl) {
#pragma unroll
      for (int c = 0; c < 3; ++c) {
        acc[c] = MFMA32(f.w[c], f.x, acc[c]);
        SBAR();
        if (dk >= 0) { if (s == 1 && c == 0) piece(dk, dsl, 0); if (s == 1 && c == 2) piece(dk, dsl, 1); if (s == 0 && c == 0) piece(dk, dsl, 2); }
        SBAR();
      }
    };
    Frag3 f0, f1;
    vmwait<9>();
    RAWBAR();
    rd(f0, 0, 0);
#define IT(kt, sl, VM, PREV, DMA, NEXT) do { \
      rd(f1, sl, 1); SBAR(); mm(f0, 0, PREV ? (kt) + 3 : -1, ((sl) + 3) & 3); SBAR(); \
      if (NEXT) { vmwait<VM>(); RAWBAR(); rd(f0, ((sl) + 1) & 3, 0); } \
      SBAR(); mm(f1, 1, DMA ? (kt) + 4 : -1, sl); SBAR(); } while (0)
    IT(0, 0, 6, false, true, true); IT(1, 1, 6, true, true, true); IT(2, 2, 6, true, true, true); IT(3, 3, 6, true, true, true);
#pragma unroll 1
    for (int kt = 4; kt < 16; kt += 4) {
      IT(kt, 0, 6, true, true, true); IT(kt + 1, 1, 6, true, true, true); IT(kt + 2, 2, 6, true, true, true); IT(kt + 3, 3, 6, true, true, true);
    }
    IT(16, 0, 6, true, false, true); IT(17, 1, 3, false, false, true); IT(18, 2, 0, false, false, true); IT(19, 3, 0, false, false, false);
#undef IT
    PIN(acc[0]); PIN(acc[1]); PIN(acc[2]); ST();
    __builtin_amdgcn_s_setprio(3);
    u32x4 hv[3];
#pragma unroll
    for (int c = 0; c < 3; ++c) {
      float v[8];
#pragma unroll
      for (int j = 0; j < 8; ++j) {
        const float gt = acc[c][j], up = acc[c][j + 8];
        v[j] = gt * __builtin_amdgcn_rcpf(1.f + EXP2(-1.4426950408889634f * gt)) * up;
      }
      hv[c] = (u32x4){cvtpk(v[0], v[1]), cvtpk(v[2], v[3]), cvtpk(v[4], v[5]), cvtpk(v[6], v[7])};
    }
#pragma unroll
    for (int c = 0; c < 3; ++c) *(LAS u32x4*)((LAS char*)lds + xrow * 208 + hc * 96 + c * 32 + h * 16) = hv[c];
    RAWBAR();
    const __amdgpu_buffer_rsrc_t hr = __builtin_amdgcn_make_buffer_rsrc(ws + WS_H, 0, S * F * 2, 0x00020000);
#pragma unroll
    for (int j = 0; j < 3; ++j) {
      const int ci = tid + 512 * j, row = ci / 12, pos = ci - row * 12;
      const u32x4 v = *(const LAS u32x4*)((LAS char*)lds + row * 208 + pos * 16);
      __builtin_amdgcn_raw_buffer_store_b128(v, hr, ((tp * 128 + row) * F + cg * 96 + pos * 8) * 2, 0, 16);
    }
    vmwait<0>();
    RAWBAR();
    if (tid == 0) __hip_atomic_fetch_add((GAS unsigned*)(ws + WS_CNT) + tp * 16, 1u, RLX_AGENT);
  }
  ST();
  const int tt = (b & 7) * 4 + (bi & 3), t0 = tt * 64, cg = bi >> 2, c0 = cg * 80;
  const int ts = w & 3, kh = w >> 2, r16 = lane & 15, gq = lane >> 4, xrow = ts * 16 + r16;
  f32x4 acc[5] = {};
  {
    const int srow = w * 8 + (lane >> 3);
    const unsigned hs = (srow * F + ((lane & 7) ^ ((srow >> 1) & 7)) * 8) * 2;
    const u16* hb = (const u16*)(ws + WS_H) + (size_t)t0 * F;
    const char* wsrc = ws + WS_W2 + (size_t)cg * 245760 + w * 1280;
    auto piece = [&](int kt, int slot, int j) {
      if (j == 0) glds16s(hb + kt * 64, hs, lds0 + slot * ST64 + w * 1024);
      else glds_lin<40>(wsrc + kt * 10240 + (j - 1) * 640, lane16, lds0 + slot * ST64 + 8192 + w * 1280 + (j - 1) * 640);
    };
#pragma unroll
    for (int i = 0; i < 4; ++i) { piece(i, i, 1); piece(i, i, 2); }
    if (w == 0) {
      wait_count((GAS unsigned*)(ws + WS_CNT) + (tt >> 1) * 16, 16u);
      __builtin_amdgcn_fence(__ATOMIC_ACQUIRE, "agent");
      vmwait<0>();
    }
    RAWBAR();
    __builtin_amdgcn_s_setprio(GPRIO);
    ST();
#pragma unroll
    for (int i = 0; i < 4; ++i) piece(i, i, 0);
    const LAS char* xb = (const LAS char*)lds + xrow * 128 + (((kh * 4 + gq) ^ ((xrow >> 1) & 7)) * 16);
    const LAS char* wb = (const LAS char*)lds + 8192 + kh * 5120 + lane * 16;
    auto rd = [&](Frag5& f, int slot) {
      f.x = *(const LAS bf16x8*)(xb + slot * ST64);
#pragma unroll
      for (int c = 0; c < 5; ++c) f.w[c] = *(const LAS bf16x8*)(wb + slot * ST64 + c * 1024);
    };
    auto mm = [&](const Frag5& f, int dk, int dsl) {
#pragma unroll
      for (int c = 0; c < 5; ++c) { acc[c] = MFMA16(f.w[c], f.x, acc[c]); SBAR(); if (dk >= 0 && !(c & 1)) piece(dk, dsl, c >> 1); SBAR(); }
    };
    Frag5 fa, fb;
    vmwait<3>();
    RAWBAR();
    rd(fa, 0);
#define IT(kt, sl, FC, FN, VM, DMA) do { vmwait<VM>(); RAWBAR(); rd(FN, ((sl) + 1) & 3); SBAR(); mm(FC, DMA ? (kt) + 4 : -1, sl); SBAR(); } while (0)
    IT(0, 0, fa, fb, 2, true); IT(1, 1, fb, fa, 4, true); IT(2, 2, fa, fb, 6, true); IT(3, 3, fb, fa, 6, true);
#pragma unroll 1
    for (int kt = 4; kt < 20; kt += 4) {
      IT(kt, 0, fa, fb, 6, true); IT(kt + 1, 1, fb, fa, 6, true); IT(kt + 2, 2, fa, fb, 6, true); IT(kt + 3, 3, fb, fa, 6, true);
    }
    IT(20, 0, fa, fb, 6, false); IT(21, 1, fb, fa, 3, false); IT(22, 2, fa, fb, 0, false);
    mm(fb, -1, 0);
#undef IT
  }
  PIN(acc[0]); ST();
  RAWBAR();
  const char* wog = ws + WS_WO + (size_t)cg * 102400;
#pragma unroll
  for (int j = 0; j < 10; ++j) glds16s(wog + (j >= 5 ? 10240 : 0) + (j * 8 + w) * 1024, lane16, lds0 + (j * 8 + w) * 1024);
  bf16x8 wr[10];
#pragma unroll
  for (int j = 0; j < 10; ++j) wr[j] = *(const bf16x8*)(wog + kh * 51200 + 40960 + j * 1024 + lane16);
  if (kh == 0) {
#pragma unroll
    for (int c = 0; c < 5; ++c) {
      const u32x2 xv = *(const u32x2*)(x + (size_t)(t0 + xrow) * D + c0 + c * 16 + gq * 4);
      acc[c] += (f32x4){bflo(xv[0]), bfhi(xv[0]), bflo(xv[1]), bfhi(xv[1])};
    }
  }
#pragma unroll
  for (int j = 0; j < 10; ++j) PIN(wr[j]);
#pragma unroll
  for (int c = 0; c < 5; ++c) PIN(acc[c]);
  vmwait<0>();
  __builtin_amdgcn_s_setprio(3);
  ST();
  RAWBAR();
  const LAS char* wbt = (const LAS char*)lds + kh * 40960 + lane * 16;
  bf16x8 w0[5];
#pragma unroll
  for (int c = 0; c < 5; ++c) { w0[c] = *(const LAS bf16x8*)(wbt + c * 1024); PIN(w0[c]); }
  if (w == 0) wait_count((GAS unsigned*)(ws + WS_CNT) + (16 + tt) * 16, 8u);
  RAWBAR();
  ST();
  {
    u32x4 of[10];
    const char* op = ws + WS_O + ((tt * 20 + kh * 10) * 4 + ts) * 1024;
#pragma unroll
    for (int s = 0; s < 10; ++s) asm volatile("global_load_dwordx4 %0, %1, %2 sc1" : "=v"(of[(s + 8) % 10]) : "v"(lane16), "s"(op + ((s + 8) % 10) * 4096) : "memory");
#define OSTEP(s, N, WC, WN) do { \
      if ((s) + 1 < 8) { _Pragma("unroll") for (int c = 0; c < 5; ++c) WN[c] = *(const LAS bf16x8*)(wbt + ((s) + 1) * 5120 + c * 1024); } \
      SBAR(); asm volatile("s_waitcnt vmcnt(" #N ")" : "+v"(of[s]) :: "memory"); SBAR(); \
      _Pragma("unroll") for (int c = 0; c < 5; ++c) acc[c] = MFMA16(WC[c], __builtin_bit_cast(bf16x8, of[s]), acc[c]); SBAR(); } while (0)
    asm volatile("s_waitcnt vmcnt(9)" : "+v"(of[8]) :: "memory");
#pragma unroll
    for (int c = 0; c < 5; ++c) acc[c] = MFMA16(wr[c], __builtin_bit_cast(bf16x8, of[8]), acc[c]);
    SBAR(); asm volatile("s_waitcnt vmcnt(8)" : "+v"(of[9]) :: "memory"); SBAR();
#pragma unroll
    for (int c = 0; c < 5; ++c) acc[c] = MFMA16(wr[5 + c], __builtin_bit_cast(bf16x8, of[9]), acc[c]);
    SBAR();
    bf16x8 wn[5];
    OSTEP(0, 7, w0, wn); OSTEP(1, 6, wn, w0); OSTEP(2, 5, w0, wn); OSTEP(3, 4, wn, w0);
    OSTEP(4, 3, w0, wn); OSTEP(5, 2, wn, w0); OSTEP(6, 1, w0, wn); OSTEP(7, 0, wn, w0);
#undef OSTEP
  }
  RAWBAR();
#pragma unroll
  for (int c = 0; c < 5; ++c) *(LAS f32x4*)((LAS char*)lds + kh * 64 * 336 + xrow * 336 + c * 64 + gq * 16) = acc[c];
  RAWBAR();
#pragma unroll
  for (int j = 0; j < 2; ++j) {
    const int ci = tid + 512 * j;
    if (ci < 640) {
      const int row = ci / 10, c = ci - row * 10;
      const LAS f32x4* a0 = (const LAS f32x4*)((LAS char*)lds + row * 336 + c * 32);
      const LAS f32x4* a1 = (const LAS f32x4*)((LAS char*)lds + 64 * 336 + row * 336 + c * 32);
      const f32x4 lo = a0[0] + a1[0], hi = a0[1] + a1[1];
      const u32x4 res = {cvtpk(lo[0], lo[1]), cvtpk(lo[2], lo[3]), cvtpk(hi[0], hi[1]), cvtpk(hi[2], hi[3])};
      *(u32x4*)(out + (size_t)(t0 + row) * D + c0 + c * 8) = res;
    }
  }
  ST(); ST_OUT(ws, 2);
}

__global__ __launch_bounds__(512, 4) void k_mid(const u16* __restrict__ x, char* __restrict__ ws, u16* __restrict__ out) {
  extern __shared__ __attribute__((aligned(16))) char lds[];
  if (blockIdx.x < 256) attn_block(ws, lds, blockIdx.x);
  else ffn_block(x, ws, out, lds, blockIdx.x - 256);
}

extern "C" void kernel_launch(void* const* d_in, const int* in_sizes, int n_in, void* d_out, int out_size, void* d_ws, size_t ws_size,
                              hipStream_t stream) {
  const u16* x = (const u16*)d_in[0];
  const u16* wq = (const u16*)d_in[1];
  const u16* wk = (const u16*)d_in[2];
  const u16* wv = (const u16*)d_in[3];
  const u16* wo = (const u16*)d_in[4];
  const u16* w1 = (const u16*)d_in[6];
  const u16* w3 = (const u16*)d_in[7];
  const u16* w2 = (const u16*)d_in[8];
  char* ws = (char*)d_ws;
  (void)hipFuncSetAttribute((const void*)k_mid, hipFuncAttributeMaxDynamicSharedMemorySize, LDS_BYTES);
  k_pre<<<256 + RT_UNITS / 4, 256, PRE_LDS, stream>>>(x, wq, wk, wv, w1, w3, w2, wo, ws);
  k_mid<<<512, 512, LDS_BYTES, stream>>>(x, ws, (u16*)d_out);
}
```

```cpp
#include <hip/hip_runtime.h>
#include <hip/hip_bf16.h>
#include <stdint.h>

typedef short bf16x8 __attribute__((ext_vector_type(8)));
typedef short s16x4 __attribute__((ext_vector_type(4)));
typedef float f32x2 __attribute__((ext_vector_type(2)));
typedef float f32x4 __attribute__((ext_vector_type(4)));
typedef float f32x16 __attribute__((ext_vector_type(16)));
typedef unsigned u32x4 __attribute__((ext_vector_type(4)));
typedef unsigned u32x2 __attribute__((ext_vector_type(2)));
typedef __bf16 bf16x2_t __attribute__((ext_vector_type(2)));
typedef unsigned short u16;
#define LAS __attribute__((address_space(3)))
#define GAS __attribute__((address_space(1)))
#define RLX_AGENT __ATOMIC_RELAXED, __HIP_MEMORY_SCOPE_AGENT
#define DI __device__ __forceinline__
#define SBAR() __builtin_amdgcn_sched_barrier(0)
#define PIN(x) asm volatile("" : "+v"(x))

#define GPRIO 1
#define LADDER 1
#define WT 0
#define GS 0
#define GAP()
#define PSLEEP 2
#define TPRIO 3
#define APRIO 2
constexpr int S = 2048, D = 640, H = 32, DH = 20, F = 1536;
constexpr int VR = 22;
constexpr int KV_K2 = 1024, KV_C = 1280, KV_V = 1296, KV_TILE = KV_V + 4 * VR * 16;
constexpr int KV_CHUNK = 8 * KV_TILE;
constexpr int KV_PIECES = (KV_CHUNK + 1023) / 1024, KV_SLOT = KV_PIECES * 1024;
constexpr size_t WS_CNT = 0;
constexpr size_t WS_KMAX = 4096;
constexpr size_t WS_KV = 8192;
constexpr size_t WS_XN = WS_KV + 192 * 1024;
constexpr size_t WS_O = WS_XN + (size_t)S * D * 2;
constexpr size_t WS_H = WS_O + (size_t)S * D * 2;
constexpr size_t WS_WQ = WS_H + (size_t)S * F * 2;
constexpr size_t WS_WO = WS_WQ + (size_t)D * D * 2;
constexpr size_t WS_W13 = WS_WO + (size_t)D * D * 2;
constexpr size_t WS_W2 = WS_W13 + (size_t)2 * D * F * 2;
constexpr float QSCALE = 0.22360679774997896f * 1.4426950408889634f;
constexpr int LDS_BYTES = 81920, SLOT = 20480;

DI unsigned cvtpk(float a, float b) { f32x2 v = {a, b}; bf16x2_t r = __builtin_convertvector(v, bf16x2_t); return __builtin_bit_cast(unsigned, r); }
DI float bflo(unsigned u) { return __uint_as_float(u << 16); }
DI float bfhi(unsigned u) { return __uint_as_float(u & 0xffff0000u); }
DI float rbf(float a) { return bflo(cvtpk(a, a)); }
DI float dot2(unsigned a, unsigned b, float c) { return __builtin_amdgcn_fdot2_f32_bf16(__builtin_bit_cast(bf16x2_t, a), __builtin_bit_cast(bf16x2_t, b), c, false); }
DI s16x4 trread(const LAS char* p) { return __builtin_bit_cast(s16x4, __builtin_amdgcn_ds_read_tr16_b64_v4i16((LAS s16x4*)p)); }
DI bf16x8 cat8(s16x4 a, s16x4 b) { return __builtin_shufflevector(a, b, 0, 1, 2, 3, 4, 5, 6, 7); }
DI void glds16s(const void* sbase, unsigned voff, unsigned lds_base) {
  unsigned sv;
  asm volatile("s_mov_b32 %0, m0\n\ts_mov_b32 m0, %3\n\ts_nop 0\n\tglobal_load_lds_dwordx4 %1, %2\n\ts_mov_b32 m0, %0" : "=&s"(sv) : "v"(voff), "s"(sbase), "s"(lds_base) : "memory");
}
DI void glds16s_sc1(const void* sbase, unsigned voff, unsigned lds_base) {
  unsigned sv;
  asm volatile("s_mov_b32 %0, m0\n\ts_mov_b32 m0, %3\n\ts_nop 0\n\tglobal_load_lds_dwordx4 %1, %2 sc1\n\ts_mov_b32 m0, %0" : "=&s"(sv) : "v"(voff), "s"(sbase), "s"(lds_base) : "memory");
}
#define ST_DECL
#define ST()
#define ST_OUT(a, b)
#define MFMA32(a, b, c) __builtin_amdgcn_mfma_f32_32x32x16_bf16(a, b, c, 0, 0, 0)
#define MFMA32K8(a, b, c) __builtin_amdgcn_mfma_f32_32x32x8bf16_1k(a, b, c, 0, 0, 0)
#define MFMA16(a, b, c) __builtin_amdgcn_mfma_f32_16x16x32_bf16(a, b, c, 0, 0, 0)
#define EXP2(v) __builtin_amdgcn_exp2f(v)
#define RAWBAR() asm volatile("s_waitcnt lgkmcnt(0)\n\ts_barrier" ::: "memory")
DI void touch(const void* p, unsigned& d) { asm volatile("global_load_dword %0, %1, off" : "+v"(d) : "v"(p) : "memory"); }
DI void st16wt(void* p, u32x4 v) {
  *(u32x4*)p = v;
}
template <int N> DI void vmwait() { asm volatile("s_waitcnt vmcnt(%0)" :: "n"(N) : "memory"); }
DI int wslot(int k) { return k ^ ((k >> 1) & 4); }
DI void ln_scalars(float sum, float sq, float& mean_b, float& rden) {
  const float mean = sum * (1.f / D);
  const float var = fmaxf(sq * (1.f / D) - mean * mean, 0.f);
  mean_b = rbf(mean);
  rden = __builtin_amdgcn_rcpf(rbf(__builtin_amdgcn_sqrtf(rbf(rbf(var) + 1e-5f))));
}

constexpr int RT_UNITS = 640 + 160 + 160 + 384;
DI void retile_wave(const u16* __restrict__ wq, const u16* __restrict__ wo, const u16* __restrict__ w1, const u16* __restrict__ w3,
                    const u16* __restrict__ w2, char* __restrict__ ws, char* lds, int u) {
  const int lane = threadIdx.x & 63, w = __builtin_amdgcn_readfirstlane(threadIdx.x >> 6);
  const unsigned ldsw = (unsigned)(uintptr_t)(LAS char*)lds + w * 6144;
  const LAS char* im = (const LAS char*)lds + w * 6144;
  const int h = lane >> 5, q4 = (lane & 15) >> 2, p = lane & 3, blk = (lane >> 4) & 1;
  ST_DECL ST();
  int rr[3], pos[3];
#pragma unroll
  for (int j = 0; j < 3; ++j) { const int ci = j * 64 + lane; rr[j] = ci / 12; pos[j] = ci - rr[j] * 12; }
  if (u < 640) {
    const int cg = u / 40, ks = u - cg * 40;
#pragma unroll
    for (int j = 0; j < 3; ++j) {
      glds16s(w1 + (size_t)ks * 16 * F + cg * 96, (rr[j] * F + pos[j] * 8) * 2, ldsw + j * 1024);
      glds16s(w3 + (size_t)ks * 16 * F + cg * 96, (rr[j] * F + pos[j] * 8) * 2, ldsw + 3072 + j * 1024);
    }
    vmwait<0>();
    const LAS char* a = im + blk * 3072 + (8 * h + q4) * 192 + (((p & 1) << 1) | (p >> 1)) * 8;
    char* dst = ws + WS_W13 + (size_t)u * 6144 + lane * 16;
#pragma unroll
    for (int c = 0; c < 6; ++c) st16wt(dst + c * 1024, __builtin_bit_cast(u32x4, cat8(trread(a + c * 32), trread(a + c * 32 + 768))));
  } else {
    const int v = u - 640, kind = v < 160 ? 0 : v < 320 ? 1 : 2;
    const int i = v - kind * 160, nk = kind == 2 ? 48 : 20, cg = i / nk, ks = i - cg * nk;
    const int odd = cg & 1, c0 = cg * 80 - odd * 16;
    const u16* src = (kind == 0 ? wq : kind == 1 ? wo : w2) + (size_t)ks * 32 * D + c0;
#pragma unroll
    for (int j = 0; j < 3; ++j) {
      const unsigned vo = (rr[j] * D + (c0 + pos[j] * 8 < D ? pos[j] : 0) * 8) * 2;
      glds16s(src, vo, ldsw + j * 1024);
      glds16s(src + 16 * D, vo, ldsw + 3072 + j * 1024);
    }
    vmwait<0>();
    if (kind == 0) {
      const int gI = blk * 4 + p, dcol = gI < 4 ? ((gI & 1) * 8 + (gI >> 1) * 4) : 16, r = lane & 31;
      const LAS char* a = im + (8 * h + q4) * 192 + odd * 32 + dcol * 2;
      char* dst = ws + WS_WQ + (size_t)i * 5120 + (h * 20 + r) * 16;
#pragma unroll
      for (int s = 0; s < 2; ++s)
#pragma unroll
        for (int hd = 0; hd < 4; ++hd) {
          const bf16x8 f = cat8(trread(a + s * 3072 + hd * 40), trread(a + s * 3072 + hd * 40 + 768));
          if (r < 20) st16wt(dst + (s * 4 + hd) * 640, __builtin_bit_cast(u32x4, f));
        }
    } else {
      const LAS char* a = im + ((lane >> 4) * 8 + q4) * 192 + odd * 32 + p * 8;
      char* dst = ws + (kind == 1 ? WS_WO : WS_W2) + (size_t)i * 5120 + lane * 16;
#pragma unroll
      for (int c = 0; c < 5; ++c) st16wt(dst + c * 1024, __builtin_bit_cast(u32x4, cat8(trread(a + c * 32), trread(a + c * 32 + 768))));
    }
  }
}

constexpr int PRE_W = 0, PRE_ST = 25600, PRE_RED = PRE_ST + 512, PRE_LDS = PRE_RED + 8192;
__global__ __launch_bounds__(256) void k_pre(const u16* __restrict__ x, const u16* __restrict__ wq, const u16* __restrict__ wk,
                                              const u16* __restrict__ wv, const u16* __restrict__ w1, const u16* __restrict__ w3,
                                              const u16* __restrict__ w2, const u16* __restrict__ wo, char* __restrict__ ws) {
  extern __shared__ __attribute__((aligned(16))) char lds[];
  const int tid = threadIdx.x, lane = tid & 63, w = __builtin_amdgcn_readfirstlane(tid >> 6);
  if (blockIdx.x >= 256) { retile_wave(wq, wo, w1, w3, w2, ws, lds, (blockIdx.x - 256) * 4 + w); return; }
  const int b = blockIdx.x, t16 = (b & 7) * 16 + (b >> 4), kv = (b >> 3) & 1;
  const int r16 = lane & 15, gq = lane >> 4;
  const unsigned lds0 = (unsigned)(uintptr_t)(LAS char*)lds;
  const int token = t16 * 16 + r16;
  ST_DECL ST();
  u32x4 xf[5];
  const u16* xp = x + (size_t)token * D + w * 160 + gq * 8;
#pragma unroll
  for (int s = 0; s < 5; ++s) asm volatile("global_load_dwordx4 %0, %1, off offset:%2" : "=v"(xf[s]) : "v"(xp), "i"(s * 64) : "memory");
  const u16* wsrc = kv ? wv : wk;
#pragma unroll
  for (int j = 0; j < 7; ++j)
    if (j < 6 || w == 0) glds16s(wsrc + (w + 4 * j) * 512, lane * 16, lds0 + PRE_W + (w + 4 * j) * 1024);
  ST();
  asm volatile("s_waitcnt vmcnt(0)" : "+v"(xf[0]), "+v"(xf[1]), "+v"(xf[2]), "+v"(xf[3]), "+v"(xf[4]) :: "memory");
  float sum = 0.f, sq = 0.f, sum2 = 0.f, sq2 = 0.f;
#pragma unroll
  for (int s = 0; s < 5; ++s)
#pragma unroll
    for (int j = 0; j < 4; j += 2) {
      sum = dot2(xf[s][j], 0x3F803F80u, sum); sq = dot2(xf[s][j], xf[s][j], sq);
      sum2 = dot2(xf[s][j + 1], 0x3F803F80u, sum2); sq2 = dot2(xf[s][j + 1], xf[s][j + 1], sq2);
    }
  sum += sum2; sq += sq2;
  sum += __shfl_xor(sum, 16); sq += __shfl_xor(sq, 16);
  sum += __shfl_xor(sum, 32); sq += __shfl_xor(sq, 32);
  if (b == 0 && tid < 48) ((unsigned*)(ws + WS_CNT))[tid * 16] = 0u;
  LAS f32x2* stp = (LAS f32x2*)((LAS char*)lds + PRE_ST);
  if (gq == 0) stp[w * 16 + r16] = (f32x2){sum, sq};
  RAWBAR();
  { const f32x2 a = stp[r16], c = stp[16 + r16], d = stp[32 + r16], e = stp[48 + r16]; sum = (a[0] + c[0]) + (d[0] + e[0]); sq = (a[1] + c[1]) + (d[1] + e[1]); }
  float mean_b, rden;
  ln_scalars(sum, sq, mean_b, rden);
  ST();
#pragma unroll
  for (int s = 0; s < 5; ++s)
#pragma unroll
    for (int j = 0; j < 4; ++j) {
      const unsigned d = cvtpk(bflo(xf[s][j]) - mean_b, bfhi(xf[s][j]) - mean_b);
      xf[s][j] = cvtpk(bflo(d) * rden, bfhi(d) * rden);
    }
  if ((w >> 1) == kv) {
    u16* xn = (u16*)(ws + WS_XN) + (size_t)token * D + w * 160 + gq * 8;
#pragma unroll
    for (int s = 0; s < 5; ++s) st16wt(xn + s * 32, xf[s]);
  }
  const int q4 = (lane & 15) >> 2, p = lane & 3;
  const LAS char* e0 = (const LAS char*)lds + PRE_W + (w * 160 + gq * 8 + q4) * 40 + p * 8;
  const LAS char* e1 = (const LAS char*)lds + PRE_W + (w * 160 + gq * 8 + q4) * 40 + 32;
  f32x4 acc[2] = {};
  if (kv == 0) {
#pragma unroll
    for (int s = 0; s < 5; ++s) {
      acc[0] = MFMA16(cat8(trread(e0 + s * 1280), trread(e0 + s * 1280 + 160)), __builtin_bit_cast(bf16x8, xf[s]), acc[0]);
      acc[1] = MFMA16(cat8(trread(e1 + s * 1280), trread(e1 + s * 1280 + 160)), __builtin_bit_cast(bf16x8, xf[s]), acc[1]);
    }
  } else {
#pragma unroll
    for (int s = 0; s < 5; ++s) {
      acc[0] = MFMA16(__builtin_bit_cast(bf16x8, xf[s]), cat8(trread(e0 + s * 1280), trread(e0 + s * 1280 + 160)), acc[0]);
      acc[1] = MFMA16(__builtin_bit_cast(bf16x8, xf[s]), cat8(trread(e1 + s * 1280), trread(e1 + s * 1280 + 160)), acc[1]);
    }
  }
  LAS f32x4* red = (LAS f32x4*)((LAS char*)lds + PRE_RED) + lane;
  red[(w * 2) * 64] = acc[0]; red[(w * 2 + 1) * 64] = acc[1];
  RAWBAR();
  ST();
  if (w < 2) {
    const f32x4 a = (red[w * 64] + red[(2 + w) * 64]) + (red[(4 + w) * 64] + red[(6 + w) * 64]);
    const u32x2 v = {cvtpk(a[0], a[1]), cvtpk(a[2], a[3])};
    char* kvp = ws + WS_KV + (t16 >> 1) * KV_TILE;
    if (kv == 0) {
      const int r = token & 31;
      float n2 = dot2(v[0], v[0], dot2(v[1], v[1], 0.f));
      if (w == 0) {
        *(u32x2*)(kvp + (gq >> 1) * 512 + r * 16 + (gq & 1) * 8) = v;
      } else {
        if (gq == 0) *(u32x2*)(kvp + KV_K2 + r * 8) = v;
        else n2 = 0.f;
        if (gq == 1 && r == 0) { const u32x4 c1 = {0x00003F80u, 0u, 0u, 0u}; *(u32x4*)(kvp + KV_C) = c1; }
      }
      n2 += __shfl_xor(n2, 16); n2 += __shfl_xor(n2, 32);
#pragma unroll
      for (int o = 1; o < 16; o <<= 1) n2 = fmaxf(n2, __shfl_xor(n2, o));
      if (lane == 0) ((float*)(ws + WS_KMAX))[w * 128 + t16] = n2;
    } else {
      char* vp = kvp + KV_V + (((t16 & 1) * 2 + (gq & 1)) * VR) * 16 + (gq >> 1) * 8;
      if (w == 0) *(u32x2*)(vp + r16 * 16) = v;
      else if (r16 < 6) {
        const u32x2 one = {0x3F803F80u, 0x3F803F80u}, zer = {0u, 0u};
        *(u32x2*)(vp + (16 + r16) * 16) = r16 < 4 ? v : (r16 == 4 ? one : zer);
      }
    }
  }
  ST(); ST_OUT(ws, 0);
}

template <int NL> DI void glds_lin(const char* src, unsigned lane16, unsigned dst) {
  unsigned sv;
  asm volatile("s_mov_b32 %0, m0\n\ts_mov_b32 m0, %3\n\ts_mov_b32 exec_hi, %4\n\tglobal_load_lds_dwordx4 %1, %2\n\ts_mov_b32 exec_hi, -1\n\ts_mov_b32 m0, %0"
               : "=&s"(sv) : "v"(lane16), "s"(src), "s"(dst), "n"((1u << (NL - 32)) - 1u) : "memory");
}

constexpr int ST64 = 18432;

constexpr int KV_A = LDS_BYTES - KV_SLOT, KV_B = 0, KV_Cs = KV_SLOT, O_STG = 2 * KV_SLOT;
struct FragQ { bf16x8 x[4]; bf16x8 w[4]; };

DI void attn_block(char* __restrict__ ws, char* lds, int b) {
  const int tid = threadIdx.x, lane = tid & 63, w = __builtin_amdgcn_readfirstlane(tid >> 6);
  const int bi = b >> 3;
  const int tt = (b & 7) * 4 + (bi & 3), t0 = tt * 64, hg = bi >> 2;
  const int th = w & 1, hd = w >> 1;
  const int r = lane & 31, h = lane >> 5;
  const unsigned lds0 = (unsigned)(uintptr_t)(LAS char*)lds;
  const char* kvg = ws + WS_KV + w * 1024;
  const unsigned lane16 = lane * 16;
  auto dma1 = [&](int chunk, int slotoff, int j) {
    if (j < 2 || w < KV_PIECES - 16) glds16s(kvg + chunk * KV_CHUNK + j * 8192, lane16, lds0 + slotoff + (w + 8 * j) * 1024);
  };
  auto dma = [&](int chunk, int slotoff) { dma1(chunk, slotoff, 0); dma1(chunk, slotoff, 1); dma1(chunk, slotoff, 2); };
  ST_DECL ST();
  if (APRIO) __builtin_amdgcn_s_setprio(APRIO);
  if (w == 0) glds16s(ws + WS_KMAX, lane16, lds0 + 4 * ST64);
  float km;
  f32x16 qa = {};
  {
    const int srow = w * 8 + (lane >> 3);
    const unsigned as = (srow * D + ((lane & 7) ^ ((srow >> 1) & 7)) * 8) * 2;
    const u16* ab = (const u16*)(ws + WS_XN) + (size_t)t0 * D;
    const char* wsrc = ws + WS_WQ + (size_t)hg * 102400 + w * 1280;
    auto piece = [&](int kt, int j) {
      const int slot = kt & 3;
      if (j == 0) glds16s(ab + kt * 64, as, lds0 + slot * ST64 + w * 1024);
      else glds_lin<40>(wsrc + kt * 10240 + (j - 1) * 640, lane16, lds0 + slot * ST64 + 8192 + w * 1280 + (j - 1) * 640);
    };
    const int xrow = th * 32 + r;
    const LAS char* xb[4];
#pragma unroll
    for (int s = 0; s < 4; ++s) xb[s] = (const LAS char*)lds + xrow * 128 + (((2 * s + h) ^ ((xrow >> 1) & 7)) * 16);
    const LAS char* wb = (const LAS char*)lds + 8192 + hd * 640 + (h * 20 + min(r, 19)) * 16;
    auto rd = [&](FragQ& f, int slot) {
#pragma unroll
      for (int s = 0; s < 4; ++s) { f.x[s] = *(const LAS bf16x8*)(xb[s] + slot * ST64); f.w[s] = *(const LAS bf16x8*)(wb + slot * ST64 + s * 2560); }
    };
    auto mm = [&](const FragQ& f, int dk) {
#pragma unroll
      for (int s = 0; s < 4; ++s) { qa = MFMA32(f.w[s], f.x[s], qa); SBAR(); if (dk >= 0 && s < 3) piece(dk, s); SBAR(); }
    };
#pragma unroll
    for (int i = 0; i < 4; ++i) { piece(i, 0); piece(i, 1); piece(i, 2); }
    FragQ fa, fb;
    vmwait<9>();
    RAWBAR();
    rd(fa, 0);
    {
      const f32x4 km4 = *(const LAS f32x4*)((LAS char*)lds + 4 * ST64 + lane * 16);
      km = fmaxf(fmaxf(km4[0], km4[1]), fmaxf(km4[2], km4[3]));
#pragma unroll
      for (int o = 1; o < 32; o <<= 1) km = fmaxf(km, __shfl_xor(km, o));
      km += __shfl_xor(km, 32);
      PIN(km);
    }
#define IT(kt, FC, FN, VM, DMA) do { vmwait<VM>(); RAWBAR(); rd(FN, ((kt) + 1) & 3); SBAR(); mm(FC, DMA ? (kt) + 4 : -1); SBAR(); } while (0)
    IT(0, fa, fb, 6, true); IT(1, fb, fa, 6, true); IT(2, fa, fb, 6, true); IT(3, fb, fa, 6, true);
    IT(4, fa, fb, 6, true); IT(5, fb, fa, 6, true); IT(6, fa, fb, 6, false);
    vmwait<3>(); RAWBAR(); dma(0, KV_A); rd(fa, 0); SBAR(); mm(fb, -1); SBAR();
    IT(8, fa, fb, 2, false);
    mm(fb, -1);
#undef IT
  }
  PIN(qa); ST();
  const u32x4 q1u = {cvtpk(qa[0] * QSCALE, qa[1] * QSCALE), cvtpk(qa[2] * QSCALE, qa[3] * QSCALE), cvtpk(qa[4] * QSCALE, qa[5] * QSCALE), cvtpk(qa[6] * QSCALE, qa[7] * QSCALE)};
  u32x2 q2u = {cvtpk(qa[8] * QSCALE, qa[9] * QSCALE), cvtpk(qa[10] * QSCALE, qa[11] * QSCALE)};
  float qn = dot2(q1u[0], q1u[0], dot2(q1u[1], q1u[1], dot2(q1u[2], q1u[2], dot2(q1u[3], q1u[3], 0.f))));
  if (h == 0) qn = dot2(q2u[0], q2u[0], dot2(q2u[1], q2u[1], qn));
  { const auto sw = __builtin_amdgcn_permlane32_swap(__float_as_uint(qn), __float_as_uint(qn), false, false); qn = __uint_as_float(sw[0]) + __uint_as_float(sw[1]); }
  const float bound = __builtin_amdgcn_sqrtf(qn * km) * 1.02f + 1e-3f;
  const bool fast = __all(bound <= 48.f);
  if (h == 1) { q2u[0] = fast ? (cvtpk(-bound, 0.f) & 0xffffu) : 0u; q2u[1] = 0u; }
  const bf16x8 q1 = __builtin_bit_cast(bf16x8, q1u);
  const s16x4 q2 = __builtin_bit_cast(s16x4, q2u);
  const int dv = r <= 20 ? r : 21;
  const LAS char* k1b = (const LAS char*)lds + lane * 16;
  const LAS char* k2b = (const LAS char*)lds + (h ? KV_C : KV_K2 + r * 8);
  const LAS char* vb = (const LAS char*)lds + KV_V + (h * VR + dv) * 16;
  f32x16 o = {};
  const f32x16 zero16 = {};
  vmwait<0>();
  RAWBAR();
  dma(1, KV_B);
  ST();
  if (fast) {
    f32x16 sA, sB;
    bf16x8 k1f, vf0, vf1; s16x4 k2f;
    u32x4 pa, pb;
    float e14, e15;
    k1f = *(const LAS bf16x8*)(k1b + KV_A); k2f = *(const LAS s16x4*)(k2b + KV_A);
    sA = MFMA32(k1f, q1, zero16); sA = MFMA32K8(k2f, q2, sA);
    k1f = *(const LAS bf16x8*)(k1b + KV_A + KV_TILE); k2f = *(const LAS s16x4*)(k2b + KV_A + KV_TILE);
    vf0 = *(const LAS bf16x8*)(vb + KV_A);
    vf1 = (bf16x8){0, 0, 0, 0, 0, 0, 0, 0}; pb = (u32x4){0u, 0u, 0u, 0u}; e14 = 0.f; e15 = 0.f;
    int cur = KV_A, nxt = KV_B, fre = KV_Cs;
#define TILE(C, N, i, DM) do { \
      const int oV = (i) * KV_TILE, oK = ((i) + 2) * KV_TILE; \
      N = MFMA32(k1f, q1, zero16); SBAR(); \
      k1f = *(const LAS bf16x8*)((i) + 2 < 8 ? kc1 + oK : kn1 + (oK - 8 * KV_TILE)); SBAR(); \
      C[0] = EXP2(C[0]); C[1] = EXP2(C[1]); SBAR(); pb[3] = cvtpk(e14, e15); PIN(pb); SBAR(); \
      C[2] = EXP2(C[2]); C[3] = EXP2(C[3]); SBAR(); pa[0] = cvtpk(C[0], C[1]); PIN(pa); SBAR(); \
      o = MFMA32(vf1, __builtin_bit_cast(bf16x8, pb), o); SBAR(); \
      vf1 = *(const LAS bf16x8*)(vc + oV + 2 * VR * 16); SBAR(); \
      C[4] = EXP2(C[4]); C[5] = EXP2(C[5]); SBAR(); pa[1] = cvtpk(C[2], C[3]); PIN(pa); SBAR(); \
      C[6] = EXP2(C[6]); C[7] = EXP2(C[7]); SBAR(); pa[2] = cvtpk(C[4], C[5]); PIN(pa); SBAR(); \
      N = MFMA32K8(k2f, q2, N); SBAR(); \
      k2f = *(const LAS s16x4*)((i) + 2 < 8 ? kc2 + oK : kn2 + (oK - 8 * KV_TILE)); SBAR(); \
      C[8] = EXP2(C[8]); C[9] = EXP2(C[9]); SBAR(); pa[3] = cvtpk(C[6], C[7]); PIN(pa); SBAR(); \
      C[10] = EXP2(C[10]); C[11] = EXP2(C[11]); SBAR(); pb[0] = cvtpk(C[8], C[9]); PIN(pb); SBAR(); \
      o = MFMA32(vf0, __builtin_bit_cast(bf16x8, pa), o); SBAR(); vf0 = *(const LAS bf16x8*)((i) + 1 < 8 ? vc + oV + KV_TILE : vb + nxt); SBAR(); DM; SBAR(); \
      C[12] = EXP2(C[12]); C[13] = EXP2(C[13]); SBAR(); pb[1] = cvtpk(C[10], C[11]); PIN(pb); SBAR(); \
      e14 = EXP2(C[14]); e15 = EXP2(C[15]); SBAR(); pb[2] = cvtpk(C[12], C[13]); PIN(pb); PIN(e14); PIN(e15); SBAR(); \
    } while (0)
    for (int c = 0; c < 8; ++c) {
      const LAS char* kc1 = k1b + cur; const LAS char* kc2 = k2b + cur; const LAS char* vc = vb + cur;
      const LAS char* kn1 = k1b + nxt; const LAS char* kn2 = k2b + nxt;
      TILE(sA, sB, 0, (void)0); TILE(sB, sA, 1, (void)0); TILE(sA, sB, 2, (void)0);
      asm volatile("s_waitcnt vmcnt(0)\n\ts_barrier" ::: "memory");
      if (LADDER && w >= 4) __builtin_amdgcn_s_setprio(0);
      TILE(sB, sA, 3, if (c + 2 < 8) dma1(c + 2, fre, 0));
      if (LADDER == 1) __builtin_amdgcn_s_setprio(APRIO);
      TILE(sA, sB, 4, if (c + 2 < 8) dma1(c + 2, fre, 1));
      if (LADDER == 2) __builtin_amdgcn_s_setprio(APRIO);
      TILE(sB, sA, 5, if (c + 2 < 8) dma1(c + 2, fre, 2)); TILE(sA, sB, 6, (void)0); TILE(sB, sA, 7, (void)0);
      const int t = cur; cur = nxt; nxt = fre; fre = t;
    }
    pb[3] = cvtpk(e14, e15);
    o = MFMA32(vf1, __builtin_bit_cast(bf16x8, pb), o);
#undef TILE
  } else {
    float m = -INFINITY;
    int cur = KV_A, nxt = KV_B, fre = KV_Cs;
#pragma unroll 1
    for (int c = 0; c < 8; ++c) {
#pragma unroll 1
      for (int i = 0; i < 8; ++i) {
        if (i == 3) {
          asm volatile("s_waitcnt vmcnt(0)\n\ts_barrier" ::: "memory");
          if (c + 2 < 8) dma(c + 2, fre);
        }
        const int off = cur + i * KV_TILE;
        const bf16x8 k1f = *(const LAS bf16x8*)(k1b + off); const s16x4 k2f = *(const LAS s16x4*)(k2b + off);
        const bf16x8 vf0 = *(const LAS bf16x8*)(vb + off), vf1 = *(const LAS bf16x8*)(vb + off + 2 * VR * 16);
        f32x16 sc = MFMA32(k1f, q1, zero16); sc = MFMA32K8(k2f, q2, sc);
        float mx = sc[0];
#pragma unroll
        for (int j = 1; j < 16; ++j) mx = fmaxf(mx, sc[j]);
        mx = fmaxf(mx, __shfl_xor(mx, 32));
        const float mn = fmaxf(m, mx);
        const float al = EXP2(m - mn);
        m = mn;
#pragma unroll
        for (int j = 0; j < 16; ++j) { o[j] *= al; sc[j] = EXP2(sc[j] - mn); }
        const u32x4 pa = {cvtpk(sc[0], sc[1]), cvtpk(sc[2], sc[3]), cvtpk(sc[4], sc[5]), cvtpk(sc[6], sc[7])};
        const u32x4 pb = {cvtpk(sc[8], sc[9]), cvtpk(sc[10], sc[11]), cvtpk(sc[12], sc[13]), cvtpk(sc[14], sc[15])};
        o = MFMA32(vf0, __builtin_bit_cast(bf16x8, pa), o);
        o = MFMA32(vf1, __builtin_bit_cast(bf16x8, pb), o);
      }
      const int t = cur; cur = nxt; nxt = fre; fre = t;
    }
  }
  PIN(o); ST();
  const float il = 1.f / __shfl(o[8], r + 32);
  LAS char* os = (LAS char*)lds + O_STG + (th * 32 + r) * 176 + hd * 40 + 8 * h;
  *(LAS u32x2*)(os) = (u32x2){cvtpk(o[0] * il, o[1] * il), cvtpk(o[2] * il, o[3] * il)};
  *(LAS u32x2*)(os + 16) = (u32x2){cvtpk(o[4] * il, o[5] * il), cvtpk(o[6] * il, o[7] * il)};
  if (h == 0) *(LAS u32x2*)(os + 32) = (u32x2){cvtpk(o[8] * il, o[9] * il), cvtpk(o[10] * il, o[11] * il)};
  RAWBAR();
  const __amdgpu_buffer_rsrc_t orr = __builtin_amdgcn_make_buffer_rsrc(ws + WS_O, 0, S * D * 2, 0x00020000);
#pragma unroll
  for (int j = 0; j < 2; ++j) {
    const int ci = tid + 512 * j;
    if (ci < 640) {
      const int c = ci >> 6, cc = hg * 10 + c;
      const u32x4 v = *(const LAS u32x4*)((LAS char*)lds + O_STG + lane * 176 + c * 16);
      __builtin_amdgcn_raw_buffer_store_b128(v, orr, ((tt * 20 + (cc >> 2)) * 4 + (lane >> 4)) * 1024 + ((cc & 3) * 16 + (lane & 15)) * 16, 0, 16);
    }
  }
  vmwait<0>();
  RAWBAR();
  if (tid == 0) __hip_atomic_fetch_add((GAS unsigned*)(ws + WS_CNT) + (16 + tt) * 16, 1u, RLX_AGENT);
  ST(); ST_OUT(ws, 1);
}

struct Frag3 { bf16x8 x; bf16x8 w[3]; };
struct Frag5 { bf16x8 x; bf16x8 w[5]; };
DI void wait_count(GAS unsigned* cnt, unsigned want) {
  for (unsigned spins = 0; spins < (1u << 22); ++spins) {
    if ((unsigned)__builtin_amdgcn_readfirstlane(__hip_atomic_load(cnt, RLX_AGENT)) >= want) break;
    __builtin_amdgcn_s_sleep(PSLEEP);
  }
}
DI void ffn_block(const u16* __restrict__ x, char* __restrict__ ws, u16* __restrict__ out, char* lds, int b) {
  const int tid = threadIdx.x, lane = tid & 63, w = __builtin_amdgcn_readfirstlane(tid >> 6);
  const int bi = b >> 3;
  const unsigned lds0 = (unsigned)(uintptr_t)(LAS char*)lds, lane16 = lane * 16;
  ST_DECL ST();
  if (GPRIO) __builtin_amdgcn_s_setprio(GPRIO);
  {
    const int tp = ((b & 7) >> 2) * 8 + (bi & 7), cg = (b & 3) * 4 + (bi >> 3);
    const int srow = w * 16 + (lane >> 2);
    const u16* xsb = (const u16*)(ws + WS_XN) + (size_t)tp * 128 * D;
    const unsigned xs = (srow * D + (((lane & 3) ^ ((srow >> 2) & 3)) * 8)) * 2;
    const char* wsrc = ws + WS_W13 + (size_t)cg * 245760 + w * 1536;
    auto piece = [&](int kt, int slot, int j) {
      if (j == 0) glds16s(xsb + kt * 32, xs, lds0 + slot * SLOT + w * 1024);
      else glds_lin<48>(wsrc + kt * 12288 + (j - 1) * 768, lane16, lds0 + slot * SLOT + 8192 + w * 1536 + (j - 1) * 768);
    };
#pragma unroll
    for (int i = 0; i < 4; ++i) { piece(i, i, 0); piece(i, i, 1); piece(i, i, 2); }
    const int tq = w & 3, hc = w >> 2, r = lane & 31, h = lane >> 5;
    const int xrow = tq * 32 + r;
    const LAS char* xb0 = (const LAS char*)lds + xrow * 64 + ((h ^ ((xrow >> 2) & 3)) * 16);
    const LAS char* xb1 = (const LAS char*)lds + xrow * 64 + (((2 + h) ^ ((xrow >> 2) & 3)) * 16);
    const LAS char* wb = (const LAS char*)lds + 8192 + hc * 3072 + lane * 16;
    f32x16 acc[3] = {};
    auto rd = [&](Frag3& f, int slot, int s) {
      f.x = *(const LAS bf16x8*)((s ? xb1 : xb0) + slot * SLOT);
#pragma unroll
      for (int c = 0; c < 3; ++c) f.w[c] = *(const LAS bf16x8*)(wb + slot * SLOT + s * 6144 + c * 1024);
    };
    auto mm = [&](const Frag3& f, int s, int dk, int dsl) {
#pragma unroll
      for (int c = 0; c < 3; ++c) {
        acc[c] = MFMA32(f.w[c], f.x, acc[c]);
        SBAR(); GAP();
        if (dk >= 0) { if (s == 1 && c == 0) piece(dk, dsl, 0); if (s == 1 && c == 2) piece(dk, dsl, 1); if (s == 0 && c == 0) piece(dk, dsl, 2); }
        SBAR();
      }
    };
    Frag3 f0, f1;
    vmwait<9>();
    RAWBAR();
    rd(f0, 0, 0);
#define IT(kt, sl, VM, PREV, DMA, NEXT) do { \
      rd(f1, sl, 1); SBAR(); mm(f0, 0, PREV ? (kt) + 3 : -1, ((sl) + 3) & 3); SBAR(); \
      if (NEXT) { vmwait<VM>(); RAWBAR(); rd(f0, ((sl) + 1) & 3, 0); } \
      SBAR(); mm(f1, 1, DMA ? (kt) + 4 : -1, sl); SBAR(); } while (0)
    IT(0, 0, 6, false, true, true); IT(1, 1, 6, true, true, true); IT(2, 2, 6, true, true, true); IT(3, 3, 6, true, true, true);
#pragma unroll 1
    for (int kt = 4; kt < 16; kt += 4) {
      IT(kt, 0, 6, true, true, true); IT(kt + 1, 1, 6, true, true, true); IT(kt + 2, 2, 6, true, true, true); IT(kt + 3, 3, 6, true, true, true);
    }
    IT(16, 0, 6, true, false, true); IT(17, 1, 3, false, false, true); IT(18, 2, 0, false, false, true); IT(19, 3, 0, false, false, false);
#undef IT
    PIN(acc[0]); PIN(acc[1]); PIN(acc[2]); ST();
    __builtin_amdgcn_s_setprio(3);
    u32x4 hv[3];
#pragma unroll
    for (int c = 0; c < 3; ++c) {
      float v[8];
#pragma unroll
      for (int j = 0; j < 8; ++j) {
        const float gt = acc[c][j], up = acc[c][j + 8];
        v[j] = gt * __builtin_amdgcn_rcpf(1.f + EXP2(-1.4426950408889634f * gt)) * up;
      }
      hv[c] = (u32x4){cvtpk(v[0], v[1]), cvtpk(v[2], v[3]), cvtpk(v[4], v[5]), cvtpk(v[6], v[7])};
    }
#pragma unroll
    for (int c = 0; c < 3; ++c) *(LAS u32x4*)((LAS char*)lds + xrow * 208 + hc * 96 + c * 32 + h * 16) = hv[c];
    RAWBAR();
    const __amdgpu_buffer_rsrc_t hr = __builtin_amdgcn_make_buffer_rsrc(ws + WS_H, 0, S * F * 2, 0x00020000);
#pragma unroll
    for (int j = 0; j < 3; ++j) {
      const int ci = tid + 512 * j, row = ci / 12, pos = ci - row * 12;
      const u32x4 v = *(const LAS u32x4*)((LAS char*)lds + row * 208 + pos * 16);
      __builtin_amdgcn_raw_buffer_store_b128(v, hr, ((tp * 128 + row) * F + cg * 96 + pos * 8) * 2, 0, 16);
    }
    vmwait<0>();
    RAWBAR();
    if (tid == 0) __hip_atomic_fetch_add((GAS unsigned*)(ws + WS_CNT) + tp * 16, 1u, RLX_AGENT);
  }
  ST();
  const int tt = (b & 7) * 4 + (bi & 3), t0 = tt * 64, cg = bi >> 2, c0 = cg * 80;
  const int ts = w & 3, kh = w >> 2, r16 = lane & 15, gq = lane >> 4, xrow = ts * 16 + r16;
  f32x4 acc[5] = {};
  {
    const int srow = w * 8 + (lane >> 3);
    const unsigned hs = (srow * F + ((lane & 7) ^ ((srow >> 1) & 7)) * 8) * 2;
    const u16* hb = (const u16*)(ws + WS_H) + (size_t)t0 * F;
    const char* wsrc = ws + WS_W2 + (size_t)cg * 245760 + w * 1280;
    auto piece = [&](int kt, int slot, int j) {
      if (j == 0) glds16s_sc1(hb + kt * 64, hs, lds0 + slot * ST64 + w * 1024);
      else glds_lin<40>(wsrc + kt * 10240 + (j - 1) * 640, lane16, lds0 + slot * ST64 + 8192 + w * 1280 + (j - 1) * 640);
    };
#pragma unroll
    for (int i = 0; i < 4; ++i) { piece(i, i, 1); piece(i, i, 2); }
    if (w == 0) {
      wait_count((GAS unsigned*)(ws + WS_CNT) + (tt >> 1) * 16, 16u);
    }
    RAWBAR();
    __builtin_amdgcn_s_setprio(GPRIO);
    ST();
#pragma unroll
    for (int i = 0; i < 4; ++i) piece(i, i, 0);
    const LAS char* xb = (const LAS char*)lds + xrow * 128 + (((kh * 4 + gq) ^ ((xrow >> 1) & 7)) * 16);
    const LAS char* wb = (const LAS char*)lds + 8192 + kh * 5120 + lane * 16;
    auto rd = [&](Frag5& f, int slot) {
      f.x = *(const LAS bf16x8*)(xb + slot * ST64);
#pragma unroll
      for (int c = 0; c < 5; ++c) f.w[c] = *(const LAS bf16x8*)(wb + slot * ST64 + c * 1024);
    };
    auto mm = [&](const Frag5& f, int dk, int dsl) {
#pragma unroll
      for (int c = 0; c < 5; ++c) {
        acc[c] = MFMA16(f.w[c], f.x, acc[c]);
        SBAR(); if (c & 1) GAP(); if (dk >= 0 && !(c & 1)) piece(dk, dsl, c >> 1); SBAR(); }
    };
    Frag5 fa, fb;
    vmwait<3>();
    RAWBAR();
    rd(fa, 0);
#define IT(kt, sl, FC, FN, VM, DMA) do { vmwait<VM>(); RAWBAR(); rd(FN, ((sl) + 1) & 3); SBAR(); mm(FC, DMA ? (kt) + 4 : -1, sl); SBAR(); } while (0)
    IT(0, 0, fa, fb, 2, true); IT(1, 1, fb, fa, 4, true); IT(2, 2, fa, fb, 6, true); IT(3, 3, fb, fa, 6, true);
#pragma unroll 1
    for (int kt = 4; kt < 20; kt += 4) {
      IT(kt, 0, fa, fb, 6, true); IT(kt + 1, 1, fb, fa, 6, true); IT(kt + 2, 2, fa, fb, 6, true); IT(kt + 3, 3, fb, fa, 6, true);
    }
    IT(20, 0, fa, fb, 6, false); IT(21, 1, fb, fa, 3, false); IT(22, 2, fa, fb, 0, false);
    mm(fb, -1, 0);
#undef IT
  }
  PIN(acc[0]); ST();
  RAWBAR();
  const char* wog = ws + WS_WO + (size_t)cg * 102400;
#pragma unroll
  for (int j = 0; j < 10; ++j) glds16s(wog + (j >= 5 ? 10240 : 0) + (j * 8 + w) * 1024, lane16, lds0 + (j * 8 + w) * 1024);
  bf16x8 wr[10];
#pragma unroll
  for (int j = 0; j < 10; ++j) wr[j] = *(const bf16x8*)(wog + kh * 51200 + 40960 + j * 1024 + lane16);
  if (kh == 0) {
#pragma unroll
    for (int c = 0; c < 5; ++c) {
      const u32x2 xv = *(const u32x2*)(x + (size_t)(t0 + xrow) * D + c0 + c * 16 + gq * 4);
      acc[c] += (f32x4){bflo(xv[0]), bfhi(xv[0]), bflo(xv[1]), bfhi(xv[1])};
    }
  }
#pragma unroll
  for (int j = 0; j < 10; ++j) PIN(wr[j]);
#pragma unroll
  for (int c = 0; c < 5; ++c) PIN(acc[c]);
  vmwait<0>();
  __builtin_amdgcn_s_setprio(TPRIO);
  ST();
  RAWBAR();
  const LAS char* wbt = (const LAS char*)lds + kh * 40960 + lane * 16;
  bf16x8 w0[5];
#pragma unroll
  for (int c = 0; c < 5; ++c) { w0[c] = *(const LAS bf16x8*)(wbt + c * 1024); PIN(w0[c]); }
  if (w == 0) wait_count((GAS unsigned*)(ws + WS_CNT) + (16 + tt) * 16, 8u);
  RAWBAR();
  ST();
  {
    u32x4 of[10];
    const char* op = ws + WS_O + ((tt * 20 + kh * 10) * 4 + ts) * 1024;
#pragma unroll
    for (int s = 0; s < 10; ++s) asm volatile("global_load_dwordx4 %0, %1, %2 sc1" : "=v"(of[(s + 8) % 10]) : "v"(lane16), "s"(op + ((s + 8) % 10) * 4096) : "memory");
#define OSTEP(s, N, WC, WN) do { \
      if ((s) + 1 < 8) { _Pragma("unroll") for (int c = 0; c < 5; ++c) WN[c] = *(const LAS bf16x8*)(wbt + ((s) + 1) * 5120 + c * 1024); } \
      SBAR(); asm volatile("s_waitcnt vmcnt(" #N ")" : "+v"(of[s]) :: "memory"); SBAR(); \
      _Pragma("unroll") for (int c = 0; c < 5; ++c) acc[c] = MFMA16(WC[c], __builtin_bit_cast(bf16x8, of[s]), acc[c]); SBAR(); } while (0)
    asm volatile("s_waitcnt vmcnt(9)" : "+v"(of[8]) :: "memory");
#pragma unroll
    for (int c = 0; c < 5; ++c) acc[c] = MFMA16(wr[c], __builtin_bit_cast(bf16x8, of[8]), acc[c]);
    SBAR(); asm volatile("s_waitcnt vmcnt(8)" : "+v"(of[9]) :: "memory"); SBAR();
#pragma unroll
    for (int c = 0; c < 5; ++c) acc[c] = MFMA16(wr[5 + c], __builtin_bit_cast(bf16x8, of[9]), acc[c]);
    SBAR();
    bf16x8 wn[5];
    OSTEP(0, 7, w0, wn); OSTEP(1, 6, wn, w0); OSTEP(2, 5, w0, wn); OSTEP(3, 4, wn, w0);
    OSTEP(4, 3, w0, wn); OSTEP(5, 2, wn, w0); OSTEP(6, 1, w0, wn); OSTEP(7, 0, wn, w0);
#undef OSTEP
  }
  RAWBAR();
#pragma unroll
  for (int c = 0; c < 5; ++c) *(LAS f32x4*)((LAS char*)lds + kh * 64 * 336 + xrow * 336 + c * 64 + gq * 16) = acc[c];
  RAWBAR();
#pragma unroll
  for (int j = 0; j < 2; ++j) {
    const int ci = tid + 512 * j;
    if (ci < 640) {
      const int row = ci / 10, c = ci - row * 10;
      const LAS f32x4* a0 = (const LAS f32x4*)((LAS char*)lds + row * 336 + c * 32);
      const LAS f32x4* a1 = (const LAS f32x4*)((LAS char*)lds + 64 * 336 + row * 336 + c * 32);
      const f32x4 lo = a0[0] + a1[0], hi = a0[1] + a1[1];
      const u32x4 res = {cvtpk(lo[0], lo[1]), cvtpk(lo[2], lo[3]), cvtpk(hi[0], hi[1]), cvtpk(hi[2], hi[3])};
      *(u32x4*)(out + (size_t)(t0 + row) * D + c0 + c * 8) = res;
    }
  }
  ST(); ST_OUT(ws, 2);
}

__global__ __launch_bounds__(512, 4) void k_mid(const u16* __restrict__ x, char* __restrict__ ws, u16* __restrict__ out) {
  extern __shared__ __attribute__((aligned(16))) char lds[];
  if (blockIdx.x < 256) attn_block(ws, lds, blockIdx.x);
  else ffn_block(x, ws, out, lds, blockIdx.x - 256);
}

extern "C" void kernel_launch(void* const* d_in, const int* in_sizes, int n_in, void* d_out, int out_size, void* d_ws, size_t ws_size,
                              hipStream_t stream) {
  const u16* x = (const u16*)d_in[0];
  const u16* wq = (const u16*)d_in[1];
  const u16* wk = (const u16*)d_in[2];
  const u16* wv = (const u16*)d_in[3];
  const u16* wo = (const u16*)d_in[4];
  const u16* w1 = (const u16*)d_in[6];
  const u16* w3 = (const u16*)d_in[7];
  const u16* w2 = (const u16*)d_in[8];
  char* ws = (char*)d_ws;
  (void)hipFuncSetAttribute((const void*)k_mid, hipFuncAttributeMaxDynamicSharedMemorySize, LDS_BYTES);
  k_pre<<<256 + RT_UNITS / 4, 256, PRE_LDS, stream>>>(x, wq, wk, wv, w1, w3, w2, wo, ws);
  k_mid<<<512, 512, LDS_BYTES, stream>>>(x, ws, (u16*)d_out);
}
```

```cpp
#include <hip/hip_runtime.h>
#include <hip/hip_bf16.h>
#include <stdint.h>

typedef short bf16x8 __attribute__((ext_vector_type(8)));
typedef short s16x4 __attribute__((ext_vector_type(4)));
typedef float f32x2 __attribute__((ext_vector_type(2)));
typedef float f32x4 __attribute__((ext_vector_type(4)));
typedef float f32x16 __attribute__((ext_vector_type(16)));
typedef unsigned u32x4 __attribute__((ext_vector_type(4)));
typedef unsigned u32x2 __attribute__((ext_vector_type(2)));
typedef __bf16 bf16x2_t __attribute__((ext_vector_type(2)));
typedef unsigned short u16;
#define LAS __attribute__((address_space(3)))
#define GAS __attribute__((address_space(1)))
#define RLX_AGENT __ATOMIC_RELAXED, __HIP_MEMORY_SCOPE_AGENT
#define DI __device__ __forceinline__
#define SBAR() __builtin_amdgcn_sched_barrier(0)
#define PIN(x) asm volatile("" : "+v"(x))

#define GPRIO 1
#define LADDER 1
#define WT 0
#define QPRIO 0
#define APRIO 2
constexpr int S = 2048, D = 640, H = 32, DH = 20, F = 1536;
constexpr int VR = 22;
constexpr int KV_K2 = 1024, KV_C = 1280, KV_V = 1296, KV_TILE = KV_V + 4 * VR * 16;
constexpr int KV_CHUNK = 8 * KV_TILE;
constexpr int KV_PIECES = (KV_CHUNK + 1023) / 1024, KV_SLOT = KV_PIECES * 1024;
constexpr size_t WS_CNT = 0;
constexpr size_t WS_KMAX = 4096;
constexpr size_t WS_KV = 8192;
constexpr size_t WS_XN = WS_KV + 192 * 1024;
constexpr size_t WS_O = WS_XN + (size_t)S * D * 2;
constexpr size_t WS_H = WS_O + (size_t)S * D * 2;
constexpr size_t WS_Q = WS_H + (size_t)S * F * 2;
constexpr size_t WS_WO = WS_Q + (size_t)S * D * 2;
constexpr size_t WS_W13 = WS_WO + (size_t)D * D * 2;
constexpr size_t WS_W2 = WS_W13 + (size_t)2 * D * F * 2;
constexpr float QSCALE = 0.22360679774997896f * 1.4426950408889634f;
constexpr int LDS_BYTES = 81920, SLOT = 20480;

DI unsigned cvtpk(float a, float b) { f32x2 v = {a, b}; bf16x2_t r = __builtin_convertvector(v, bf16x2_t); return __builtin_bit_cast(unsigned, r); }
DI float bflo(unsigned u) { return __uint_as_float(u << 16); }
DI float bfhi(unsigned u) { return __uint_as_float(u & 0xffff0000u); }
DI float rbf(float a) { return bflo(cvtpk(a, a)); }
DI float dot2(unsigned a, unsigned b, float c) { return __builtin_amdgcn_fdot2_f32_bf16(__builtin_bit_cast(bf16x2_t, a), __builtin_bit_cast(bf16x2_t, b), c, false); }
DI s16x4 trread(const LAS char* p) { return __builtin_bit_cast(s16x4, __builtin_amdgcn_ds_read_tr16_b64_v4i16((LAS s16x4*)p)); }
DI bf16x8 cat8(s16x4 a, s16x4 b) { return __builtin_shufflevector(a, b, 0, 1, 2, 3, 4, 5, 6, 7); }
DI void glds16s(const void* sbase, unsigned voff, unsigned lds_base) {
  unsigned sv;
  asm volatile("s_mov_b32 %0, m0\n\ts_mov_b32 m0, %3\n\ts_nop 0\n\tglobal_load_lds_dwordx4 %1, %2\n\ts_mov_b32 m0, %0" : "=&s"(sv) : "v"(voff), "s"(sbase), "s"(lds_base) : "memory");
}
DI void glds16s_sc1(const void* sbase, unsigned voff, unsigned lds_base) {
  unsigned sv;
  asm volatile("s_mov_b32 %0, m0\n\ts_mov_b32 m0, %3\n\ts_nop 0\n\tglobal_load_lds_dwordx4 %1, %2 sc1\n\ts_mov_b32 m0, %0" : "=&s"(sv) : "v"(voff), "s"(sbase), "s"(lds_base) : "memory");
}
#define ST_DECL
#define ST()
#define ST_OUT(a, b)
#define MFMA32(a, b, c) __builtin_amdgcn_mfma_f32_32x32x16_bf16(a, b, c, 0, 0, 0)
#define MFMA32K8(a, b, c) __builtin_amdgcn_mfma_f32_32x32x8bf16_1k(a, b, c, 0, 0, 0)
#define MFMA16(a, b, c) __builtin_amdgcn_mfma_f32_16x16x32_bf16(a, b, c, 0, 0, 0)
#define EXP2(v) __builtin_amdgcn_exp2f(v)
#define RAWBAR() asm volatile("s_waitcnt lgkmcnt(0)\n\ts_barrier" ::: "memory")
DI void touch(const void* p, unsigned& d) { asm volatile("global_load_dword %0, %1, off" : "+v"(d) : "v"(p) : "memory"); }
DI void st16wt(void* p, u32x4 v) {
  *(u32x4*)p = v;
}
template <int N> DI void vmwait() { asm volatile("s_waitcnt vmcnt(%0)" :: "n"(N) : "memory"); }
DI int wslot(int k) { return k ^ ((k >> 1) & 4); }
DI void ln_scalars(float sum, float sq, float& mean_b, float& rden) {
  const float mean = sum * (1.f / D);
  const float var = fmaxf(sq * (1.f / D) - mean * mean, 0.f);
  mean_b = rbf(mean);
  rden = __builtin_amdgcn_rcpf(rbf(__builtin_amdgcn_sqrtf(rbf(rbf(var) + 1e-5f))));
}

template <int NL> DI void glds_lin(const char* src, unsigned lane16, unsigned dst) {
  unsigned sv;
  asm volatile("s_mov_b32 %0, m0\n\ts_mov_b32 m0, %3\n\ts_mov_b32 exec_hi, %4\n\tglobal_load_lds_dwordx4 %1, %2\n\ts_mov_b32 exec_hi, -1\n\ts_mov_b32 m0, %0"
               : "=&s"(sv) : "v"(lane16), "s"(src), "s"(dst), "n"((1u << (NL - 32)) - 1u) : "memory");
}

constexpr int ST64 = 18432;

DI void retile_issue(const u16* __restrict__ wo, const u16* __restrict__ w1, const u16* __restrict__ w3, const u16* __restrict__ w2,
                     unsigned ldsu, int u, int lane) {
#pragma unroll
  for (int j = 0; j < 3; ++j) {
    const int ci = j * 64 + lane, rr = ci / 12, pos = ci - rr * 12;
    if (u < 640) {
      const int cg = u / 40, ks = u - cg * 40;
      glds16s(w1 + (size_t)ks * 16 * F + cg * 96, (rr * F + pos * 8) * 2, ldsu + j * 1024);
      glds16s(w3 + (size_t)ks * 16 * F + cg * 96, (rr * F + pos * 8) * 2, ldsu + 3072 + j * 1024);
    } else {
      const int i = u < 800 ? u - 640 : u - 800, nk = u < 800 ? 20 : 48, cg = i / nk, ks = i - cg * nk, c0 = cg * 80 - (cg & 1) * 16;
      const u16* src = (u < 800 ? wo : w2) + (size_t)ks * 32 * D + c0;
      const unsigned vo = (rr * D + (c0 + pos * 8 < D ? pos : 0) * 8) * 2;
      glds16s(src, vo, ldsu + j * 1024);
      glds16s(src + 16 * D, vo, ldsu + 3072 + j * 1024);
    }
  }
}
DI void retile_finish(char* __restrict__ ws, const LAS char* im, int u, int lane) {
  const int h = lane >> 5, q4 = (lane & 15) >> 2, p = lane & 3, blk = (lane >> 4) & 1;
  if (u < 640) {
    const LAS char* a = im + blk * 3072 + (8 * h + q4) * 192 + (((p & 1) << 1) | (p >> 1)) * 8;
    char* dst = ws + WS_W13 + (size_t)u * 6144 + lane * 16;
#pragma unroll
    for (int c = 0; c < 6; ++c) *(bf16x8*)(dst + c * 1024) = cat8(trread(a + c * 32), trread(a + c * 32 + 768));
  } else {
    const int i = u < 800 ? u - 640 : u - 800, cg = i / (u < 800 ? 20 : 48);
    const LAS char* a = im + ((lane >> 4) * 8 + q4) * 192 + (cg & 1) * 32 + p * 8;
    char* dst = ws + (u < 800 ? WS_WO : WS_W2) + (size_t)i * 5120 + lane * 16;
#pragma unroll
    for (int c = 0; c < 5; ++c) *(bf16x8*)(dst + c * 1024) = cat8(trread(a + c * 32), trread(a + c * 32 + 768));
  }
}

constexpr int LN_ST = 25600, LN_RED = LN_ST + 512, LN_HALF = LN_RED + 8192;
DI void ln_block(const u16* __restrict__ x, const u16* __restrict__ wk, const u16* __restrict__ wv, char* __restrict__ ws, char* ldsb, int b) {
  const int tid = threadIdx.x, lane = tid & 63, w8 = __builtin_amdgcn_readfirstlane(tid >> 6), w = w8 & 3, kv = w8 >> 2;
  const int t16 = (b & 7) * 16 + (b >> 3);
  const int r16 = lane & 15, gq = lane >> 4;
  LAS char* lds = (LAS char*)ldsb + kv * LN_HALF;
  const unsigned lds0 = (unsigned)(uintptr_t)lds;
  const int token = t16 * 16 + r16;
  ST_DECL ST();
  __builtin_amdgcn_s_setprio(3);
  u32x4 xf[5];
  const u16* xp = x + (size_t)token * D + w * 160 + gq * 8;
#pragma unroll
  for (int s = 0; s < 5; ++s) asm volatile("global_load_dwordx4 %0, %1, off offset:%2" : "=v"(xf[s]) : "v"(xp), "i"(s * 64) : "memory");
  const u16* wsrc = kv ? wv : wk;
#pragma unroll
  for (int j = 0; j < 7; ++j)
    if (j < 6 || w == 0) glds16s(wsrc + (w + 4 * j) * 512, lane * 16, lds0 + (w + 4 * j) * 1024);
  ST();
  asm volatile("s_waitcnt vmcnt(0)" : "+v"(xf[0]), "+v"(xf[1]), "+v"(xf[2]), "+v"(xf[3]), "+v"(xf[4]) :: "memory");
  float sum = 0.f, sq = 0.f, sum2 = 0.f, sq2 = 0.f;
#pragma unroll
  for (int s = 0; s < 5; ++s)
#pragma unroll
    for (int j = 0; j < 4; j += 2) {
      sum = dot2(xf[s][j], 0x3F803F80u, sum); sq = dot2(xf[s][j], xf[s][j], sq);
      sum2 = dot2(xf[s][j + 1], 0x3F803F80u, sum2); sq2 = dot2(xf[s][j + 1], xf[s][j + 1], sq2);
    }
  sum += sum2; sq += sq2;
  sum += __shfl_xor(sum, 16); sq += __shfl_xor(sq, 16);
  sum += __shfl_xor(sum, 32); sq += __shfl_xor(sq, 32);
  if (b == 0 && tid < 48) ((unsigned*)(ws + WS_CNT))[tid * 16] = 0u;
  LAS f32x2* stp = (LAS f32x2*)(lds + LN_ST);
  if (gq == 0) stp[w * 16 + r16] = (f32x2){sum, sq};
  RAWBAR();
  { const f32x2 a = stp[r16], c = stp[16 + r16], d = stp[32 + r16], e = stp[48 + r16]; sum = (a[0] + c[0]) + (d[0] + e[0]); sq = (a[1] + c[1]) + (d[1] + e[1]); }
  float mean_b, rden;
  ln_scalars(sum, sq, mean_b, rden);
  ST();
#pragma unroll
  for (int s = 0; s < 5; ++s)
#pragma unroll
    for (int j = 0; j < 4; ++j) {
      const unsigned d = cvtpk(bflo(xf[s][j]) - mean_b, bfhi(xf[s][j]) - mean_b);
      xf[s][j] = cvtpk(bflo(d) * rden, bfhi(d) * rden);
    }
  if ((w >> 1) == kv) {
    u16* xn = (u16*)(ws + WS_XN) + (size_t)token * D + w * 160 + gq * 8;
#pragma unroll
    for (int s = 0; s < 5; ++s) *(u32x4*)(xn + s * 32) = xf[s];
  }
  const int q4 = (lane & 15) >> 2, p = lane & 3;
  const LAS char* e0 = lds + (w * 160 + gq * 8 + q4) * 40 + p * 8;
  const LAS char* e1 = lds + (w * 160 + gq * 8 + q4) * 40 + 32;
  f32x4 acc[2] = {};
  if (kv == 0) {
#pragma unroll
    for (int s = 0; s < 5; ++s) {
      acc[0] = MFMA16(cat8(trread(e0 + s * 1280), trread(e0 + s * 1280 + 160)), __builtin_bit_cast(bf16x8, xf[s]), acc[0]);
      acc[1] = MFMA16(cat8(trread(e1 + s * 1280), trread(e1 + s * 1280 + 160)), __builtin_bit_cast(bf16x8, xf[s]), acc[1]);
    }
  } else {
#pragma unroll
    for (int s = 0; s < 5; ++s) {
      acc[0] = MFMA16(__builtin_bit_cast(bf16x8, xf[s]), cat8(trread(e0 + s * 1280), trread(e0 + s * 1280 + 160)), acc[0]);
      acc[1] = MFMA16(__builtin_bit_cast(bf16x8, xf[s]), cat8(trread(e1 + s * 1280), trread(e1 + s * 1280 + 160)), acc[1]);
    }
  }
  LAS f32x4* red = (LAS f32x4*)(lds + LN_RED) + lane;
  red[(w * 2) * 64] = acc[0]; red[(w * 2 + 1) * 64] = acc[1];
  RAWBAR();
  ST();
  if (w < 2) {
    const f32x4 a = (red[w * 64] + red[(2 + w) * 64]) + (red[(4 + w) * 64] + red[(6 + w) * 64]);
    const u32x2 v = {cvtpk(a[0], a[1]), cvtpk(a[2], a[3])};
    char* kvp = ws + WS_KV + (t16 >> 1) * KV_TILE;
    if (kv == 0) {
      const int r = token & 31;
      float n2 = dot2(v[0], v[0], dot2(v[1], v[1], 0.f));
      if (w == 0) {
        *(u32x2*)(kvp + (gq >> 1) * 512 + r * 16 + (gq & 1) * 8) = v;
      } else {
        if (gq == 0) *(u32x2*)(kvp + KV_K2 + r * 8) = v;
        else n2 = 0.f;
        if (gq == 1 && r == 0) { const u32x4 c1 = {0x00003F80u, 0u, 0u, 0u}; *(u32x4*)(kvp + KV_C) = c1; }
      }
      n2 += __shfl_xor(n2, 16); n2 += __shfl_xor(n2, 32);
#pragma unroll
      for (int o = 1; o < 16; o <<= 1) n2 = fmaxf(n2, __shfl_xor(n2, o));
      if (lane == 0) ((float*)(ws + WS_KMAX))[w * 128 + t16] = n2;
    } else {
      char* vp = kvp + KV_V + (((t16 & 1) * 2 + (gq & 1)) * VR) * 16 + (gq >> 1) * 8;
      if (w == 0) *(u32x2*)(vp + r16 * 16) = v;
      else if (r16 < 6) {
        const u32x2 one = {0x3F803F80u, 0x3F803F80u}, zer = {0u, 0u};
        *(u32x2*)(vp + (16 + r16) * 16) = r16 < 4 ? v : (r16 == 4 ? one : zer);
      }
    }
  }
  ST(); ST_OUT(ws, 0);
}

struct FragT { bf16x8 x; s16x4 w[10]; };
DI void q_block(const u16* __restrict__ x, const u16* __restrict__ wq, char* __restrict__ ws, char* lds, int b) {
  const int tid = threadIdx.x, lane = tid & 63, w = __builtin_amdgcn_readfirstlane(tid >> 6);
  const int bi = b >> 3, tt = (b & 7) * 4 + (bi & 3), cg = bi >> 2, odd = cg & 1, c0 = cg * 80 - odd * 16;
  const int ts = w & 3, kh = w >> 2, r16 = lane & 15, gq = lane >> 4, q4 = (lane & 15) >> 2, p = lane & 3;
  const unsigned lds0 = (unsigned)(uintptr_t)(LAS char*)lds;
  ST_DECL ST();
  const int srow = w * 8 + (lane >> 3);
  const unsigned as = (srow * D + ((lane & 7) ^ ((srow >> 1) & 7)) * 8) * 2;
  const u16* ab = x + (size_t)tt * 64 * D;
  const int rr = lane / 12, ch = (lane - rr * 12) ^ ((w & 1) << 1);
  const unsigned bs = ((rr & 3) * D + (c0 + ch * 8 < D ? ch : 0) * 8) * 2;
  const u16* bb = wq + (size_t)w * 8 * D + c0;
  auto piece = [&](int kt, int j) {
    const int slot = kt & 3;
    if (j == 0) glds16s(ab + kt * 64, as, lds0 + slot * SLOT + w * 1024);
    else glds_lin<48>((const char*)(bb + (size_t)(kt * 64 + (j - 1) * 4) * D), bs, lds0 + slot * SLOT + 8192 + (2 * w + j - 1) * 768);
  };
  if (QPRIO) __builtin_amdgcn_s_setprio(QPRIO);
  unsigned pf = 0;
#pragma unroll
  for (int i = 0; i < 4; ++i) { piece(i, 0); piece(i, 1); piece(i, 2); }
  const int xrow = ts * 16 + r16;
  const LAS char* xb = (const LAS char*)lds + xrow * 128 + (((kh * 4 + gq) ^ ((xrow >> 1) & 7)) * 16);
  const LAS char* wb[2];
#pragma unroll
  for (int e = 0; e < 2; ++e) wb[e] = (const LAS char*)lds + 8192 + (kh * 32 + gq * 8 + q4) * 192 + (((e + odd) ^ (gq & 1)) * 32) + p * 8;
  const bf16x8 ones = {0x3F80, 0x3F80, 0x3F80, 0x3F80, 0x3F80, 0x3F80, 0x3F80, 0x3F80};
  f32x4 acc[5] = {}, sx = {}, sxx = {}, csa = {}, csb = {};
  auto rd = [&](FragT& f, int slot) {
    f.x = *(const LAS bf16x8*)(xb + slot * SLOT);
#pragma unroll
    for (int c = 0; c < 5; ++c) { f.w[2 * c] = trread(wb[c & 1] + slot * SLOT + (c >> 1) * 64); f.w[2 * c + 1] = trread(wb[c & 1] + slot * SLOT + (c >> 1) * 64 + 768); }
  };
  auto mm = [&](const FragT& f, int dk) {
#pragma unroll
    for (int c = 0; c < 5; ++c) {
      acc[c] = MFMA16(cat8(f.w[2 * c], f.w[2 * c + 1]), f.x, acc[c]);
      SBAR(); if (dk >= 0 && !(c & 1)) piece(dk, c >> 1); SBAR();
    }
    sx = MFMA16(ones, f.x, sx); sxx = MFMA16(f.x, f.x, sxx);
#pragma unroll
    for (int c = 0; c < 4; ++c) if (ts == c) csa = MFMA16(cat8(f.w[2 * c], f.w[2 * c + 1]), ones, csa);
    if (ts == 0) csb = MFMA16(cat8(f.w[8], f.w[9]), ones, csb);
  };
  FragT fa, fb;
  ST();
  vmwait<9>(); PIN(pf);
  RAWBAR();
  ST();
  rd(fa, 0);
#define IT(kt, FC, FN, VM, DMA) do { vmwait<VM>(); RAWBAR(); rd(FN, ((kt) + 1) & 3); SBAR(); mm(FC, DMA ? (kt) + 4 : -1); SBAR(); } while (0)
  IT(0, fa, fb, 6, true); IT(1, fb, fa, 6, true); IT(2, fa, fb, 6, true); ST(); IT(3, fb, fa, 6, true); IT(4, fa, fb, 6, true);
  IT(5, fb, fa, 6, true); ST(); IT(6, fa, fb, 6, false); IT(7, fb, fa, 3, false); IT(8, fa, fb, 0, false);
  mm(fb, -1);
#undef IT
  ST();
  LAS f32x4* red = (LAS f32x4*)((LAS char*)lds + 2 * SLOT) + ts * 5 * 64 + lane;
  LAS f32x2* stp = (LAS f32x2*)((LAS char*)lds + 3 * SLOT);
  LAS f32x4* csp = (LAS f32x4*)((LAS char*)lds + 3 * SLOT + 1024);
  if (kh == 0) { red[3 * 64] = acc[3]; red[4 * 64] = acc[4]; }
  else { red[0] = acc[0]; red[64] = acc[1]; red[128] = acc[2]; }
  if (gq == q4) stp[kh * 64 + xrow] = (f32x2){sx[0], p == 0 ? sxx[0] : p == 1 ? sxx[1] : p == 2 ? sxx[2] : sxx[3]};
  if (r16 == 0) { csp[(kh * 5 + ts) * 4 + gq] = csa; if (ts == 0) csp[(kh * 5 + 4) * 4 + gq] = csb; }
  RAWBAR();
  float mean_b, rden;
  { const f32x2 a = stp[xrow], c = stp[64 + xrow]; ln_scalars(a[0] + c[0], a[1] + c[1], mean_b, rden); }
  rden *= QSCALE;
  u16* qp = (u16*)(ws + WS_Q) + (size_t)(tt * 8 + cg) * 5120 + xrow * 80 + gq * 4;
#pragma unroll
  for (int c = 0; c < 5; ++c)
    if ((c < 3) == (kh == 0)) {
      const f32x4 a = (acc[c] + red[c * 64] - (csp[c * 4 + gq] + csp[(5 + c) * 4 + gq]) * mean_b) * rden;
      *(u32x2*)(qp + c * 16) = (u32x2){cvtpk(a[0], a[1]), cvtpk(a[2], a[3])};
    }
  ST(); ST_OUT(ws, 5);
}

__global__ __launch_bounds__(512, 4) void k_pre(const u16* __restrict__ x, const u16* __restrict__ wq, const u16* __restrict__ wk,
                                                 const u16* __restrict__ wv, const u16* __restrict__ w1, const u16* __restrict__ w3,
                                                 const u16* __restrict__ w2, const u16* __restrict__ wo, char* __restrict__ ws, const u16* __restrict__ out) {
  extern __shared__ __attribute__((aligned(16))) char lds[];
  const int b = blockIdx.x;
  if (b < 128) ln_block(x, wk, wv, ws, lds, b);
  else if (b < 384) q_block(x, wq, ws, lds, b - 128);
  else {
    const int lane = threadIdx.x & 63, w = __builtin_amdgcn_readfirstlane(threadIdx.x >> 6), rb = b - 384;
    const unsigned lds0 = (unsigned)(uintptr_t)(LAS char*)lds;
    const int u1 = rb * 8 + w, u2 = 1024 + rb * 2 + w;
    const bool two = w < 2 && u2 < 1184;
    ST_DECL ST();
    retile_issue(wo, w1, w3, w2, lds0 + w * 6144, u1, lane);
    if (two) retile_issue(wo, w1, w3, w2, lds0 + 49152 + w * 6144, u2, lane);
    vmwait<0>();
    retile_finish(ws, (const LAS char*)lds + w * 6144, u1, lane);
    if (two) retile_finish(ws, (const LAS char*)lds + 49152 + w * 6144, u2, lane);
    ST(); ST_OUT(ws, 6);
  }
}

constexpr int KV_A = LDS_BYTES - KV_SLOT, KV_B = 0, KV_Cs = KV_SLOT, O_STG = 2 * KV_SLOT;

DI void attn_block(char* __restrict__ ws, char* lds, int b) {
  const int tid = threadIdx.x, lane = tid & 63, w = __builtin_amdgcn_readfirstlane(tid >> 6);
  const int bi = b >> 3;
  const int tt = (b & 7) * 4 + (bi & 3), t0 = tt * 64, hg = bi >> 2;
  const int th = w & 1, hd = w >> 1;
  const int r = lane & 31, h = lane >> 5;
  const unsigned lds0 = (unsigned)(uintptr_t)(LAS char*)lds;
  const char* kvg = ws + WS_KV + w * 1024;
  const unsigned lane16 = lane * 16;
  auto dma1 = [&](int chunk, int slotoff, int j) {
    if (j < 2 || w < KV_PIECES - 16) glds16s(kvg + chunk * KV_CHUNK + j * 8192, lane16, lds0 + slotoff + (w + 8 * j) * 1024);
  };
  auto dma = [&](int chunk, int slotoff) { dma1(chunk, slotoff, 0); dma1(chunk, slotoff, 1); dma1(chunk, slotoff, 2); };
  ST_DECL ST();
  if (APRIO) __builtin_amdgcn_s_setprio(APRIO);
  glds_lin<40>(ws + WS_Q + (size_t)(tt * 8 + hg) * 10240 + w * 1280, lane16, lds0 + O_STG + w * 1280);
  glds_lin<40>(ws + WS_Q + (size_t)(tt * 8 + hg) * 10240 + w * 1280 + 640, lane16, lds0 + O_STG + w * 1280 + 640);
  if (w == 0) glds16s(ws + WS_KMAX, lane16, lds0 + O_STG + 10240);
  dma(0, KV_A); dma(1, KV_B);
  vmwait<2>();
  RAWBAR();
  ST();
  u32x4 q1u; u32x2 q2u;
  {
    const LAS char* qs = (const LAS char*)lds + O_STG + (th * 32 + r) * 160 + hd * 40;
    const u32x2 a = *(const LAS u32x2*)(qs + 16 * h), c = *(const LAS u32x2*)(qs + 16 * h + 8);
    q1u = (u32x4){a[0], a[1], c[0], c[1]}; q2u = *(const LAS u32x2*)(qs + 32);
  }
  float km;
  {
    const f32x4 km4 = *(const LAS f32x4*)((LAS char*)lds + O_STG + 10240 + lane * 16);
    int k = __float_as_int(fmaxf(fmaxf(km4[0], km4[1]), fmaxf(km4[2], km4[3])));
    k = max(k, __builtin_amdgcn_update_dpp(0, k, 0x128, 0xf, 0xf, false)); k = max(k, __builtin_amdgcn_update_dpp(0, k, 0x124, 0xf, 0xf, false));
    k = max(k, __builtin_amdgcn_update_dpp(0, k, 0x122, 0xf, 0xf, false)); k = max(k, __builtin_amdgcn_update_dpp(0, k, 0x121, 0xf, 0xf, false));
    km = __int_as_float(max(__builtin_amdgcn_readlane(k, 0), __builtin_amdgcn_readlane(k, 16))) +
         __int_as_float(max(__builtin_amdgcn_readlane(k, 32), __builtin_amdgcn_readlane(k, 48)));
  }
  float qn = dot2(q1u[0], q1u[0], dot2(q1u[1], q1u[1], dot2(q1u[2], q1u[2], dot2(q1u[3], q1u[3], 0.f))));
  if (h == 0) qn = dot2(q2u[0], q2u[0], dot2(q2u[1], q2u[1], qn));
  { const auto sw = __builtin_amdgcn_permlane32_swap(__float_as_uint(qn), __float_as_uint(qn), false, false); qn = __uint_as_float(sw[0]) + __uint_as_float(sw[1]); }
  const float bound = __builtin_amdgcn_sqrtf(qn * km) * 1.02f + 1e-3f;
  const bool fast = __all(bound <= 48.f);
  if (h == 1) { q2u[0] = fast ? (cvtpk(-bound, 0.f) & 0xffffu) : 0u; q2u[1] = 0u; }
  const bf16x8 q1 = __builtin_bit_cast(bf16x8, q1u);
  const s16x4 q2 = __builtin_bit_cast(s16x4, q2u);
  const int dv = r <= 20 ? r : 21;
  const LAS char* k1b = (const LAS char*)lds + lane * 16;
  const LAS char* k2b = (const LAS char*)lds + (h ? KV_C : KV_K2 + r * 8);
  const LAS char* vb = (const LAS char*)lds + KV_V + (h * VR + dv) * 16;
  f32x16 o = {};
  const f32x16 zero16 = {};
  ST();
  if (fast) {
    f32x16 sA, sB;
    bf16x8 k1f, vf0, vf1; s16x4 k2f;
    u32x4 pa, pb;
    float e14, e15;
    k1f = *(const LAS bf16x8*)(k1b + KV_A); k2f = *(const LAS s16x4*)(k2b + KV_A);
    sA = MFMA32(k1f, q1, zero16); sA = MFMA32K8(k2f, q2, sA);
    k1f = *(const LAS bf16x8*)(k1b + KV_A + KV_TILE); k2f = *(const LAS s16x4*)(k2b + KV_A + KV_TILE);
    vf0 = *(const LAS bf16x8*)(vb + KV_A);
    vf1 = (bf16x8){0, 0, 0, 0, 0, 0, 0, 0}; pb = (u32x4){0u, 0u, 0u, 0u}; e14 = 0.f; e15 = 0.f;
    int cur = KV_A, nxt = KV_B, fre = KV_Cs;
#define TILE(C, N, i, DM) do { \
      const int oV = (i) * KV_TILE, oK = ((i) + 2) * KV_TILE; \
      N = MFMA32(k1f, q1, zero16); SBAR(); \
      k1f = *(const LAS bf16x8*)((i) + 2 < 8 ? kc1 + oK : kn1 + (oK - 8 * KV_TILE)); SBAR(); \
      C[0] = EXP2(C[0]); C[1] = EXP2(C[1]); SBAR(); pb[3] = cvtpk(e14, e15); PIN(pb); SBAR(); \
      C[2] = EXP2(C[2]); C[3] = EXP2(C[3]); SBAR(); pa[0] = cvtpk(C[0], C[1]); PIN(pa); SBAR(); \
      o = MFMA32(vf1, __builtin_bit_cast(bf16x8, pb), o); SBAR(); \
      vf1 = *(const LAS bf16x8*)(vc + oV + 2 * VR * 16); SBAR(); \
      C[4] = EXP2(C[4]); C[5] = EXP2(C[5]); SBAR(); pa[1] = cvtpk(C[2], C[3]); PIN(pa); SBAR(); \
      C[6] = EXP2(C[6]); C[7] = EXP2(C[7]); SBAR(); pa[2] = cvtpk(C[4], C[5]); PIN(pa); SBAR(); \
      N = MFMA32K8(k2f, q2, N); SBAR(); \
      k2f = *(const LAS s16x4*)((i) + 2 < 8 ? kc2 + oK : kn2 + (oK - 8 * KV_TILE)); SBAR(); \
      C[8] = EXP2(C[8]); C[9] = EXP2(C[9]); SBAR(); pa[3] = cvtpk(C[6], C[7]); PIN(pa); SBAR(); \
      C[10] = EXP2(C[10]); C[11] = EXP2(C[11]); SBAR(); pb[0] = cvtpk(C[8], C[9]); PIN(pb); SBAR(); \
      o = MFMA32(vf0, __builtin_bit_cast(bf16x8, pa), o); SBAR(); vf0 = *(const LAS bf16x8*)((i) + 1 < 8 ? vc + oV + KV_TILE : vb + nxt); SBAR(); DM; SBAR(); \
      C[12] = EXP2(C[12]); C[13] = EXP2(C[13]); SBAR(); pb[1] = cvtpk(C[10], C[11]); PIN(pb); SBAR(); \
      e14 = EXP2(C[14]); e15 = EXP2(C[15]); SBAR(); pb[2] = cvtpk(C[12], C[13]); PIN(pb); PIN(e14); PIN(e15); SBAR(); \
    } while (0)
    for (int c = 0; c < 8; ++c) {
      const LAS char* kc1 = k1b + cur; const LAS char* kc2 = k2b + cur; const LAS char* vc = vb + cur;
      const LAS char* kn1 = k1b + nxt; const LAS char* kn2 = k2b + nxt;
      TILE(sA, sB, 0, (void)0); TILE(sB, sA, 1, (void)0); TILE(sA, sB, 2, (void)0);
      asm volatile("s_waitcnt vmcnt(0)\n\ts_barrier" ::: "memory");
      if (LADDER && w >= 4) __builtin_amdgcn_s_setprio(0);
      TILE(sB, sA, 3, if (c + 2 < 8) dma1(c + 2, fre, 0));
      if (LADDER == 1) __builtin_amdgcn_s_setprio(APRIO);
      TILE(sA, sB, 4, if (c + 2 < 8) dma1(c + 2, fre, 1));
      if (LADDER == 2) __builtin_amdgcn_s_setprio(APRIO);
      TILE(sB, sA, 5, if (c + 2 < 8) dma1(c + 2, fre, 2)); TILE(sA, sB, 6, (void)0); TILE(sB, sA, 7, (void)0);
      const int t = cur; cur = nxt; nxt = fre; fre = t;
    }
    pb[3] = cvtpk(e14, e15);
    o = MFMA32(vf1, __builtin_bit_cast(bf16x8, pb), o);
#undef TILE
  } else {
    float m = -INFINITY;
    int cur = KV_A, nxt = KV_B, fre = KV_Cs;
#pragma unroll 1
    for (int c = 0; c < 8; ++c) {
#pragma unroll 1
      for (int i = 0; i < 8; ++i) {
        if (i == 3) {
          asm volatile("s_waitcnt vmcnt(0)\n\ts_barrier" ::: "memory");
          if (c + 2 < 8) dma(c + 2, fre);
        }
        const int off = cur + i * KV_TILE;
        const bf16x8 k1f = *(const LAS bf16x8*)(k1b + off); const s16x4 k2f = *(const LAS s16x4*)(k2b + off);
        const bf16x8 vf0 = *(const LAS bf16x8*)(vb + off), vf1 = *(const LAS bf16x8*)(vb + off + 2 * VR * 16);
        f32x16 sc = MFMA32(k1f, q1, zero16); sc = MFMA32K8(k2f, q2, sc);
        float mx = sc[0];
#pragma unroll
        for (int j = 1; j < 16; ++j) mx = fmaxf(mx, sc[j]);
        mx = fmaxf(mx, __shfl_xor(mx, 32));
        const float mn = fmaxf(m, mx);
        const float al = EXP2(m - mn);
        m = mn;
#pragma unroll
        for (int j = 0; j < 16; ++j) { o[j] *= al; sc[j] = EXP2(sc[j] - mn); }
        const u32x4 pa = {cvtpk(sc[0], sc[1]), cvtpk(sc[2], sc[3]), cvtpk(sc[4], sc[5]), cvtpk(sc[6], sc[7])};
        const u32x4 pb = {cvtpk(sc[8], sc[9]), cvtpk(sc[10], sc[11]), cvtpk(sc[12], sc[13]), cvtpk(sc[14], sc[15])};
        o = MFMA32(vf0, __builtin_bit_cast(bf16x8, pa), o);
        o = MFMA32(vf1, __builtin_bit_cast(bf16x8, pb), o);
      }
      const int t = cur; cur = nxt; nxt = fre; fre = t;
    }
  }
  PIN(o); ST();
  const float il = 1.f / __shfl(o[8], r + 32);
  LAS char* os = (LAS char*)lds + O_STG + (th * 32 + r) * 176 + hd * 40 + 8 * h;
  *(LAS u32x2*)(os) = (u32x2){cvtpk(o[0] * il, o[1] * il), cvtpk(o[2] * il, o[3] * il)};
  *(LAS u32x2*)(os + 16) = (u32x2){cvtpk(o[4] * il, o[5] * il), cvtpk(o[6] * il, o[7] * il)};
  if (h == 0) *(LAS u32x2*)(os + 32) = (u32x2){cvtpk(o[8] * il, o[9] * il), cvtpk(o[10] * il, o[11] * il)};
  RAWBAR();
  const __amdgpu_buffer_rsrc_t orr = __builtin_amdgcn_make_buffer_rsrc(ws + WS_O, 0, S * D * 2, 0x00020000);
#pragma unroll
  for (int j = 0; j < 2; ++j) {
    const int ci = tid + 512 * j;
    if (ci < 640) {
      const int c = ci >> 6, cc = hg * 10 + c;
      const u32x4 v = *(const LAS u32x4*)((LAS char*)lds + O_STG + lane * 176 + c * 16);
      __builtin_amdgcn_raw_buffer_store_b128(v, orr, ((tt * 20 + (cc >> 2)) * 4 + (lane >> 4)) * 1024 + ((cc & 3) * 16 + (lane & 15)) * 16, 0, 16);
    }
  }
  vmwait<0>();
  RAWBAR();
  if (tid == 0) __hip_atomic_fetch_add((GAS unsigned*)(ws + WS_CNT) + (16 + tt) * 16, 1u, RLX_AGENT);
  ST(); ST_OUT(ws, 1);
}

struct Frag3 { bf16x8 x; bf16x8 w[3]; };
struct Frag5 { bf16x8 x; bf16x8 w[5]; };
DI void wait_count(GAS unsigned* cnt, unsigned want) {
  for (unsigned spins = 0; spins < (1u << 22); ++spins) {
    if ((unsigned)__builtin_amdgcn_readfirstlane(__hip_atomic_load(cnt, RLX_AGENT)) >= want) break;
    __builtin_amdgcn_s_sleep(2);
  }
}
DI void ffn_block(const u16* __restrict__ x, char* __restrict__ ws, u16* __restrict__ out, char* lds, int b) {
  const int tid = threadIdx.x, lane = tid & 63, w = __builtin_amdgcn_readfirstlane(tid >> 6);
  const int bi = b >> 3;
  const unsigned lds0 = (unsigned)(uintptr_t)(LAS char*)lds, lane16 = lane * 16;
  ST_DECL ST();
  if (GPRIO) __builtin_amdgcn_s_setprio(GPRIO);
  {
    const int tp = ((b & 7) >> 2) * 8 + (bi & 7), cg = (b & 3) * 4 + (bi >> 3);
    const int srow = w * 16 + (lane >> 2);
    const u16* xsb = (const u16*)(ws + WS_XN) + (size_t)tp * 128 * D;
    const unsigned xs = (srow * D + (((lane & 3) ^ ((srow >> 2) & 3)) * 8)) * 2;
    const char* wsrc = ws + WS_W13 + (size_t)cg * 245760 + w * 1536;
    auto piece = [&](int kt, int slot, int j) {
      if (j == 0) glds16s(xsb + kt * 32, xs, lds0 + slot * SLOT + w * 1024);
      else glds_lin<48>(wsrc + kt * 12288 + (j - 1) * 768, lane16, lds0 + slot * SLOT + 8192 + w * 1536 + (j - 1) * 768);
    };
#pragma unroll
    for (int i = 0; i < 4; ++i) { piece(i, i, 0); piece(i, i, 1); piece(i, i, 2); }
    const int tq = w & 3, hc = w >> 2, r = lane & 31, h = lane >> 5;
    const int xrow = tq * 32 + r;
    const LAS char* xb0 = (const LAS char*)lds + xrow * 64 + ((h ^ ((xrow >> 2) & 3)) * 16);
    const LAS char* xb1 = (const LAS char*)lds + xrow * 64 + (((2 + h) ^ ((xrow >> 2) & 3)) * 16);
    const LAS char* wb = (const LAS char*)lds + 8192 + hc * 3072 + lane * 16;
    f32x16 acc[3] = {};
    auto rd = [&](Frag3& f, int slot, int s) {
      f.x = *(const LAS bf16x8*)((s ? xb1 : xb0) + slot * SLOT);
#pragma unroll
      for (int c = 0; c < 3; ++c) f.w[c] = *(const LAS bf16x8*)(wb + slot * SLOT + s * 6144 + c * 1024);
    };
    auto mm = [&](const Frag3& f, int s, int dk, int dsl) {
#pragma unroll
      for (int c = 0; c < 3; ++c) {
        acc[c] = MFMA32(f.w[c], f.x, acc[c]);
        SBAR();
        if (dk >= 0) { if (s == 1 && c == 0) piece(dk, dsl, 0); if (s == 1 && c == 2) piece(dk, dsl, 1); if (s == 0 && c == 0) piece(dk, dsl, 2); }
        SBAR();
      }
    };
    Frag3 f0, f1;
    vmwait<9>();
    RAWBAR();
    rd(f0, 0, 0);
#define IT(kt, sl, VM, PREV, DMA, NEXT) do { \
      rd(f1, sl, 1); SBAR(); mm(f0, 0, PREV ? (kt) + 3 : -1, ((sl) + 3) & 3); SBAR(); \
      if (NEXT) { vmwait<VM>(); RAWBAR(); rd(f0, ((sl) + 1) & 3, 0); } \
      SBAR(); mm(f1, 1, DMA ? (kt) + 4 : -1, sl); SBAR(); } while (0)
    IT(0, 0, 6, false, true, true); IT(1, 1, 6, true, true, true); IT(2, 2, 6, true, true, true); IT(3, 3, 6, true, true, true);
#pragma unroll 1
    for (int kt = 4; kt < 16; kt += 4) {
      IT(kt, 0, 6, true, true, true); IT(kt + 1, 1, 6, true, true, true); IT(kt + 2, 2, 6, true, true, true); IT(kt + 3, 3, 6, true, true, true);
    }
    IT(16, 0, 6, true, false, true); IT(17, 1, 3, false, false, true); IT(18, 2, 0, false, false, true); IT(19, 3, 0, false, false, false);
#undef IT
    PIN(acc[0]); PIN(acc[1]); PIN(acc[2]); ST();
    __builtin_amdgcn_s_setprio(3);
    u32x4 hv[3];
#pragma unroll
    for (int c = 0; c < 3; ++c) {
      float v[8];
#pragma unroll
      for (int j = 0; j < 8; ++j) {
        const float gt = acc[c][j], up = acc[c][j + 8];
        v[j] = gt * __builtin_amdgcn_rcpf(1.f + EXP2(-1.4426950408889634f * gt)) * up;
      }
      hv[c] = (u32x4){cvtpk(v[0], v[1]), cvtpk(v[2], v[3]), cvtpk(v[4], v[5]), cvtpk(v[6], v[7])};
    }
#pragma unroll
    for (int c = 0; c < 3; ++c) *(LAS u32x4*)((LAS char*)lds + xrow * 208 + hc * 96 + c * 32 + h * 16) = hv[c];
    RAWBAR();
    const __amdgpu_buffer_rsrc_t hr = __builtin_amdgcn_make_buffer_rsrc(ws + WS_H, 0, S * F * 2, 0x00020000);
#pragma unroll
    for (int j = 0; j < 3; ++j) {
      const int ci = tid + 512 * j, row = ci / 12, pos = ci - row * 12;
      const u32x4 v = *(const LAS u32x4*)((LAS char*)lds + row * 208 + pos * 16);
      __builtin_amdgcn_raw_buffer_store_b128(v, hr, ((tp * 128 + row) * F + cg * 96 + pos * 8) * 2, 0, 16);
    }
    vmwait<0>();
    RAWBAR();
    if (tid == 0) __hip_atomic_fetch_add((GAS unsigned*)(ws + WS_CNT) + tp * 16, 1u, RLX_AGENT);
  }
  ST();
  const int tt = (b & 7) * 4 + (bi & 3), t0 = tt * 64, cg = bi >> 2, c0 = cg * 80;
  const int ts = w & 3, kh = w >> 2, r16 = lane & 15, gq = lane >> 4, xrow = ts * 16 + r16;
  f32x4 acc[5] = {};
  const char* wog = ws + WS_WO + (size_t)cg * 102400;
  bf16x8 wr[10]; u32x2 xv[5];
  auto wop = [&](int j) { glds16s(wog + (j >= 5 ? 10240 : 0) + (j * 8 + w) * 1024, lane16, lds0 + (j * 8 + w) * 1024); };
  {
    const int srow = w * 8 + (lane >> 3);
    const unsigned hs = (srow * F + ((lane & 7) ^ ((srow >> 1) & 7)) * 8) * 2;
    const u16* hb = (const u16*)(ws + WS_H) + (size_t)t0 * F;
    const char* wsrc = ws + WS_W2 + (size_t)cg * 245760 + w * 1280;
    auto piece = [&](int kt, int slot, int j) {
      if (j == 0) glds16s_sc1(hb + kt * 64, hs, lds0 + slot * ST64 + w * 1024);
      else glds_lin<40>(wsrc + kt * 10240 + (j - 1) * 640, lane16, lds0 + slot * ST64 + 8192 + w * 1280 + (j - 1) * 640);
    };
#pragma unroll
    for (int i = 0; i < 4; ++i) { piece(i, i, 1); piece(i, i, 2); }
    if (w == 0) wait_count((GAS unsigned*)(ws + WS_CNT) + (tt >> 1) * 16, 16u);
    RAWBAR();
    __builtin_amdgcn_s_setprio(GPRIO);
    ST();
#pragma unroll
    for (int i = 0; i < 4; ++i) piece(i, i, 0);
    const LAS char* xb = (const LAS char*)lds + xrow * 128 + (((kh * 4 + gq) ^ ((xrow >> 1) & 7)) * 16);
    const LAS char* wb = (const LAS char*)lds + 8192 + kh * 5120 + lane * 16;
    auto rd = [&](Frag5& f, int slot) {
      f.x = *(const LAS bf16x8*)(xb + slot * ST64);
#pragma unroll
      for (int c = 0; c < 5; ++c) f.w[c] = *(const LAS bf16x8*)(wb + slot * ST64 + c * 1024);
    };
    auto mm = [&](const Frag5& f, int dk, int dsl) {
#pragma unroll
      for (int c = 0; c < 5; ++c) { acc[c] = MFMA16(f.w[c], f.x, acc[c]); SBAR(); if (dk >= 0 && !(c & 1)) piece(dk, dsl, c >> 1); SBAR(); }
    };
    Frag5 fa, fb;
    vmwait<3>();
    RAWBAR();
    rd(fa, 0);
#define IT(kt, sl, FC, FN, VM, DMA) do { vmwait<VM>(); RAWBAR(); rd(FN, ((sl) + 1) & 3); SBAR(); mm(FC, DMA ? (kt) + 4 : -1, sl); SBAR(); } while (0)
    IT(0, 0, fa, fb, 2, true); IT(1, 1, fb, fa, 4, true); IT(2, 2, fa, fb, 6, true); IT(3, 3, fb, fa, 6, true);
#pragma unroll 1
    for (int kt = 4; kt < 20; kt += 4) {
      IT(kt, 0, fa, fb, 6, true); IT(kt + 1, 1, fb, fa, 6, true); IT(kt + 2, 2, fa, fb, 6, true); IT(kt + 3, 3, fb, fa, 6, true);
    }
    IT(20, 0, fa, fb, 6, false); wop(0); wop(1); wop(9);
    IT(21, 1, fb, fa, 6, false); wop(2); wop(3);
    IT(22, 2, fa, fb, 5, false); wop(4); wop(5);
    {
      const char* wrp = wog + kh * 51200 + 40960;
#pragma unroll
      for (int j = 0; j < 10; ++j) asm volatile("global_load_dwordx4 %0, %1, %2" : "=v"(wr[j]) : "v"(lane16), "s"(wrp + j * 1024) : "memory");
      const u16* xp = x + (size_t)(t0 + xrow) * D + c0 + gq * 4;
#pragma unroll
      for (int c = 0; c < 5; ++c) asm volatile("global_load_dwordx2 %0, %1, off offset:%2" : "=v"(xv[c]) : "v"(xp), "i"(c * 32) : "memory");
    }
    SBAR();
    mm(fb, -1, 0);
#undef IT
  }
  PIN(acc[0]); ST();
  RAWBAR();
  wop(6); wop(7); wop(8);
  asm volatile("s_waitcnt vmcnt(3)" : "+v"(wr[0]), "+v"(wr[1]), "+v"(wr[2]), "+v"(wr[3]), "+v"(wr[4]), "+v"(wr[5]), "+v"(wr[6]), "+v"(wr[7]), "+v"(wr[8]), "+v"(wr[9]),
               "+v"(xv[0]), "+v"(xv[1]), "+v"(xv[2]), "+v"(xv[3]), "+v"(xv[4]) :: "memory");
  if (kh == 0) {
#pragma unroll
    for (int c = 0; c < 5; ++c) acc[c] += (f32x4){bflo(xv[c][0]), bfhi(xv[c][0]), bflo(xv[c][1]), bfhi(xv[c][1])};
  }
#pragma unroll
  for (int c = 0; c < 5; ++c) PIN(acc[c]);
  vmwait<0>();
  __builtin_amdgcn_s_setprio(3);
  ST();
  RAWBAR();
  const LAS char* wbt = (const LAS char*)lds + kh * 40960 + lane * 16;
  bf16x8 w0[5];
#pragma unroll
  for (int c = 0; c < 5; ++c) { w0[c] = *(const LAS bf16x8*)(wbt + c * 1024); PIN(w0[c]); }
  if (w == 0) wait_count((GAS unsigned*)(ws + WS_CNT) + (16 + tt) * 16, 8u);
  RAWBAR();
  ST();
  {
    u32x4 of[10];
    const char* op = ws + WS_O + ((tt * 20 + kh * 10) * 4 + ts) * 1024;
#pragma unroll
    for (int s = 0; s < 10; ++s) asm volatile("global_load_dwordx4 %0, %1, %2 sc1" : "=v"(of[(s + 8) % 10]) : "v"(lane16), "s"(op + ((s + 8) % 10) * 4096) : "memory");
#define OSTEP(s, N, WC, WN) do { \
      if ((s) + 1 < 8) { _Pragma("unroll") for (int c = 0; c < 5; ++c) WN[c] = *(const LAS bf16x8*)(wbt + ((s) + 1) * 5120 + c * 1024); } \
      SBAR(); asm volatile("s_waitcnt vmcnt(" #N ")" : "+v"(of[s]) :: "memory"); SBAR(); \
      _Pragma("unroll") for (int c = 0; c < 5; ++c) acc[c] = MFMA16(WC[c], __builtin_bit_cast(bf16x8, of[s]), acc[c]); SBAR(); } while (0)
    asm volatile("s_waitcnt vmcnt(9)" : "+v"(of[8]) :: "memory");
#pragma unroll
    for (int c = 0; c < 5; ++c) acc[c] = MFMA16(wr[c], __builtin_bit_cast(bf16x8, of[8]), acc[c]);
    SBAR(); asm volatile("s_waitcnt vmcnt(8)" : "+v"(of[9]) :: "memory"); SBAR();
#pragma unroll
    for (int c = 0; c < 5; ++c) acc[c] = MFMA16(wr[5 + c], __builtin_bit_cast(bf16x8, of[9]), acc[c]);
    SBAR();
    bf16x8 wn[5];
    OSTEP(0, 7, w0, wn); OSTEP(1, 6, wn, w0); OSTEP(2, 5, w0, wn); OSTEP(3, 4, wn, w0);
    OSTEP(4, 3, w0, wn); OSTEP(5, 2, wn, w0); OSTEP(6, 1, w0, wn); OSTEP(7, 0, wn, w0);
#undef OSTEP
  }
  RAWBAR();
#pragma unroll
  for (int c = 0; c < 5; ++c) *(LAS f32x4*)((LAS char*)lds + kh * 64 * 336 + xrow * 336 + c * 64 + gq * 16) = acc[c];
  RAWBAR();
#pragma unroll
  for (int j = 0; j < 2; ++j) {
    const int ci = tid + 512 * j;
    if (ci < 640) {
      const int row = ci / 10, c = ci - row * 10;
      const LAS f32x4* a0 = (const LAS f32x4*)((LAS char*)lds + row * 336 + c * 32);
      const LAS f32x4* a1 = (const LAS f32x4*)((LAS char*)lds + 64 * 336 + row * 336 + c * 32);
      const f32x4 lo = a0[0] + a1[0], hi = a0[1] + a1[1];
      const u32x4 res = {cvtpk(lo[0], lo[1]), cvtpk(lo[2], lo[3]), cvtpk(hi[0], hi[1]), cvtpk(hi[2], hi[3])};
      *(u32x4*)(out + (size_t)(t0 + row) * D + c0 + c * 8) = res;
    }
  }
  ST(); ST_OUT(ws, 2);
}

__global__ __launch_bounds__(512, 4) void k_mid(const u16* __restrict__ x, char* __restrict__ ws, u16* __restrict__ out) {
  extern __shared__ __attribute__((aligned(16))) char lds[];
  if (blockIdx.x < 256) attn_block(ws, lds, blockIdx.x);
  else ffn_block(x, ws, out, lds, blockIdx.x - 256);
}

extern "C" void kernel_launch(void* const* d_in, const int* in_sizes, int n_in, void* d_out, int out_size, void* d_ws, size_t ws_size,
                              hipStream_t stream) {
  const u16* x = (const u16*)d_in[0];
  const u16* wq = (const u16*)d_in[1];
  const u16* wk = (const u16*)d_in[2];
  const u16* wv = (const u16*)d_in[3];
  const u16* wo = (const u16*)d_in[4];
  const u16* w1 = (const u16*)d_in[6];
  const u16* w3 = (const u16*)d_in[7];
  const u16* w2 = (const u16*)d_in[8];
  char* ws = (char*)d_ws;
  (void)hipFuncSetAttribute((const void*)k_mid, hipFuncAttributeMaxDynamicSharedMemorySize, LDS_BYTES);
  (void)hipFuncSetAttribute((const void*)k_pre, hipFuncAttributeMaxDynamicSharedMemorySize, LDS_BYTES);
  k_pre<<<512, 512, LDS_BYTES, stream>>>(x, wq, wk, wv, w1, w3, w2, wo, ws, (const u16*)d_out);
  k_mid<<<512, 512, LDS_BYTES, stream>>>(x, ws, (u16*)d_out);
}
```

```cpp
#include <hip/hip_runtime.h>
#include <hip/hip_bf16.h>
#include <stdint.h>

typedef short bf16x8 __attribute__((ext_vector_type(8)));
typedef short s16x4 __attribute__((ext_vector_type(4)));
typedef float f32x2 __attribute__((ext_vector_type(2)));
typedef float f32x4 __attribute__((ext_vector_type(4)));
typedef float f32x16 __attribute__((ext_vector_type(16)));
typedef unsigned u32x4 __attribute__((ext_vector_type(4)));
typedef unsigned u32x2 __attribute__((ext_vector_type(2)));
typedef __bf16 bf16x2_t __attribute__((ext_vector_type(2)));
typedef unsigned short u16;
#define LAS __attribute__((address_space(3)))
#define GAS __attribute__((address_space(1)))
#define RLX_AGENT __ATOMIC_RELAXED, __HIP_MEMORY_SCOPE_AGENT
#define DI __device__ __forceinline__
#define SBAR() __builtin_amdgcn_sched_barrier(0)
#define PIN(x) asm volatile("" : "+v"(x))

#define GPRIO 1
#define LADDER 1
#define WT 0
#define QPRIO 0
#define P2PRIO GPRIO
#define G1LATE 99
#define APRIO 2
constexpr int S = 2048, D = 640, H = 32, DH = 20, F = 1536;
constexpr int VR = 22;
constexpr int KV_K2 = 1024, KV_C = 1280, KV_V = 1296, KV_TILE = KV_V + 4 * VR * 16;
constexpr int KV_CHUNK = 8 * KV_TILE;
constexpr int KV_PIECES = (KV_CHUNK + 1023) / 1024, KV_SLOT = KV_PIECES * 1024;
constexpr size_t WS_CNT = 0;
constexpr size_t WS_KMAX = 4096;
constexpr size_t WS_KV = 8192;
constexpr size_t WS_XN = WS_KV + 192 * 1024;
constexpr size_t WS_O = WS_XN + (size_t)S * D * 2;
constexpr size_t WS_H = WS_O + (size_t)S * D * 2;
constexpr size_t WS_Q = WS_H + (size_t)S * F * 2;
constexpr size_t WS_WO = WS_Q + (size_t)S * D * 2;
constexpr size_t WS_W13 = WS_WO + (size_t)D * D * 2;
constexpr size_t WS_W2 = WS_W13 + (size_t)2 * D * F * 2;
constexpr float QSCALE = 0.22360679774997896f * 1.4426950408889634f;
constexpr int LDS_BYTES = 81920, SLOT = 20480;

DI unsigned cvtpk(float a, float b) { f32x2 v = {a, b}; bf16x2_t r = __builtin_convertvector(v, bf16x2_t); return __builtin_bit_cast(unsigned, r); }
DI float bflo(unsigned u) { return __uint_as_float(u << 16); }
DI float bfhi(unsigned u) { return __uint_as_float(u & 0xffff0000u); }
DI float rbf(float a) { return bflo(cvtpk(a, a)); }
DI float dot2(unsigned a, unsigned b, float c) { return __builtin_amdgcn_fdot2_f32_bf16(__builtin_bit_cast(bf16x2_t, a), __builtin_bit_cast(bf16x2_t, b), c, false); }
DI s16x4 trread(const LAS char* p) { return __builtin_bit_cast(s16x4, __builtin_amdgcn_ds_read_tr16_b64_v4i16((LAS s16x4*)p)); }
DI bf16x8 cat8(s16x4 a, s16x4 b) { return __builtin_shufflevector(a, b, 0, 1, 2, 3, 4, 5, 6, 7); }
DI void glds16s(const void* sbase, unsigned voff, unsigned lds_base) {
  unsigned sv;
  asm volatile("s_mov_b32 %0, m0\n\ts_mov_b32 m0, %3\n\ts_nop 0\n\tglobal_load_lds_dwordx4 %1, %2\n\ts_mov_b32 m0, %0" : "=&s"(sv) : "v"(voff), "s"(sbase), "s"(lds_base) : "memory");
}
DI void glds16s_sc1(const void* sbase, unsigned voff, unsigned lds_base) {
  unsigned sv;
  asm volatile("s_mov_b32 %0, m0\n\ts_mov_b32 m0, %3\n\ts_nop 0\n\tglobal_load_lds_dwordx4 %1, %2 sc1\n\ts_mov_b32 m0, %0" : "=&s"(sv) : "v"(voff), "s"(sbase), "s"(lds_base) : "memory");
}
#define ST_DECL
#define ST()
#define ST_OUT(a, b)
#define MFMA32(a, b, c) __builtin_amdgcn_mfma_f32_32x32x16_bf16(a, b, c, 0, 0, 0)
#define MFMA32K8(a, b, c) __builtin_amdgcn_mfma_f32_32x32x8bf16_1k(a, b, c, 0, 0, 0)
#define MFMA16(a, b, c) __builtin_amdgcn_mfma_f32_16x16x32_bf16(a, b, c, 0, 0, 0)
#define EXP2(v) __builtin_amdgcn_exp2f(v)
#define RAWBAR() asm volatile("s_waitcnt lgkmcnt(0)\n\ts_barrier" ::: "memory")
DI void touch(const void* p, unsigned& d) { asm volatile("global_load_dword %0, %1, off" : "+v"(d) : "v"(p) : "memory"); }
DI void st16wt(void* p, u32x4 v) {
  *(u32x4*)p = v;
}
template <int N> DI void vmwait() { asm volatile("s_waitcnt vmcnt(%0)" :: "n"(N) : "memory"); }
DI int wslot(int k) { return k ^ ((k >> 1) & 4); }
DI void ln_scalars(float sum, float sq, float& mean_b, float& rden) {
  const float mean = sum * (1.f / D);
  const float var = fmaxf(sq * (1.f / D) - mean * mean, 0.f);
  mean_b = rbf(mean);
  rden = __builtin_amdgcn_rcpf(rbf(__builtin_amdgcn_sqrtf(rbf(rbf(var) + 1e-5f))));
}

template <int NL> DI void glds_lin(const char* src, unsigned lane16, unsigned dst) {
  unsigned sv;
  asm volatile("s_mov_b32 %0, m0\n\ts_mov_b32 m0, %3\n\ts_mov_b32 exec_hi, %4\n\tglobal_load_lds_dwordx4 %1, %2\n\ts_mov_b32 exec_hi, -1\n\ts_mov_b32 m0, %0"
               : "=&s"(sv) : "v"(lane16), "s"(src), "s"(dst), "n"((1u << (NL - 32)) - 1u) : "memory");
}

constexpr int ST64 = 18432;

DI void retile_issue(const u16* __restrict__ wo, const u16* __restrict__ w1, const u16* __restrict__ w3, const u16* __restrict__ w2,
                     unsigned ldsu, int u, int lane) {
#pragma unroll
  for (int j = 0; j < 3; ++j) {
    const int ci = j * 64 + lane, rr = ci / 12, pos = ci - rr * 12;
    if (u < 640) {
      const int cg = u / 40, ks = u - cg * 40;
      glds16s(w1 + (size_t)ks * 16 * F + cg * 96, (rr * F + pos * 8) * 2, ldsu + j * 1024);
      glds16s(w3 + (size_t)ks * 16 * F + cg * 96, (rr * F + pos * 8) * 2, ldsu + 3072 + j * 1024);
    } else {
      const int i = u < 800 ? u - 640 : u - 800, nk = u < 800 ? 20 : 48, cg = i / nk, ks = i - cg * nk, c0 = cg * 80 - (cg & 1) * 16;
      const u16* src = (u < 800 ? wo : w2) + (size_t)ks * 32 * D + c0;
      const unsigned vo = (rr * D + (c0 + pos * 8 < D ? pos : 0) * 8) * 2;
      glds16s(src, vo, ldsu + j * 1024);
      glds16s(src + 16 * D, vo, ldsu + 3072 + j * 1024);
    }
  }
}
DI void retile_finish(char* __restrict__ ws, const LAS char* im, int u, int lane) {
  const int h = lane >> 5, q4 = (lane & 15) >> 2, p = lane & 3, blk = (lane >> 4) & 1;
  if (u < 640) {
    const LAS char* a = im + blk * 3072 + (8 * h + q4) * 192 + (((p & 1) << 1) | (p >> 1)) * 8;
    char* dst = ws + WS_W13 + (size_t)u * 6144 + lane * 16;
#pragma unroll
    for (int c = 0; c < 6; ++c) *(bf16x8*)(dst + c * 1024) = cat8(trread(a + c * 32), trread(a + c * 32 + 768));
  } else {
    const int i = u < 800 ? u - 640 : u - 800, cg = i / (u < 800 ? 20 : 48);
    const LAS char* a = im + ((lane >> 4) * 8 + q4) * 192 + (cg & 1) * 32 + p * 8;
    char* dst = ws + (u < 800 ? WS_WO : WS_W2) + (size_t)i * 5120 + lane * 16;
#pragma unroll
    for (int c = 0; c < 5; ++c) *(bf16x8*)(dst + c * 1024) = cat8(trread(a + c * 32), trread(a + c * 32 + 768));
  }
}

constexpr int LN_ST = 25600, LN_RED = LN_ST + 512, LN_HALF = LN_RED + 8192;
DI void ln_block(const u16* __restrict__ x, const u16* __restrict__ wk, const u16* __restrict__ wv, char* __restrict__ ws, char* ldsb, int b) {
  const int tid = threadIdx.x, lane = tid & 63, w8 = __builtin_amdgcn_readfirstlane(tid >> 6), w = w8 & 3, kv = w8 >> 2;
  const int t16 = (b & 7) * 16 + (b >> 3);
  const int r16 = lane & 15, gq = lane >> 4;
  LAS char* lds = (LAS char*)ldsb + kv * LN_HALF;
  const unsigned lds0 = (unsigned)(uintptr_t)lds;
  const int token = t16 * 16 + r16;
  ST_DECL ST();
  __builtin_amdgcn_s_setprio(3);
  u32x4 xf[5];
  const u16* xp = x + (size_t)token * D + w * 160 + gq * 8;
#pragma unroll
  for (int s = 0; s < 5; ++s) asm volatile("global_load_dwordx4 %0, %1, off offset:%2" : "=v"(xf[s]) : "v"(xp), "i"(s * 64) : "memory");
  const u16* wsrc = kv ? wv : wk;
#pragma unroll
  for (int j = 0; j < 7; ++j)
    if (j < 6 || w == 0) glds16s(wsrc + (w + 4 * j) * 512, lane * 16, lds0 + (w + 4 * j) * 1024);
  ST();
  asm volatile("s_waitcnt vmcnt(0)" : "+v"(xf[0]), "+v"(xf[1]), "+v"(xf[2]), "+v"(xf[3]), "+v"(xf[4]) :: "memory");
  float sum = 0.f, sq = 0.f, sum2 = 0.f, sq2 = 0.f;
#pragma unroll
  for (int s = 0; s < 5; ++s)
#pragma unroll
    for (int j = 0; j < 4; j += 2) {
      sum = dot2(xf[s][j], 0x3F803F80u, sum); sq = dot2(xf[s][j], xf[s][j], sq);
      sum2 = dot2(xf[s][j + 1], 0x3F803F80u, sum2); sq2 = dot2(xf[s][j + 1], xf[s][j + 1], sq2);
    }
  sum += sum2; sq += sq2;
  sum += __shfl_xor(sum, 16); sq += __shfl_xor(sq, 16);
  sum += __shfl_xor(sum, 32); sq += __shfl_xor(sq, 32);
  if (b == 0 && tid < 48) ((unsigned*)(ws + WS_CNT))[tid * 16] = 0u;
  LAS f32x2* stp = (LAS f32x2*)(lds + LN_ST);
  if (gq == 0) stp[w * 16 + r16] = (f32x2){sum, sq};
  RAWBAR();
  { const f32x2 a = stp[r16], c = stp[16 + r16], d = stp[32 + r16], e = stp[48 + r16]; sum = (a[0] + c[0]) + (d[0] + e[0]); sq = (a[1] + c[1]) + (d[1] + e[1]); }
  float mean_b, rden;
  ln_scalars(sum, sq, mean_b, rden);
  ST();
#pragma unroll
  for (int s = 0; s < 5; ++s)
#pragma unroll
    for (int j = 0; j < 4; ++j) {
      const unsigned d = cvtpk(bflo(xf[s][j]) - mean_b, bfhi(xf[s][j]) - mean_b);
      xf[s][j] = cvtpk(bflo(d) * rden, bfhi(d) * rden);
    }
  if ((w >> 1) == kv) {
    u16* xn = (u16*)(ws + WS_XN) + (size_t)token * D + w * 160 + gq * 8;
#pragma unroll
    for (int s = 0; s < 5; ++s) *(u32x4*)(xn + s * 32) = xf[s];
  }
  const int q4 = (lane & 15) >> 2, p = lane & 3;
  const LAS char* e0 = lds + (w * 160 + gq * 8 + q4) * 40 + p * 8;
  const LAS char* e1 = lds + (w * 160 + gq * 8 + q4) * 40 + 32;
  f32x4 acc[2] = {};
  if (kv == 0) {
#pragma unroll
    for (int s = 0; s < 5; ++s) {
      acc[0] = MFMA16(cat8(trread(e0 + s * 1280), trread(e0 + s * 1280 + 160)), __builtin_bit_cast(bf16x8, xf[s]), acc[0]);
      acc[1] = MFMA16(cat8(trread(e1 + s * 1280), trread(e1 + s * 1280 + 160)), __builtin_bit_cast(bf16x8, xf[s]), acc[1]);
    }
  } else {
#pragma unroll
    for (int s = 0; s < 5; ++s) {
      acc[0] = MFMA16(__builtin_bit_cast(bf16x8, xf[s]), cat8(trread(e0 + s * 1280), trread(e0 + s * 1280 + 160)), acc[0]);
      acc[1] = MFMA16(__builtin_bit_cast(bf16x8, xf[s]), cat8(trread(e1 + s * 1280), trread(e1 + s * 1280 + 160)), acc[1]);
    }
  }
  LAS f32x4* red = (LAS f32x4*)(lds + LN_RED) + lane;
  red[(w * 2) * 64] = acc[0]; red[(w * 2 + 1) * 64] = acc[1];
  RAWBAR();
  ST();
  if (w < 2) {
    const f32x4 a = (red[w * 64] + red[(2 + w) * 64]) + (red[(4 + w) * 64] + red[(6 + w) * 64]);
    const u32x2 v = {cvtpk(a[0], a[1]), cvtpk(a[2], a[3])};
    char* kvp = ws + WS_KV + (t16 >> 1) * KV_TILE;
    if (kv == 0) {
      const int r = token & 31;
      float n2 = dot2(v[0], v[0], dot2(v[1], v[1], 0.f));
      if (w == 0) {
        *(u32x2*)(kvp + (gq >> 1) * 512 + r * 16 + (gq & 1) * 8) = v;
      } else {
        if (gq == 0) *(u32x2*)(kvp + KV_K2 + r * 8) = v;
        else n2 = 0.f;
        if (gq == 1 && r == 0) { const u32x4 c1 = {0x00003F80u, 0u, 0u, 0u}; *(u32x4*)(kvp + KV_C) = c1; }
      }
      n2 += __shfl_xor(n2, 16); n2 += __shfl_xor(n2, 32);
#pragma unroll
      for (int o = 1; o < 16; o <<= 1) n2 = fmaxf(n2, __shfl_xor(n2, o));
      if (lane == 0) ((float*)(ws + WS_KMAX))[w * 128 + t16] = n2;
    } else {
      char* vp = kvp + KV_V + (((t16 & 1) * 2 + (gq & 1)) * VR) * 16 + (gq >> 1) * 8;
      if (w == 0) *(u32x2*)(vp + r16 * 16) = v;
      else if (r16 < 6) {
        const u32x2 one = {0x3F803F80u, 0x3F803F80u}, zer = {0u, 0u};
        *(u32x2*)(vp + (16 + r16) * 16) = r16 < 4 ? v : (r16 == 4 ? one : zer);
      }
    }
  }
  ST(); ST_OUT(ws, 0);
}

struct FragT { bf16x8 x; s16x4 w[10]; };
DI void q_block(const u16* __restrict__ x, const u16* __restrict__ wq, char* __restrict__ ws, char* lds, int b) {
  const int tid = threadIdx.x, lane = tid & 63, w = __builtin_amdgcn_readfirstlane(tid >> 6);
  const int bi = b >> 3, tt = (b & 7) * 4 + (bi & 3), cg = bi >> 2, odd = cg & 1, c0 = cg * 80 - odd * 16;
  const int ts = w & 3, kh = w >> 2, r16 = lane & 15, gq = lane >> 4, q4 = (lane & 15) >> 2, p = lane & 3;
  const unsigned lds0 = (unsigned)(uintptr_t)(LAS char*)lds;
  ST_DECL ST();
  const int srow = w * 8 + (lane >> 3);
  const unsigned as = (srow * D + ((lane & 7) ^ ((srow >> 1) & 7)) * 8) * 2;
  const u16* ab = x + (size_t)tt * 64 * D;
  const int rr = lane / 12, ch = (lane - rr * 12) ^ ((w & 1) << 1);
  const unsigned bs = ((rr & 3) * D + (c0 + ch * 8 < D ? ch : 0) * 8) * 2;
  const u16* bb = wq + (size_t)w * 8 * D + c0;
  auto piece = [&](int kt, int j) {
    const int slot = kt & 3;
    if (j == 0) glds16s(ab + kt * 64, as, lds0 + slot * SLOT + w * 1024);
    else glds_lin<48>((const char*)(bb + (size_t)(kt * 64 + (j - 1) * 4) * D), bs, lds0 + slot * SLOT + 8192 + (2 * w + j - 1) * 768);
  };
  if (QPRIO) __builtin_amdgcn_s_setprio(QPRIO);
  unsigned pf = 0;
#pragma unroll
  for (int i = 0; i < 4; ++i) { piece(i, 0); piece(i, 1); piece(i, 2); }
  const int xrow = ts * 16 + r16;
  const LAS char* xb = (const LAS char*)lds + xrow * 128 + (((kh * 4 + gq) ^ ((xrow >> 1) & 7)) * 16);
  const LAS char* wb[2];
#pragma unroll
  for (int e = 0; e < 2; ++e) wb[e] = (const LAS char*)lds + 8192 + (kh * 32 + gq * 8 + q4) * 192 + (((e + odd) ^ (gq & 1)) * 32) + p * 8;
  const bf16x8 ones = {0x3F80, 0x3F80, 0x3F80, 0x3F80, 0x3F80, 0x3F80, 0x3F80, 0x3F80};
  f32x4 acc[5] = {}, sx = {}, sxx = {}, csa = {}, csb = {};
  auto rd = [&](FragT& f, int slot) {
    f.x = *(const LAS bf16x8*)(xb + slot * SLOT);
#pragma unroll
    for (int c = 0; c < 5; ++c) { f.w[2 * c] = trread(wb[c & 1] + slot * SLOT + (c >> 1) * 64); f.w[2 * c + 1] = trread(wb[c & 1] + slot * SLOT + (c >> 1) * 64 + 768); }
  };
  auto mm = [&](const FragT& f, int dk) {
#pragma unroll
    for (int c = 0; c < 5; ++c) {
      acc[c] = MFMA16(cat8(f.w[2 * c], f.w[2 * c + 1]), f.x, acc[c]);
      SBAR(); if (dk >= 0 && !(c & 1)) piece(dk, c >> 1); SBAR();
    }
    sx = MFMA16(ones, f.x, sx); sxx = MFMA16(f.x, f.x, sxx);
#pragma unroll
    for (int c = 0; c < 4; ++c) if (ts == c) csa = MFMA16(cat8(f.w[2 * c], f.w[2 * c + 1]), ones, csa);
    if (ts == 0) csb = MFMA16(cat8(f.w[8], f.w[9]), ones, csb);
  };
  FragT fa, fb;
  ST();
  vmwait<9>(); PIN(pf);
  RAWBAR();
  ST();
  rd(fa, 0);
#define IT(kt, FC, FN, VM, DMA) do { vmwait<VM>(); RAWBAR(); rd(FN, ((kt) + 1) & 3); SBAR(); mm(FC, DMA ? (kt) + 4 : -1); SBAR(); } while (0)
  IT(0, fa, fb, 6, true); IT(1, fb, fa, 6, true); IT(2, fa, fb, 6, true); ST(); IT(3, fb, fa, 6, true); IT(4, fa, fb, 6, true);
  IT(5, fb, fa, 6, true); ST(); IT(6, fa, fb, 6, false); IT(7, fb, fa, 3, false); IT(8, fa, fb, 0, false);
  mm(fb, -1);
#undef IT
  ST();
  LAS f32x4* red = (LAS f32x4*)((LAS char*)lds + 2 * SLOT) + ts * 5 * 64 + lane;
  LAS f32x2* stp = (LAS f32x2*)((LAS char*)lds + 3 * SLOT);
  LAS f32x4* csp = (LAS f32x4*)((LAS char*)lds + 3 * SLOT + 1024);
  if (kh == 0) { red[3 * 64] = acc[3]; red[4 * 64] = acc[4]; }
  else { red[0] = acc[0]; red[64] = acc[1]; red[128] = acc[2]; }
  if (gq == q4) stp[kh * 64 + xrow] = (f32x2){sx[0], p == 0 ? sxx[0] : p == 1 ? sxx[1] : p == 2 ? sxx[2] : sxx[3]};
  if (r16 == 0) { csp[(kh * 5 + ts) * 4 + gq] = csa; if (ts == 0) csp[(kh * 5 + 4) * 4 + gq] = csb; }
  RAWBAR();
  float mean_b, rden;
  { const f32x2 a = stp[xrow], c = stp[64 + xrow]; ln_scalars(a[0] + c[0], a[1] + c[1], mean_b, rden); }
  rden *= QSCALE;
  u16* qp = (u16*)(ws + WS_Q) + (size_t)(tt * 8 + cg) * 5120 + xrow * 80 + gq * 4;
#pragma unroll
  for (int c = 0; c < 5; ++c)
    if ((c < 3) == (kh == 0)) {
      const f32x4 a = (acc[c] + red[c * 64] - (csp[c * 4 + gq] + csp[(5 + c) * 4 + gq]) * mean_b) * rden;
      *(u32x2*)(qp + c * 16) = (u32x2){cvtpk(a[0], a[1]), cvtpk(a[2], a[3])};
    }
  ST(); ST_OUT(ws, 5);
}

__global__ __launch_bounds__(512, 4) void k_pre(const u16* __restrict__ x, const u16* __restrict__ wq, const u16* __restrict__ wk,
                                                 const u16* __restrict__ wv, const u16* __restrict__ w1, const u16* __restrict__ w3,
                                                 const u16* __restrict__ w2, const u16* __restrict__ wo, char* __restrict__ ws, const u16* __restrict__ out) {
  extern __shared__ __attribute__((aligned(16))) char lds[];
  const int b = blockIdx.x;
  if (b < 128) ln_block(x, wk, wv, ws, lds, b);
  else if (b < 384) q_block(x, wq, ws, lds, b - 128);
  else {
    const int lane = threadIdx.x & 63, w = __builtin_amdgcn_readfirstlane(threadIdx.x >> 6), rb = b - 384;
    const unsigned lds0 = (unsigned)(uintptr_t)(LAS char*)lds;
    const int u1 = rb * 8 + w, u2 = 1024 + rb * 2 + w;
    const bool two = w < 2 && u2 < 1184;
    ST_DECL ST();
    retile_issue(wo, w1, w3, w2, lds0 + w * 6144, u1, lane);
    if (two) retile_issue(wo, w1, w3, w2, lds0 + 49152 + w * 6144, u2, lane);
    vmwait<0>();
    retile_finish(ws, (const LAS char*)lds + w * 6144, u1, lane);
    if (two) retile_finish(ws, (const LAS char*)lds + 49152 + w * 6144, u2, lane);
    ST(); ST_OUT(ws, 6);
  }
}

constexpr int KV_A = LDS_BYTES - KV_SLOT, KV_B = 0, KV_Cs = KV_SLOT, O_STG = 2 * KV_SLOT;

DI void attn_block(char* __restrict__ ws, char* lds, int b) {
  const int tid = threadIdx.x, lane = tid & 63, w = __builtin_amdgcn_readfirstlane(tid >> 6);
  const int bi = b >> 3;
  const int tt = (b & 7) * 4 + (bi & 3), hg = bi >> 2;
  const int r = lane & 31, h = lane >> 5;
  const unsigned lds0 = (unsigned)(uintptr_t)(LAS char*)lds;
  const unsigned lane16 = lane * 16;
  ST_DECL ST();
  __builtin_amdgcn_s_setprio(3);
  {
    const char* qg = ws + WS_Q + (size_t)(tt * 8 + hg) * 10240;
    glds16s(qg + w * 1024, lane16, lds0 + O_STG + w * 1024);
    if (w < 3) glds16s(w < 2 ? qg + 8192 + w * 1024 : ws + WS_KMAX, lane16, lds0 + O_STG + 8192 + w * 1024);
#pragma unroll
    for (int j = 0; j < 3; ++j) {
      const int pc = (7 - w) + 8 * j;
      if (pc < KV_PIECES) glds16s(ws + WS_KV + pc * 1024, lane16, lds0 + KV_A + pc * 1024);
    }
  }
  if (w >= 4) {
    const int lw = w - 4;
    const char* kvg = ws + WS_KV + lw * 1024;
    auto dma = [&](int chunk, int slotoff) {
#pragma unroll
      for (int j = 0; j < 6; ++j)
        if (j < 5 || lw < KV_PIECES - 20) glds16s(kvg + chunk * KV_CHUNK + j * 4096, lane16, lds0 + slotoff + (lw + 4 * j) * 1024);
    };
    dma(1, KV_B);
    vmwait<5>();
    asm volatile("s_barrier" ::: "memory");
    int fre = KV_Cs, nx = KV_A, nn = KV_B;
#pragma unroll 1
    for (int c = 0; c < 8; ++c) {
      asm volatile("s_waitcnt vmcnt(0)\n\ts_barrier" ::: "memory");
      if (c + 2 < 8) dma(c + 2, fre);
      const int t = fre; fre = nx; nx = nn; nn = t;
    }
  } else {
    asm volatile("s_waitcnt vmcnt(0)\n\ts_barrier" ::: "memory");
    __builtin_amdgcn_s_setprio(APRIO);
    ST();
    u32x4 q1a, q1b; u32x2 q2a, q2b;
    {
      const LAS char* qs = (const LAS char*)lds + O_STG + r * 160 + w * 40;
      const u32x2 a0 = *(const LAS u32x2*)(qs + 16 * h), a1 = *(const LAS u32x2*)(qs + 16 * h + 8);
      const u32x2 b0 = *(const LAS u32x2*)(qs + 5120 + 16 * h), b1 = *(const LAS u32x2*)(qs + 5120 + 16 * h + 8);
      q1a = (u32x4){a0[0], a0[1], a1[0], a1[1]}; q1b = (u32x4){b0[0], b0[1], b1[0], b1[1]};
      q2a = *(const LAS u32x2*)(qs + 32); q2b = *(const LAS u32x2*)(qs + 5120 + 32);
    }
    float km;
    {
      const f32x4 km4 = *(const LAS f32x4*)((LAS char*)lds + O_STG + 10240 + lane * 16);
      int k = __float_as_int(fmaxf(fmaxf(km4[0], km4[1]), fmaxf(km4[2], km4[3])));
      k = max(k, __builtin_amdgcn_update_dpp(0, k, 0x128, 0xf, 0xf, false)); k = max(k, __builtin_amdgcn_update_dpp(0, k, 0x124, 0xf, 0xf, false));
      k = max(k, __builtin_amdgcn_update_dpp(0, k, 0x122, 0xf, 0xf, false)); k = max(k, __builtin_amdgcn_update_dpp(0, k, 0x121, 0xf, 0xf, false));
      km = __int_as_float(max(__builtin_amdgcn_readlane(k, 0), __builtin_amdgcn_readlane(k, 16))) +
           __int_as_float(max(__builtin_amdgcn_readlane(k, 32), __builtin_amdgcn_readlane(k, 48)));
    }
    float qna = dot2(q1a[0], q1a[0], dot2(q1a[1], q1a[1], dot2(q1a[2], q1a[2], dot2(q1a[3], q1a[3], 0.f))));
    float qnb = dot2(q1b[0], q1b[0], dot2(q1b[1], q1b[1], dot2(q1b[2], q1b[2], dot2(q1b[3], q1b[3], 0.f))));
    if (h == 0) { qna = dot2(q2a[0], q2a[0], dot2(q2a[1], q2a[1], qna)); qnb = dot2(q2b[0], q2b[0], dot2(q2b[1], q2b[1], qnb)); }
    { const auto sw = __builtin_amdgcn_permlane32_swap(__float_as_uint(qna), __float_as_uint(qna), false, false); qna = __uint_as_float(sw[0]) + __uint_as_float(sw[1]); }
    { const auto sw = __builtin_amdgcn_permlane32_swap(__float_as_uint(qnb), __float_as_uint(qnb), false, false); qnb = __uint_as_float(sw[0]) + __uint_as_float(sw[1]); }
    const float bda = __builtin_amdgcn_sqrtf(qna * km) * 1.02f + 1e-3f, bdb = __builtin_amdgcn_sqrtf(qnb * km) * 1.02f + 1e-3f;
    const bool fast = __all(bda <= 48.f && bdb <= 48.f);
    if (h == 1) {
      q2a[0] = fast ? (cvtpk(-bda, 0.f) & 0xffffu) : 0u; q2a[1] = 0u;
      q2b[0] = fast ? (cvtpk(-bdb, 0.f) & 0xffffu) : 0u; q2b[1] = 0u;
    }
    const bf16x8 QA1 = __builtin_bit_cast(bf16x8, q1a), QB1 = __builtin_bit_cast(bf16x8, q1b);
    const s16x4 QA2 = __builtin_bit_cast(s16x4, q2a), QB2 = __builtin_bit_cast(s16x4, q2b);
    const int dv = r <= 20 ? r : 21;
    const LAS char* k1b = (const LAS char*)lds + lane * 16;
    const LAS char* k2b = (const LAS char*)lds + (h ? KV_C : KV_K2 + r * 8);
    const LAS char* vb = (const LAS char*)lds + KV_V + (h * VR + dv) * 16;
    f32x16 oa = {}, ob = {};
    const f32x16 zero16 = {};
    ST();
    if (fast) {
      f32x16 sA, sB;
      bf16x8 k1f, vf0, vf1; s16x4 k2f;
      u32x4 pa, pb;
      float e14, e15;
      k1f = *(const LAS bf16x8*)(k1b + KV_A); k2f = *(const LAS s16x4*)(k2b + KV_A); vf0 = *(const LAS bf16x8*)(vb + KV_A);
      sA = MFMA32(k1f, QA1, zero16); sA = MFMA32K8(k2f, QA2, sA);
      vf1 = (bf16x8){0, 0, 0, 0, 0, 0, 0, 0}; pb = (u32x4){0u, 0u, 0u, 0u}; e14 = 0.f; e15 = 0.f;
      int cur = KV_A, nxt = KV_B, fre = KV_Cs;
#define STEP(C, N, Q1, Q2, OC, OP, L1, L2, L3, L4) do { \
        N = MFMA32(k1f, Q1, zero16); SBAR(); L1; SBAR(); \
        C[0] = EXP2(C[0]); C[1] = EXP2(C[1]); SBAR(); pb[3] = cvtpk(e14, e15); PIN(pb); SBAR(); \
        C[2] = EXP2(C[2]); C[3] = EXP2(C[3]); SBAR(); pa[0] = cvtpk(C[0], C[1]); PIN(pa); SBAR(); \
        OP = MFMA32(vf1, __builtin_bit_cast(bf16x8, pb), OP); SBAR(); L2; SBAR(); \
        C[4] = EXP2(C[4]); C[5] = EXP2(C[5]); SBAR(); pa[1] = cvtpk(C[2], C[3]); PIN(pa); SBAR(); \
        C[6] = EXP2(C[6]); C[7] = EXP2(C[7]); SBAR(); pa[2] = cvtpk(C[4], C[5]); PIN(pa); SBAR(); \
        N = MFMA32K8(k2f, Q2, N); SBAR(); L3; SBAR(); \
        C[8] = EXP2(C[8]); C[9] = EXP2(C[9]); SBAR(); pa[3] = cvtpk(C[6], C[7]); PIN(pa); SBAR(); \
        C[10] = EXP2(C[10]); C[11] = EXP2(C[11]); SBAR(); pb[0] = cvtpk(C[8], C[9]); PIN(pb); SBAR(); \
        OC = MFMA32(vf0, __builtin_bit_cast(bf16x8, pa), OC); SBAR(); L4; SBAR(); \
        C[12] = EXP2(C[12]); C[13] = EXP2(C[13]); SBAR(); pb[1] = cvtpk(C[10], C[11]); PIN(pb); SBAR(); \
        e14 = EXP2(C[14]); e15 = EXP2(C[15]); SBAR(); pb[2] = cvtpk(C[12], C[13]); PIN(pb); PIN(e14); PIN(e15); SBAR(); \
      } while (0)
#define KT(i) do { \
        STEP(sA, sB, QB1, QB2, oa, ob, k1f = *(const LAS bf16x8*)((i) < 7 ? kc1 + ((i) + 1) * KV_TILE : k1b + nxt), \
             vf1 = *(const LAS bf16x8*)(vc + (i) * KV_TILE + 2 * VR * 16), k2f = *(const LAS s16x4*)((i) < 7 ? kc2 + ((i) + 1) * KV_TILE : k2b + nxt), (void)0); \
        STEP(sB, sA, QA1, QA2, ob, oa, (void)0, (void)0, (void)0, vf0 = *(const LAS bf16x8*)((i) < 7 ? vc + ((i) + 1) * KV_TILE : vb + nxt)); \
      } while (0)
#pragma unroll 1
      for (int c = 0; c < 8; ++c) {
        const LAS char* kc1 = k1b + cur; const LAS char* kc2 = k2b + cur; const LAS char* vc = vb + cur;
        KT(0); KT(1); KT(2);
        asm volatile("s_barrier" ::: "memory");
        KT(3); KT(4); KT(5); KT(6); KT(7);
        const int t = cur; cur = nxt; nxt = fre; fre = t;
      }
      pb[3] = cvtpk(e14, e15);
      ob = MFMA32(vf1, __builtin_bit_cast(bf16x8, pb), ob);
#undef KT
#undef STEP
    } else {
      float ma = -INFINITY, mb = -INFINITY;
      int cur = KV_A, nxt = KV_B, fre = KV_Cs;
#pragma unroll 1
      for (int c = 0; c < 8; ++c) {
#pragma unroll 1
        for (int i = 0; i < 8; ++i) {
          if (i == 3) RAWBAR();
          const int off = cur + i * KV_TILE;
          const bf16x8 k1f = *(const LAS bf16x8*)(k1b + off); const s16x4 k2f = *(const LAS s16x4*)(k2b + off);
          const bf16x8 vf0 = *(const LAS bf16x8*)(vb + off), vf1 = *(const LAS bf16x8*)(vb + off + 2 * VR * 16);
#pragma unroll
          for (int qs = 0; qs < 2; ++qs) {
            f32x16 sc = MFMA32(k1f, qs ? QB1 : QA1, zero16); sc = MFMA32K8(k2f, qs ? QB2 : QA2, sc);
            float& m = qs ? mb : ma; f32x16& o = qs ? ob : oa;
            float mx = sc[0];
#pragma unroll
            for (int j = 1; j < 16; ++j) mx = fmaxf(mx, sc[j]);
            mx = fmaxf(mx, __shfl_xor(mx, 32));
            const float mn = fmaxf(m, mx);
            const float al = EXP2(m - mn);
            m = mn;
#pragma unroll
            for (int j = 0; j < 16; ++j) { o[j] *= al; sc[j] = EXP2(sc[j] - mn); }
            const u32x4 pa = {cvtpk(sc[0], sc[1]), cvtpk(sc[2], sc[3]), cvtpk(sc[4], sc[5]), cvtpk(sc[6], sc[7])};
            const u32x4 pb = {cvtpk(sc[8], sc[9]), cvtpk(sc[10], sc[11]), cvtpk(sc[12], sc[13]), cvtpk(sc[14], sc[15])};
            o = MFMA32(vf0, __builtin_bit_cast(bf16x8, pa), o);
            o = MFMA32(vf1, __builtin_bit_cast(bf16x8, pb), o);
          }
        }
        const int t = cur; cur = nxt; nxt = fre; fre = t;
      }
    }
    PIN(oa); PIN(ob); ST();
#pragma unroll
    for (int qs = 0; qs < 2; ++qs) {
      const f32x16 o = qs ? ob : oa;
      const auto sw = __builtin_amdgcn_permlane32_swap(__float_as_uint(o[8]), __float_as_uint(o[8]), false, false);
      const float il = __builtin_amdgcn_rcpf(__uint_as_float(sw[1]));
      LAS char* os = (LAS char*)lds + O_STG + (qs * 32 + r) * 176 + w * 40 + 8 * h;
      *(LAS u32x2*)(os) = (u32x2){cvtpk(o[0] * il, o[1] * il), cvtpk(o[2] * il, o[3] * il)};
      *(LAS u32x2*)(os + 16) = (u32x2){cvtpk(o[4] * il, o[5] * il), cvtpk(o[6] * il, o[7] * il)};
      if (h == 0) *(LAS u32x2*)(os + 32) = (u32x2){cvtpk(o[8] * il, o[9] * il), cvtpk(o[10] * il, o[11] * il)};
    }
  }
  RAWBAR();
  const __amdgpu_buffer_rsrc_t orr = __builtin_amdgcn_make_buffer_rsrc(ws + WS_O, 0, S * D * 2, 0x00020000);
#pragma unroll
  for (int j = 0; j < 2; ++j) {
    const int ci = tid + 512 * j;
    if (ci < 640) {
      const int c = ci >> 6, cc = hg * 10 + c;
      const u32x4 v = *(const LAS u32x4*)((LAS char*)lds + O_STG + lane * 176 + c * 16);
      __builtin_amdgcn_raw_buffer_store_b128(v, orr, ((tt * 20 + (cc >> 2)) * 4 + (lane >> 4)) * 1024 + ((cc & 3) * 16 + (lane & 15)) * 16, 0, 16);
    }
  }
  vmwait<0>();
  RAWBAR();
  if (tid == 0) __hip_atomic_fetch_add((GAS unsigned*)(ws + WS_CNT) + (16 + tt) * 16, 1u, RLX_AGENT);
}

struct Frag3 { bf16x8 x; bf16x8 w[3]; };
struct Frag5 { bf16x8 x; bf16x8 w[5]; };
DI void wait_count(GAS unsigned* cnt, unsigned want) {
  for (unsigned spins = 0; spins < (1u << 22); ++spins) {
    if ((unsigned)__builtin_amdgcn_readfirstlane(__hip_atomic_load(cnt, RLX_AGENT)) >= want) break;
    __builtin_amdgcn_s_sleep(1);
  }
}
DI void ffn_block(const u16* __restrict__ x, char* __restrict__ ws, u16* __restrict__ out, char* lds, int b) {
  const int tid = threadIdx.x, lane = tid & 63, w = __builtin_amdgcn_readfirstlane(tid >> 6);
  const int bi = b >> 3;
  const unsigned lds0 = (unsigned)(uintptr_t)(LAS char*)lds, lane16 = lane * 16;
  ST_DECL ST();
  if (GPRIO) __builtin_amdgcn_s_setprio(GPRIO);
  {
    const int tp = ((b & 7) >> 2) * 8 + (bi & 7), cg = (b & 3) * 4 + (bi >> 3);
    const int srow = w * 16 + (lane >> 2);
    const u16* xsb = (const u16*)(ws + WS_XN) + (size_t)tp * 128 * D;
    const unsigned xs = (srow * D + (((lane & 3) ^ ((srow >> 2) & 3)) * 8)) * 2;
    const char* wsrc = ws + WS_W13 + (size_t)cg * 245760 + w * 1536;
    auto piece = [&](int kt, int slot, int j) {
      if (j == 0) glds16s(xsb + kt * 32, xs, lds0 + slot * SLOT + w * 1024);
      else glds_lin<48>(wsrc + kt * 12288 + (j - 1) * 768, lane16, lds0 + slot * SLOT + 8192 + w * 1536 + (j - 1) * 768);
    };
#pragma unroll
    for (int i = 0; i < 4; ++i) { piece(i, i, 0); piece(i, i, 1); piece(i, i, 2); }
    const int tq = w & 3, hc = w >> 2, r = lane & 31, h = lane >> 5;
    const int xrow = tq * 32 + r;
    const LAS char* xb0 = (const LAS char*)lds + xrow * 64 + ((h ^ ((xrow >> 2) & 3)) * 16);
    const LAS char* xb1 = (const LAS char*)lds + xrow * 64 + (((2 + h) ^ ((xrow >> 2) & 3)) * 16);
    const LAS char* wb = (const LAS char*)lds + 8192 + hc * 3072 + lane * 16;
    f32x16 acc[3] = {};
    auto rd = [&](Frag3& f, int slot, int s) {
      f.x = *(const LAS bf16x8*)((s ? xb1 : xb0) + slot * SLOT);
#pragma unroll
      for (int c = 0; c < 3; ++c) f.w[c] = *(const LAS bf16x8*)(wb + slot * SLOT + s * 6144 + c * 1024);
    };
    auto mm = [&](const Frag3& f, int s, int dk, int dsl) {
#pragma unroll
      for (int c = 0; c < 3; ++c) {
        acc[c] = MFMA32(f.w[c], f.x, acc[c]);
        SBAR();
        if (dk >= 0) { if (s == 1 && c == 0) piece(dk, dsl, 0); if (s == 1 && c == 2) piece(dk, dsl, 1); if (s == 0 && c == 0) piece(dk, dsl, 2); }
        SBAR();
      }
    };
    Frag3 f0, f1;
    vmwait<9>();
    RAWBAR();
    rd(f0, 0, 0);
#define IT(kt, sl, VM, PREV, DMA, NEXT) do { \
      rd(f1, sl, 1); SBAR(); mm(f0, 0, PREV ? (kt) + 3 : -1, ((sl) + 3) & 3); SBAR(); \
      if (NEXT) { vmwait<VM>(); RAWBAR(); rd(f0, ((sl) + 1) & 3, 0); } \
      SBAR(); mm(f1, 1, DMA ? (kt) + 4 : -1, sl); SBAR(); } while (0)
    IT(0, 0, 6, false, true, true); IT(1, 1, 6, true, true, true); IT(2, 2, 6, true, true, true); IT(3, 3, 6, true, true, true);
#pragma unroll 1
    for (int kt = 4; kt < 16; kt += 4) {
      if (kt == G1LATE) __builtin_amdgcn_s_setprio(3);
      IT(kt, 0, 6, true, true, true); IT(kt + 1, 1, 6, true, true, true); IT(kt + 2, 2, 6, true, true, true); IT(kt + 3, 3, 6, true, true, true);
    }
    IT(16, 0, 6, true, false, true); IT(17, 1, 3, false, false, true); IT(18, 2, 0, false, false, true); IT(19, 3, 0, false, false, false);
#undef IT
    PIN(acc[0]); PIN(acc[1]); PIN(acc[2]); ST();
    __builtin_amdgcn_s_setprio(3);
    u32x4 hv[3];
#pragma unroll
    for (int c = 0; c < 3; ++c) {
      float v[8];
#pragma unroll
      for (int j = 0; j < 8; ++j) {
        const float gt = acc[c][j], up = acc[c][j + 8];
        v[j] = gt * __builtin_amdgcn_rcpf(1.f + EXP2(-1.4426950408889634f * gt)) * up;
      }
      hv[c] = (u32x4){cvtpk(v[0], v[1]), cvtpk(v[2], v[3]), cvtpk(v[4], v[5]), cvtpk(v[6], v[7])};
    }
#pragma unroll
    for (int c = 0; c < 3; ++c) *(LAS u32x4*)((LAS char*)lds + xrow * 208 + hc * 96 + c * 32 + h * 16) = hv[c];
    RAWBAR();
    const __amdgpu_buffer_rsrc_t hr = __builtin_amdgcn_make_buffer_rsrc(ws + WS_H, 0, S * F * 2, 0x00020000);
#pragma unroll
    for (int j = 0; j < 3; ++j) {
      const int ci = tid + 512 * j, row = ci / 12, pos = ci - row * 12;
      const u32x4 v = *(const LAS u32x4*)((LAS char*)lds + row * 208 + pos * 16);
      __builtin_amdgcn_raw_buffer_store_b128(v, hr, ((tp * 128 + row) * F + cg * 96 + pos * 8) * 2, 0, 16);
    }
    vmwait<0>();
    RAWBAR();
    if (tid == 0) __hip_atomic_fetch_add((GAS unsigned*)(ws + WS_CNT) + tp * 16, 1u, RLX_AGENT);
  }
  ST();
  const int tt = (b & 7) * 4 + (bi & 3), t0 = tt * 64, cg = bi >> 2, c0 = cg * 80;
  const int ts = w & 3, kh = w >> 2, r16 = lane & 15, gq = lane >> 4, xrow = ts * 16 + r16;
  f32x4 acc[5] = {};
  const char* wog = ws + WS_WO + (size_t)cg * 102400;
  bf16x8 wr[10]; u32x2 xv[5];
  auto wop = [&](int j) { glds16s(wog + (j >= 5 ? 10240 : 0) + (j * 8 + w) * 1024, lane16, lds0 + (j * 8 + w) * 1024); };
  {
    const int srow = w * 8 + (lane >> 3);
    const unsigned hs = (srow * F + ((lane & 7) ^ ((srow >> 1) & 7)) * 8) * 2;
    const u16* hb = (const u16*)(ws + WS_H) + (size_t)t0 * F;
    const char* wsrc = ws + WS_W2 + (size_t)cg * 245760 + w * 1280;
    auto piece = [&](int kt, int slot, int j) {
      if (j == 0) glds16s_sc1(hb + kt * 64, hs, lds0 + slot * ST64 + w * 1024);
      else glds_lin<40>(wsrc + kt * 10240 + (j - 1) * 640, lane16, lds0 + slot * ST64 + 8192 + w * 1280 + (j - 1) * 640);
    };
#pragma unroll
    for (int i = 0; i < 4; ++i) { piece(i, i, 1); piece(i, i, 2); }
    if (w == 0) wait_count((GAS unsigned*)(ws + WS_CNT) + (tt >> 1) * 16, 16u);
    RAWBAR();
    __builtin_amdgcn_s_setprio(P2PRIO);
    ST();
#pragma unroll
    for (int i = 0; i < 4; ++i) piece(i, i, 0);
    const LAS char* xb = (const LAS char*)lds + xrow * 128 + (((kh * 4 + gq) ^ ((xrow >> 1) & 7)) * 16);
    const LAS char* wb = (const LAS char*)lds + 8192 + kh * 5120 + lane * 16;
    auto rd = [&](Frag5& f, int slot) {
      f.x = *(const LAS bf16x8*)(xb + slot * ST64);
#pragma unroll
      for (int c = 0; c < 5; ++c) f.w[c] = *(const LAS bf16x8*)(wb + slot * ST64 + c * 1024);
    };
    auto mm = [&](const Frag5& f, int dk, int dsl) {
#pragma unroll
      for (int c = 0; c < 5; ++c) { acc[c] = MFMA16(f.w[c], f.x, acc[c]); SBAR(); if (dk >= 0 && !(c & 1)) piece(dk, dsl, c >> 1); SBAR(); }
    };
    Frag5 fa, fb;
    vmwait<3>();
    RAWBAR();
    rd(fa, 0);
#define IT(kt, sl, FC, FN, VM, DMA) do { vmwait<VM>(); RAWBAR(); rd(FN, ((sl) + 1) & 3); SBAR(); mm(FC, DMA ? (kt) + 4 : -1, sl); SBAR(); } while (0)
    IT(0, 0, fa, fb, 2, true); IT(1, 1, fb, fa, 4, true); IT(2, 2, fa, fb, 6, true); IT(3, 3, fb, fa, 6, true);
#pragma unroll 1
    for (int kt = 4; kt < 20; kt += 4) {
      IT(kt, 0, fa, fb, 6, true); IT(kt + 1, 1, fb, fa, 6, true); IT(kt + 2, 2, fa, fb, 6, true); IT(kt + 3, 3, fb, fa, 6, true);
    }
    IT(20, 0, fa, fb, 6, false); wop(0); wop(1); wop(9);
    IT(21, 1, fb, fa, 6, false); wop(2); wop(3);
    IT(22, 2, fa, fb, 5, false); wop(4); wop(5);
    {
      const char* wrp = wog + kh * 51200 + 40960;
#pragma unroll
      for (int j = 0; j < 10; ++j) asm volatile("global_load_dwordx4 %0, %1, %2" : "=v"(wr[j]) : "v"(lane16), "s"(wrp + j * 1024) : "memory");
      const u16* xp = x + (size_t)(t0 + xrow) * D + c0 + gq * 4;
#pragma unroll
      for (int c = 0; c < 5; ++c) asm volatile("global_load_dwordx2 %0, %1, off offset:%2" : "=v"(xv[c]) : "v"(xp), "i"(c * 32) : "memory");
    }
    SBAR();
    mm(fb, -1, 0);
#undef IT
  }
  PIN(acc[0]); ST();
  RAWBAR();
  wop(6); wop(7); wop(8);
  asm volatile("s_waitcnt vmcnt(3)" : "+v"(wr[0]), "+v"(wr[1]), "+v"(wr[2]), "+v"(wr[3]), "+v"(wr[4]), "+v"(wr[5]), "+v"(wr[6]), "+v"(wr[7]), "+v"(wr[8]), "+v"(wr[9]),
               "+v"(xv[0]), "+v"(xv[1]), "+v"(xv[2]), "+v"(xv[3]), "+v"(xv[4]) :: "memory");
  if (kh == 0) {
#pragma unroll
    for (int c = 0; c < 5; ++c) acc[c] += (f32x4){bflo(xv[c][0]), bfhi(xv[c][0]), bflo(xv[c][1]), bfhi(xv[c][1])};
  }
#pragma unroll
  for (int c = 0; c < 5; ++c) PIN(acc[c]);
  vmwait<0>();
  __builtin_amdgcn_s_setprio(3);
  ST();
  const LAS char* wbt = (const LAS char*)lds + kh * 40960 + lane * 16;
  if (w == 0) wait_count((GAS unsigned*)(ws + WS_CNT) + (16 + tt) * 16, 8u);
  RAWBAR();
  ST();
  {
    u32x4 of[10];
    const char* op = ws + WS_O + ((tt * 20 + kh * 10) * 4 + ts) * 1024;
#pragma unroll
    for (int s = 0; s < 10; ++s) asm volatile("global_load_dwordx4 %0, %1, %2 sc1" : "=v"(of[(s + 8) % 10]) : "v"(lane16), "s"(op + ((s + 8) % 10) * 4096) : "memory");
#define OSTEP(s, N, WC, WN) do { \
      if ((s) + 1 < 8) { _Pragma("unroll") for (int c = 0; c < 5; ++c) WN[c] = *(const LAS bf16x8*)(wbt + ((s) + 1) * 5120 + c * 1024); } \
      SBAR(); asm volatile("s_waitcnt vmcnt(" #N ")" : "+v"(of[s]) :: "memory"); SBAR(); \
      _Pragma("unroll") for (int c = 0; c < 5; ++c) acc[c] = MFMA16(WC[c], __builtin_bit_cast(bf16x8, of[s]), acc[c]); SBAR(); } while (0)
    asm volatile("s_waitcnt vmcnt(9)" : "+v"(of[8]) :: "memory");
#pragma unroll
    for (int c = 0; c < 5; ++c) acc[c] = MFMA16(wr[c], __builtin_bit_cast(bf16x8, of[8]), acc[c]);
    SBAR();
    bf16x8 w0[5];
#pragma unroll
    for (int c = 0; c < 5; ++c) w0[c] = *(const LAS bf16x8*)(wbt + c * 1024);
    SBAR(); asm volatile("s_waitcnt vmcnt(8)" : "+v"(of[9]) :: "memory"); SBAR();
#pragma unroll
    for (int c = 0; c < 5; ++c) acc[c] = MFMA16(wr[5 + c], __builtin_bit_cast(bf16x8, of[9]), acc[c]);
    SBAR();
    bf16x8 wn[5];
    OSTEP(0, 7, w0, wn); OSTEP(1, 6, wn, w0); OSTEP(2, 5, w0, wn); OSTEP(3, 4, wn, w0);
    OSTEP(4, 3, w0, wn); OSTEP(5, 2, wn, w0); OSTEP(6, 1, w0, wn); OSTEP(7, 0, wn, w0);
#undef OSTEP
  }
  RAWBAR();
#pragma unroll
  for (int c = 0; c < 5; ++c) *(LAS f32x4*)((LAS char*)lds + kh * 64 * 336 + xrow * 336 + c * 64 + gq * 16) = acc[c];
  RAWBAR();
#pragma unroll
  for (int j = 0; j < 2; ++j) {
    const int ci = tid + 512 * j;
    if (ci < 640) {
      const int row = ci / 10, c = ci - row * 10;
      const LAS f32x4* a0 = (const LAS f32x4*)((LAS char*)lds + row * 336 + c * 32);
      const LAS f32x4* a1 = (const LAS f32x4*)((LAS char*)lds + 64 * 336 + row * 336 + c * 32);
      const f32x4 lo = a0[0] + a1[0], hi = a0[1] + a1[1];
      const u32x4 res = {cvtpk(lo[0], lo[1]), cvtpk(lo[2], lo[3]), cvtpk(hi[0], hi[1]), cvtpk(hi[2], hi[3])};
      *(u32x4*)(out + (size_t)(t0 + row) * D + c0 + c * 8) = res;
    }
  }
  ST(); ST_OUT(ws, 2);
}

__global__ __launch_bounds__(512, 4) void k_mid(const u16* __restrict__ x, char* __restrict__ ws, u16* __restrict__ out) {
  extern __shared__ __attribute__((aligned(16))) char lds[];
  if (blockIdx.x < 256) attn_block(ws, lds, blockIdx.x);
  else ffn_block(x, ws, out, lds, blockIdx.x - 256);
}

extern "C" void kernel_launch(void* const* d_in, const int* in_sizes, int n_in, void* d_out, int out_size, void* d_ws, size_t ws_size,
                              hipStream_t stream) {
  const u16* x = (const u16*)d_in[0];
  const u16* wq = (const u16*)d_in[1];
  const u16* wk = (const u16*)d_in[2];
  const u16* wv = (const u16*)d_in[3];
  const u16* wo = (const u16*)d_in[4];
  const u16* w1 = (const u16*)d_in[6];
  const u16* w3 = (const u16*)d_in[7];
  const u16* w2 = (const u16*)d_in[8];
  char* ws = (char*)d_ws;
  (void)hipFuncSetAttribute((const void*)k_mid, hipFuncAttributeMaxDynamicSharedMemorySize, LDS_BYTES);
  (void)hipFuncSetAttribute((const void*)k_pre, hipFuncAttributeMaxDynamicSharedMemorySize, LDS_BYTES);
  k_pre<<<512, 512, LDS_BYTES, stream>>>(x, wq, wk, wv, w1, w3, w2, wo, ws, (const u16*)d_out);
  k_mid<<<512, 512, LDS_BYTES, stream>>>(x, ws, (u16*)d_out);
}
```

```cpp
#include <hip/hip_runtime.h>
#include <hip/hip_bf16.h>
#include <stdint.h>

typedef short bf16x8 __attribute__((ext_vector_type(8)));
typedef short s16x4 __attribute__((ext_vector_type(4)));
typedef float f32x2 __attribute__((ext_vector_type(2)));
typedef float f32x4 __attribute__((ext_vector_type(4)));
typedef float f32x16 __attribute__((ext_vector_type(16)));
typedef unsigned u32x4 __attribute__((ext_vector_type(4)));
typedef unsigned u32x2 __attribute__((ext_vector_type(2)));
typedef __bf16 bf16x2_t __attribute__((ext_vector_type(2)));
typedef unsigned short u16;
#define LAS __attribute__((address_space(3)))
#define GAS __attribute__((address_space(1)))
#define RLX_AGENT __ATOMIC_RELAXED, __HIP_MEMORY_SCOPE_AGENT
#define DI __device__ __forceinline__
#define SBAR() __builtin_amdgcn_sched_barrier(0)
#define PIN(x) asm volatile("" : "+v"(x))

constexpr int GPRIO = 1, APRIO = 2;
constexpr int S = 2048, D = 640, H = 32, DH = 20, F = 1536;
constexpr int VR = 22;
constexpr int KV_K2 = 1024, KV_C = 1280, KV_V = 1296, KV_TILE = KV_V + 4 * VR * 16;
constexpr int KV_CHUNK = 8 * KV_TILE;
constexpr int KV_PIECES = (KV_CHUNK + 1023) / 1024, KV_SLOT = KV_PIECES * 1024;
constexpr size_t WS_CNT = 0;
constexpr size_t WS_KMAX = 12288;
constexpr size_t WS_KV = 16384;
constexpr size_t WS_XN = WS_KV + 192 * 1024;
constexpr size_t WS_O = WS_XN + (size_t)S * D * 2;
constexpr size_t WS_H = WS_O + (size_t)S * D * 2;
constexpr size_t WS_Q = WS_H + (size_t)S * F * 2;
constexpr size_t WS_WO = WS_Q + (size_t)S * D * 2;
constexpr size_t WS_W13 = WS_WO + (size_t)D * D * 2;
constexpr size_t WS_W2 = WS_W13 + (size_t)2 * D * F * 2;
constexpr float QSCALE = 0.22360679774997896f * 1.4426950408889634f;
constexpr int LDS_BYTES = 81920, SLOT = 20480;

DI unsigned cvtpk(float a, float b) { f32x2 v = {a, b}; bf16x2_t r = __builtin_convertvector(v, bf16x2_t); return __builtin_bit_cast(unsigned, r); }
DI float bflo(unsigned u) { return __uint_as_float(u << 16); }
DI float bfhi(unsigned u) { return __uint_as_float(u & 0xffff0000u); }
DI float rbf(float a) { return bflo(cvtpk(a, a)); }
DI float dot2(unsigned a, unsigned b, float c) { return __builtin_amdgcn_fdot2_f32_bf16(__builtin_bit_cast(bf16x2_t, a), __builtin_bit_cast(bf16x2_t, b), c, false); }
DI s16x4 trread(const LAS char* p) { return __builtin_bit_cast(s16x4, __builtin_amdgcn_ds_read_tr16_b64_v4i16((LAS s16x4*)p)); }
DI bf16x8 cat8(s16x4 a, s16x4 b) { return __builtin_shufflevector(a, b, 0, 1, 2, 3, 4, 5, 6, 7); }
DI void glds16s(const void* sbase, unsigned voff, unsigned lds_base) {
  unsigned sv;
  asm volatile("s_mov_b32 %0, m0\n\ts_mov_b32 m0, %3\n\ts_nop 0\n\tglobal_load_lds_dwordx4 %1, %2\n\ts_mov_b32 m0, %0" : "=&s"(sv) : "v"(voff), "s"(sbase), "s"(lds_base) : "memory");
}
DI void glds16s_sc1(const void* sbase, unsigned voff, unsigned lds_base) {
  unsigned sv;
  asm volatile("s_mov_b32 %0, m0\n\ts_mov_b32 m0, %3\n\ts_nop 0\n\tglobal_load_lds_dwordx4 %1, %2 sc1\n\ts_mov_b32 m0, %0" : "=&s"(sv) : "v"(voff), "s"(sbase), "s"(lds_base) : "memory");
}
#define MFMA32(a, b, c) __builtin_amdgcn_mfma_f32_32x32x16_bf16(a, b, c, 0, 0, 0)
#define MFMA32K8(a, b, c) __builtin_amdgcn_mfma_f32_32x32x8bf16_1k(a, b, c, 0, 0, 0)
#define MFMA16(a, b, c) __builtin_amdgcn_mfma_f32_16x16x32_bf16(a, b, c, 0, 0, 0)
#define EXP2(v) __builtin_amdgcn_exp2f(v)
#define RAWBAR() asm volatile("s_waitcnt lgkmcnt(0)\n\ts_barrier" ::: "memory")
template <int N> DI void vmwait() { asm volatile("s_waitcnt vmcnt(%0)" :: "n"(N) : "memory"); }
DI void ln_scalars(float sum, float sq, float& mean_b, float& rden) {
  const float mean = sum * (1.f / D);
  const float var = fmaxf(sq * (1.f / D) - mean * mean, 0.f);
  mean_b = rbf(mean);
  rden = __builtin_amdgcn_rcpf(rbf(__builtin_amdgcn_sqrtf(rbf(rbf(var) + 1e-5f))));
}

template <int NL> DI void glds_lin(const char* src, unsigned lane16, unsigned dst) {
  unsigned sv;
  asm volatile("s_mov_b32 %0, m0\n\ts_mov_b32 m0, %3\n\ts_mov_b32 exec_hi, %4\n\tglobal_load_lds_dwordx4 %1, %2\n\ts_mov_b32 exec_hi, -1\n\ts_mov_b32 m0, %0"
               : "=&s"(sv) : "v"(lane16), "s"(src), "s"(dst), "n"((1u << (NL - 32)) - 1u) : "memory");
}

constexpr int ST64 = 18432;

DI void retile_issue(const u16* __restrict__ wo, const u16* __restrict__ w1, const u16* __restrict__ w3, const u16* __restrict__ w2,
                     unsigned ldsu, int u, int lane) {
#pragma unroll
  for (int j = 0; j < 3; ++j) {
    const int ci = j * 64 + lane, rr = ci / 12, pos = ci - rr * 12;
    if (u < 640) {
      const int cg = u / 40, ks = u - cg * 40;
      glds16s(w1 + (size_t)ks * 16 * F + cg * 96, (rr * F + pos * 8) * 2, ldsu + j * 1024);
      glds16s(w3 + (size_t)ks * 16 * F + cg * 96, (rr * F + pos * 8) * 2, ldsu + 3072 + j * 1024);
    } else {
      const int i = u < 800 ? u - 640 : u - 800, nk = u < 800 ? 20 : 48, cg = i / nk, ks = i - cg * nk, c0 = cg * 80 - (cg & 1) * 16;
      const u16* src = (u < 800 ? wo : w2) + (size_t)ks * 32 * D + c0;
      const unsigned vo = (rr * D + (c0 + pos * 8 < D ? pos : 0) * 8) * 2;
      glds16s(src, vo, ldsu + j * 1024);
      glds16s(src + 16 * D, vo, ldsu + 3072 + j * 1024);
    }
  }
}
DI void retile_finish(char* __restrict__ ws, const LAS char* im, int u, int lane) {
  const int h = lane >> 5, q4 = (lane & 15) >> 2, p = lane & 3, blk = (lane >> 4) & 1;
  if (u < 640) {
    const LAS char* a = im + blk * 3072 + (8 * h + q4) * 192 + (((p & 1) << 1) | (p >> 1)) * 8;
    char* dst = ws + WS_W13 + (size_t)u * 6144 + lane * 16;
#pragma unroll
    for (int c = 0; c < 6; ++c) *(bf16x8*)(dst + c * 1024) = cat8(trread(a + c * 32), trread(a + c * 32 + 768));
  } else {
    const int i = u < 800 ? u - 640 : u - 800, cg = i / (u < 800 ? 20 : 48);
    const LAS char* a = im + ((lane >> 4) * 8 + q4) * 192 + (cg & 1) * 32 + p * 8;
    char* dst = ws + (u < 800 ? WS_WO : WS_W2) + (size_t)i * 5120 + lane * 16;
#pragma unroll
    for (int c = 0; c < 5; ++c) *(bf16x8*)(dst + c * 1024) = cat8(trread(a + c * 32), trread(a + c * 32 + 768));
  }
}

constexpr int LN_ST = 25600, LN_RED = LN_ST + 512, LN_HALF = LN_RED + 8192;
DI void ln_block(const u16* __restrict__ x, const u16* __restrict__ wk, const u16* __restrict__ wv, char* __restrict__ ws, char* ldsb, int b) {
  const int tid = threadIdx.x, lane = tid & 63, w8 = __builtin_amdgcn_readfirstlane(tid >> 6), w = w8 & 3, kv = w8 >> 2;
  const int t16 = (b & 7) * 16 + (b >> 3);
  const int r16 = lane & 15, gq = lane >> 4;
  LAS char* lds = (LAS char*)ldsb + kv * LN_HALF;
  const unsigned lds0 = (unsigned)(uintptr_t)lds;
  const int token = t16 * 16 + r16;
  __builtin_amdgcn_s_setprio(3);
  u32x4 xf[5];
  const u16* xp = x + (size_t)token * D + w * 160 + gq * 8;
#pragma unroll
  for (int s = 0; s < 5; ++s) asm volatile("global_load_dwordx4 %0, %1, off offset:%2" : "=v"(xf[s]) : "v"(xp), "i"(s * 64) : "memory");
  const u16* wsrc = kv ? wv : wk;
#pragma unroll
  for (int j = 0; j < 7; ++j)
    if (j < 6 || w == 0) glds16s(wsrc + (w + 4 * j) * 512, lane * 16, lds0 + (w + 4 * j) * 1024);
  asm volatile("s_waitcnt vmcnt(0)" : "+v"(xf[0]), "+v"(xf[1]), "+v"(xf[2]), "+v"(xf[3]), "+v"(xf[4]) :: "memory");
  float sum = 0.f, sq = 0.f, sum2 = 0.f, sq2 = 0.f;
#pragma unroll
  for (int s = 0; s < 5; ++s)
#pragma unroll
    for (int j = 0; j < 4; j += 2) {
      sum = dot2(xf[s][j], 0x3F803F80u, sum); sq = dot2(xf[s][j], xf[s][j], sq);
      sum2 = dot2(xf[s][j + 1], 0x3F803F80u, sum2); sq2 = dot2(xf[s][j + 1], xf[s][j + 1], sq2);
    }
  sum += sum2; sq += sq2;
  sum += __shfl_xor(sum, 16); sq += __shfl_xor(sq, 16);
  sum += __shfl_xor(sum, 32); sq += __shfl_xor(sq, 32);
  if (b == 0 && tid < 48) ((unsigned*)(ws + WS_CNT))[tid * 16] = 0u;
  LAS f32x2* stp = (LAS f32x2*)(lds + LN_ST);
  if (gq == 0) stp[w * 16 + r16] = (f32x2){sum, sq};
  RAWBAR();
  { const f32x2 a = stp[r16], c = stp[16 + r16], d = stp[32 + r16], e = stp[48 + r16]; sum = (a[0] + c[0]) + (d[0] + e[0]); sq = (a[1] + c[1]) + (d[1] + e[1]); }
  float mean_b, rden;
  ln_scalars(sum, sq, mean_b, rden);
#pragma unroll
  for (int s = 0; s < 5; ++s)
#pragma unroll
    for (int j = 0; j < 4; ++j) {
      const unsigned d = cvtpk(bflo(xf[s][j]) - mean_b, bfhi(xf[s][j]) - mean_b);
      xf[s][j] = cvtpk(bflo(d) * rden, bfhi(d) * rden);
    }
  if ((w >> 1) == kv) {
    const int row = token & 127;
    char* xn = ws + WS_XN + ((size_t)((token >> 7) * 20 + w * 5) * 128 + row) * 64 + ((gq ^ ((row >> 2) & 3)) * 16);
#pragma unroll
    for (int s = 0; s < 5; ++s) *(u32x4*)(xn + s * 8192) = xf[s];
  }
  const int q4 = (lane & 15) >> 2, p = lane & 3;
  const LAS char* e0 = lds + (w * 160 + gq * 8 + q4) * 40 + p * 8;
  const LAS char* e1 = lds + (w * 160 + gq * 8 + q4) * 40 + 32;
  f32x4 acc[2] = {};
  if (kv == 0) {
#pragma unroll
    for (int s = 0; s < 5; ++s) {
      acc[0] = MFMA16(cat8(trread(e0 + s * 1280), trread(e0 + s * 1280 + 160)), __builtin_bit_cast(bf16x8, xf[s]), acc[0]);
      acc[1] = MFMA16(cat8(trread(e1 + s * 1280), trread(e1 + s * 1280 + 160)), __builtin_bit_cast(bf16x8, xf[s]), acc[1]);
    }
  } else {
#pragma unroll
    for (int s = 0; s < 5; ++s) {
      acc[0] = MFMA16(__builtin_bit_cast(bf16x8, xf[s]), cat8(trread(e0 + s * 1280), trread(e0 + s * 1280 + 160)), acc[0]);
      acc[1] = MFMA16(__builtin_bit_cast(bf16x8, xf[s]), cat8(trread(e1 + s * 1280), trread(e1 + s * 1280 + 160)), acc[1]);
    }
  }
  LAS f32x4* red = (LAS f32x4*)(lds + LN_RED) + lane;
  red[(w * 2) * 64] = acc[0]; red[(w * 2 + 1) * 64] = acc[1];
  RAWBAR();
  if (w < 2) {
    const f32x4 a = (red[w * 64] + red[(2 + w) * 64]) + (red[(4 + w) * 64] + red[(6 + w) * 64]);
    const u32x2 v = {cvtpk(a[0], a[1]), cvtpk(a[2], a[3])};
    char* kvp = ws + WS_KV + (t16 >> 1) * KV_TILE;
    if (kv == 0) {
      const int r = token & 31;
      float n2 = dot2(v[0], v[0], dot2(v[1], v[1], 0.f));
      if (w == 0) {
        *(u32x2*)(kvp + (gq >> 1) * 512 + r * 16 + (gq & 1) * 8) = v;
      } else {
        if (gq == 0) *(u32x2*)(kvp + KV_K2 + r * 8) = v;
        else n2 = 0.f;
        if (gq == 1 && r == 0) { const u32x4 c1 = {0x00003F80u, 0u, 0u, 0u}; *(u32x4*)(kvp + KV_C) = c1; }
      }
      n2 += __shfl_xor(n2, 16); n2 += __shfl_xor(n2, 32);
#pragma unroll
      for (int o = 1; o < 16; o <<= 1) n2 = fmaxf(n2, __shfl_xor(n2, o));
      if (lane == 0) ((float*)(ws + WS_KMAX))[w * 128 + t16] = n2;
    } else {
      char* vp = kvp + KV_V + (((t16 & 1) * 2 + (gq & 1)) * VR) * 16 + (gq >> 1) * 8;
      if (w == 0) *(u32x2*)(vp + r16 * 16) = v;
      else if (r16 < 6) {
        const u32x2 one = {0x3F803F80u, 0x3F803F80u}, zer = {0u, 0u};
        *(u32x2*)(vp + (16 + r16) * 16) = r16 < 4 ? v : (r16 == 4 ? one : zer);
      }
    }
  }
}

struct FragT { bf16x8 x; s16x4 w[10]; };
DI void q_block(const u16* __restrict__ x, const u16* __restrict__ wq, char* __restrict__ ws, char* lds, int b) {
  const int tid = threadIdx.x, lane = tid & 63, w = __builtin_amdgcn_readfirstlane(tid >> 6);
  const int bi = b >> 3, tt = (b & 7) * 4 + (bi & 3), cg = bi >> 2, odd = cg & 1, c0 = cg * 80 - odd * 16;
  const int ts = w & 3, kh = w >> 2, r16 = lane & 15, gq = lane >> 4, q4 = (lane & 15) >> 2, p = lane & 3;
  const unsigned lds0 = (unsigned)(uintptr_t)(LAS char*)lds;
  const int srow = w * 8 + (lane >> 3);
  const unsigned as = (srow * D + ((lane & 7) ^ ((srow >> 1) & 7)) * 8) * 2;
  const u16* ab = x + (size_t)tt * 64 * D;
  const int rr = lane / 12, ch = (lane - rr * 12) ^ ((w & 1) << 1);
  const unsigned bs = ((rr & 3) * D + (c0 + ch * 8 < D ? ch : 0) * 8) * 2;
  const u16* bb = wq + (size_t)w * 8 * D + c0;
  auto piece = [&](int kt, int j) {
    const int slot = kt & 3;
    if (j == 0) glds16s(ab + kt * 64, as, lds0 + slot * SLOT + w * 1024);
    else glds_lin<48>((const char*)(bb + (size_t)(kt * 64 + (j - 1) * 4) * D), bs, lds0 + slot * SLOT + 8192 + (2 * w + j - 1) * 768);
  };
#pragma unroll
  for (int i = 0; i < 4; ++i) { piece(i, 0); piece(i, 1); piece(i, 2); }
  const int xrow = ts * 16 + r16;
  const LAS char* xb = (const LAS char*)lds + xrow * 128 + (((kh * 4 + gq) ^ ((xrow >> 1) & 7)) * 16);
  const LAS char* wb[2];
#pragma unroll
  for (int e = 0; e < 2; ++e) wb[e] = (const LAS char*)lds + 8192 + (kh * 32 + gq * 8 + q4) * 192 + (((e + odd) ^ (gq & 1)) * 32) + p * 8;
  const bf16x8 ones = {0x3F80, 0x3F80, 0x3F80, 0x3F80, 0x3F80, 0x3F80, 0x3F80, 0x3F80};
  f32x4 acc[5] = {}, sx = {}, sxx = {}, csa = {}, csb = {};
  auto rd = [&](FragT& f, int slot) {
    f.x = *(const LAS bf16x8*)(xb + slot * SLOT);
#pragma unroll
    for (int c = 0; c < 5; ++c) { f.w[2 * c] = trread(wb[c & 1] + slot * SLOT + (c >> 1) * 64); f.w[2 * c + 1] = trread(wb[c & 1] + slot * SLOT + (c >> 1) * 64 + 768); }
  };
  auto mm = [&](const FragT& f, int dk) {
#pragma unroll
    for (int c = 0; c < 5; ++c) {
      acc[c] = MFMA16(cat8(f.w[2 * c], f.w[2 * c + 1]), f.x, acc[c]);
      SBAR(); if (dk >= 0 && !(c & 1)) piece(dk, c >> 1); SBAR();
    }
    sx = MFMA16(ones, f.x, sx); sxx = MFMA16(f.x, f.x, sxx);
#pragma unroll
    for (int c = 0; c < 4; ++c) if (ts == c) csa = MFMA16(cat8(f.w[2 * c], f.w[2 * c + 1]), ones, csa);
    if (ts == 0) csb = MFMA16(cat8(f.w[8], f.w[9]), ones, csb);
  };
  FragT fa, fb;
  vmwait<9>();
  RAWBAR();
  rd(fa, 0);
#define IT(kt, FC, FN, VM, DMA) do { vmwait<VM>(); RAWBAR(); rd(FN, ((kt) + 1) & 3); SBAR(); mm(FC, DMA ? (kt) + 4 : -1); SBAR(); } while (0)
  IT(0, fa, fb, 6, true); IT(1, fb, fa, 6, true); IT(2, fa, fb, 6, true);  IT(3, fb, fa, 6, true); IT(4, fa, fb, 6, true);
  IT(5, fb, fa, 6, true);  IT(6, fa, fb, 6, false); IT(7, fb, fa, 3, false); IT(8, fa, fb, 0, false);
  mm(fb, -1);
#undef IT

  LAS f32x4* red = (LAS f32x4*)((LAS char*)lds + 2 * SLOT) + ts * 5 * 64 + lane;
  LAS f32x2* stp = (LAS f32x2*)((LAS char*)lds + 3 * SLOT);
  LAS f32x4* csp = (LAS f32x4*)((LAS char*)lds + 3 * SLOT + 1024);
  if (kh == 0) { red[3 * 64] = acc[3]; red[4 * 64] = acc[4]; }
  else { red[0] = acc[0]; red[64] = acc[1]; red[128] = acc[2]; }
  if (gq == q4) stp[kh * 64 + xrow] = (f32x2){sx[0], p == 0 ? sxx[0] : p == 1 ? sxx[1] : p == 2 ? sxx[2] : sxx[3]};
  if (r16 == 0) { csp[(kh * 5 + ts) * 4 + gq] = csa; if (ts == 0) csp[(kh * 5 + 4) * 4 + gq] = csb; }
  RAWBAR();
  float mean_b, rden;
  { const f32x2 a = stp[xrow], c = stp[64 + xrow]; ln_scalars(a[0] + c[0], a[1] + c[1], mean_b, rden); }
  rden *= QSCALE;
  u16* qp = (u16*)(ws + WS_Q) + (size_t)(tt * 8 + cg) * 5120 + xrow * 80 + gq * 4;
#pragma unroll
  for (int c = 0; c < 5; ++c)
    if ((c < 3) == (kh == 0)) {
      const f32x4 a = (acc[c] + red[c * 64] - (csp[c * 4 + gq] + csp[(5 + c) * 4 + gq]) * mean_b) * rden;
      *(u32x2*)(qp + c * 16) = (u32x2){cvtpk(a[0], a[1]), cvtpk(a[2], a[3])};
    }
}

__global__ __launch_bounds__(512, 4) void k_pre(const u16* __restrict__ x, const u16* __restrict__ wq, const u16* __restrict__ wk,
                                                 const u16* __restrict__ wv, const u16* __restrict__ w1, const u16* __restrict__ w3,
                                                 const u16* __restrict__ w2, const u16* __restrict__ wo, char* __restrict__ ws) {
  extern __shared__ __attribute__((aligned(16))) char lds[];
  const int b = blockIdx.x;
  if (b < 128) ln_block(x, wk, wv, ws, lds, b);
  else if (b < 384) q_block(x, wq, ws, lds, b - 128);
  else {
    const int lane = threadIdx.x & 63, w = __builtin_amdgcn_readfirstlane(threadIdx.x >> 6), rb = b - 384;
    const unsigned lds0 = (unsigned)(uintptr_t)(LAS char*)lds;
    const int u1 = rb * 8 + w, u2 = 1024 + rb * 2 + w;
    const bool two = w < 2 && u2 < 1184;
    retile_issue(wo, w1, w3, w2, lds0 + w * 6144, u1, lane);
    if (two) retile_issue(wo, w1, w3, w2, lds0 + 49152 + w * 6144, u2, lane);
    vmwait<0>();
    retile_finish(ws, (const LAS char*)lds + w * 6144, u1, lane);
    if (two) retile_finish(ws, (const LAS char*)lds + 49152 + w * 6144, u2, lane);
  }
}

DI void arrive(char* ws, int i) {
  __hip_atomic_fetch_add((GAS unsigned*)(ws + WS_CNT) + i * 16, 1u, RLX_AGENT);
}
DI GAS unsigned* flagp(char* ws, int i) {
  return (GAS unsigned*)(ws + WS_CNT) + i * 16;
}
DI void wait_count(GAS unsigned* cnt, unsigned want) {
  for (unsigned spins = 0; spins < (1u << 22); ++spins) {
    if ((unsigned)__builtin_amdgcn_readfirstlane(__hip_atomic_load(cnt, RLX_AGENT)) >= want) break;
    __builtin_amdgcn_s_sleep(1);
  }
}

constexpr int KV_A = LDS_BYTES - KV_SLOT, KV_B = 0, KV_Cs = KV_SLOT, O_STG = 2 * KV_SLOT;

DI void attn_block(char* __restrict__ ws, char* lds, int b) {
  const int tid = threadIdx.x, lane = tid & 63, w = __builtin_amdgcn_readfirstlane(tid >> 6);
  const int bi = b >> 3;
  const int tt = (b & 7) * 4 + (bi & 3), hg = bi >> 2;
  const int r = lane & 31, h = lane >> 5;
  const unsigned lds0 = (unsigned)(uintptr_t)(LAS char*)lds;
  const unsigned lane16 = lane * 16;
  __builtin_amdgcn_s_setprio(3);
  {
    const char* qg = ws + WS_Q + (size_t)(tt * 8 + hg) * 10240;
    glds16s(qg + w * 1024, lane16, lds0 + O_STG + w * 1024);
    if (w < 3) glds16s(w < 2 ? qg + 8192 + w * 1024 : ws + WS_KMAX, lane16, lds0 + O_STG + 8192 + w * 1024);
#pragma unroll
    for (int j = 0; j < 3; ++j) {
      const int pc = (7 - w) + 8 * j;
      if (pc < KV_PIECES) glds16s(ws + WS_KV + pc * 1024, lane16, lds0 + KV_A + pc * 1024);
    }
  }
  if (w >= 4) {
    const int lw = w - 4;
    const char* kvg = ws + WS_KV + lw * 1024;
    auto dma = [&](int chunk, int slotoff) {
#pragma unroll
      for (int j = 0; j < 6; ++j)
        if (j < 5 || lw < KV_PIECES - 20) glds16s(kvg + chunk * KV_CHUNK + j * 4096, lane16, lds0 + slotoff + (lw + 4 * j) * 1024);
    };
    dma(1, KV_B);
    vmwait<5>();
    asm volatile("s_barrier" ::: "memory");
    int fre = KV_Cs, nx = KV_A, nn = KV_B;
#pragma unroll 1
    for (int c = 0; c < 8; ++c) {
      asm volatile("s_waitcnt vmcnt(0)\n\ts_barrier" ::: "memory");
      if (c + 2 < 8) dma(c + 2, fre);
      const int t = fre; fre = nx; nx = nn; nn = t;
    }
  } else {
    asm volatile("s_waitcnt vmcnt(0)\n\ts_barrier" ::: "memory");
    __builtin_amdgcn_s_setprio(APRIO);
    u32x4 q1a, q1b; u32x2 q2a, q2b;
    {
      const LAS char* qs = (const LAS char*)lds + O_STG + r * 160 + w * 40;
      const u32x2 a0 = *(const LAS u32x2*)(qs + 16 * h), a1 = *(const LAS u32x2*)(qs + 16 * h + 8);
      const u32x2 b0 = *(const LAS u32x2*)(qs + 5120 + 16 * h), b1 = *(const LAS u32x2*)(qs + 5120 + 16 * h + 8);
      q1a = (u32x4){a0[0], a0[1], a1[0], a1[1]}; q1b = (u32x4){b0[0], b0[1], b1[0], b1[1]};
      q2a = *(const LAS u32x2*)(qs + 32); q2b = *(const LAS u32x2*)(qs + 5120 + 32);
    }
    float km;
    {
      const f32x4 km4 = *(const LAS f32x4*)((LAS char*)lds + O_STG + 10240 + lane * 16);
      int k = __float_as_int(fmaxf(fmaxf(km4[0], km4[1]), fmaxf(km4[2], km4[3])));
      k = max(k, __builtin_amdgcn_update_dpp(0, k, 0x128, 0xf, 0xf, false)); k = max(k, __builtin_amdgcn_update_dpp(0, k, 0x124, 0xf, 0xf, false));
      k = max(k, __builtin_amdgcn_update_dpp(0, k, 0x122, 0xf, 0xf, false)); k = max(k, __builtin_amdgcn_update_dpp(0, k, 0x121, 0xf, 0xf, false));
      km = __int_as_float(max(__builtin_amdgcn_readlane(k, 0), __builtin_amdgcn_readlane(k, 16))) +
           __int_as_float(max(__builtin_amdgcn_readlane(k, 32), __builtin_amdgcn_readlane(k, 48)));
    }
    float qna = dot2(q1a[0], q1a[0], dot2(q1a[1], q1a[1], dot2(q1a[2], q1a[2], dot2(q1a[3], q1a[3], 0.f))));
    float qnb = dot2(q1b[0], q1b[0], dot2(q1b[1], q1b[1], dot2(q1b[2], q1b[2], dot2(q1b[3], q1b[3], 0.f))));
    if (h == 0) { qna = dot2(q2a[0], q2a[0], dot2(q2a[1], q2a[1], qna)); qnb = dot2(q2b[0], q2b[0], dot2(q2b[1], q2b[1], qnb)); }
    { const auto sw = __builtin_amdgcn_permlane32_swap(__float_as_uint(qna), __float_as_uint(qna), false, false); qna = __uint_as_float(sw[0]) + __uint_as_float(sw[1]); }
    { const auto sw = __builtin_amdgcn_permlane32_swap(__float_as_uint(qnb), __float_as_uint(qnb), false, false); qnb = __uint_as_float(sw[0]) + __uint_as_float(sw[1]); }
    const float bda = __builtin_amdgcn_sqrtf(qna * km) * 1.02f + 1e-3f, bdb = __builtin_amdgcn_sqrtf(qnb * km) * 1.02f + 1e-3f;
    const bool fast = __all(bda <= 48.f && bdb <= 48.f);
    if (h == 1) {
      q2a[0] = fast ? (cvtpk(-bda, 0.f) & 0xffffu) : 0u; q2a[1] = 0u;
      q2b[0] = fast ? (cvtpk(-bdb, 0.f) & 0xffffu) : 0u; q2b[1] = 0u;
    }
    const bf16x8 QA1 = __builtin_bit_cast(bf16x8, q1a), QB1 = __builtin_bit_cast(bf16x8, q1b);
    const s16x4 QA2 = __builtin_bit_cast(s16x4, q2a), QB2 = __builtin_bit_cast(s16x4, q2b);
    const int dv = r <= 20 ? r : 21;
    const LAS char* k1b = (const LAS char*)lds + lane * 16;
    const LAS char* k2b = (const LAS char*)lds + (h ? KV_C : KV_K2 + r * 8);
    const LAS char* vb = (const LAS char*)lds + KV_V + (h * VR + dv) * 16;
    f32x16 oa = {}, ob = {};
    const f32x16 zero16 = {};
    if (fast) {
      f32x16 sA, sB;
      bf16x8 k1f, vf0, vf1; s16x4 k2f;
      u32x4 pa, pb;
      float e14, e15;
      k1f = *(const LAS bf16x8*)(k1b + KV_A); k2f = *(const LAS s16x4*)(k2b + KV_A); vf0 = *(const LAS bf16x8*)(vb + KV_A);
      sA = MFMA32(k1f, QA1, zero16); sA = MFMA32K8(k2f, QA2, sA);
      vf1 = (bf16x8){0, 0, 0, 0, 0, 0, 0, 0}; pb = (u32x4){0u, 0u, 0u, 0u}; e14 = 0.f; e15 = 0.f;
      int cur = KV_A, nxt = KV_B, fre = KV_Cs;
#define STEP(C, N, Q1, Q2, OC, OP, V0, L1, L2, L3, L4) do { \
        N = MFMA32(k1f, Q1, zero16); SBAR(); L1; SBAR(); \
        C[0] = EXP2(C[0]); C[1] = EXP2(C[1]); SBAR(); pb[3] = cvtpk(e14, e15); PIN(pb); SBAR(); \
        C[2] = EXP2(C[2]); C[3] = EXP2(C[3]); SBAR(); pa[0] = cvtpk(C[0], C[1]); PIN(pa); SBAR(); \
        OP = MFMA32(vf1, __builtin_bit_cast(bf16x8, pb), OP); SBAR(); L2; SBAR(); \
        C[4] = EXP2(C[4]); C[5] = EXP2(C[5]); SBAR(); pa[1] = cvtpk(C[2], C[3]); PIN(pa); SBAR(); \
        C[6] = EXP2(C[6]); C[7] = EXP2(C[7]); SBAR(); pa[2] = cvtpk(C[4], C[5]); PIN(pa); SBAR(); \
        N = MFMA32K8(k2f, Q2, N); SBAR(); L3; SBAR(); \
        C[8] = EXP2(C[8]); C[9] = EXP2(C[9]); SBAR(); pa[3] = cvtpk(C[6], C[7]); PIN(pa); SBAR(); \
        C[10] = EXP2(C[10]); C[11] = EXP2(C[11]); SBAR(); pb[0] = cvtpk(C[8], C[9]); PIN(pb); SBAR(); \
        OC = MFMA32(V0, __builtin_bit_cast(bf16x8, pa), OC); SBAR(); L4; SBAR(); \
        C[12] = EXP2(C[12]); C[13] = EXP2(C[13]); SBAR(); pb[1] = cvtpk(C[10], C[11]); PIN(pb); SBAR(); \
        e14 = EXP2(C[14]); e15 = EXP2(C[15]); SBAR(); pb[2] = cvtpk(C[12], C[13]); PIN(pb); PIN(e14); PIN(e15); SBAR(); \
      } while (0)
#define KT(i) do { \
        STEP(sA, sB, QB1, QB2, oa, ob, vf0, k1f = *(const LAS bf16x8*)((i) < 7 ? kc1 + ((i) + 1) * KV_TILE : k1b + nxt), \
             vf1 = *(const LAS bf16x8*)(vc + (i) * KV_TILE + 2 * VR * 16), k2f = *(const LAS s16x4*)((i) < 7 ? kc2 + ((i) + 1) * KV_TILE : k2b + nxt), (void)0); \
        STEP(sB, sA, QA1, QA2, ob, oa, vf0, (void)0, (void)0, (void)0, vf0 = *(const LAS bf16x8*)((i) < 7 ? vc + ((i) + 1) * KV_TILE : vb + nxt)); \
      } while (0)
#pragma unroll 1
      for (int c = 0; c < 8; ++c) {
        const LAS char* kc1 = k1b + cur; const LAS char* kc2 = k2b + cur; const LAS char* vc = vb + cur;
        KT(0); KT(1); KT(2);
        asm volatile("s_barrier" ::: "memory");
        KT(3); KT(4); KT(5); KT(6); KT(7);
        const int t = cur; cur = nxt; nxt = fre; fre = t;
      }
      pb[3] = cvtpk(e14, e15);
      ob = MFMA32(vf1, __builtin_bit_cast(bf16x8, pb), ob);
#undef KT
#undef STEP
    } else {
      float ma = -INFINITY, mb = -INFINITY;
      int cur = KV_A, nxt = KV_B, fre = KV_Cs;
#pragma unroll 1
      for (int c = 0; c < 8; ++c) {
#pragma unroll 1
        for (int i = 0; i < 8; ++i) {
          if (i == 3) RAWBAR();
          const int off = cur + i * KV_TILE;
          const bf16x8 k1f = *(const LAS bf16x8*)(k1b + off); const s16x4 k2f = *(const LAS s16x4*)(k2b + off);
          const bf16x8 vf0 = *(const LAS bf16x8*)(vb + off), vf1 = *(const LAS bf16x8*)(vb + off + 2 * VR * 16);
#pragma unroll
          for (int qs = 0; qs < 2; ++qs) {
            f32x16 sc = MFMA32(k1f, qs ? QB1 : QA1, zero16); sc = MFMA32K8(k2f, qs ? QB2 : QA2, sc);
            float& m = qs ? mb : ma; f32x16& o = qs ? ob : oa;
            float mx = sc[0];
#pragma unroll
            for (int j = 1; j < 16; ++j) mx = fmaxf(mx, sc[j]);
            mx = fmaxf(mx, __shfl_xor(mx, 32));
            const float mn = fmaxf(m, mx);
            const float al = EXP2(m - mn);
            m = mn;
#pragma unroll
            for (int j = 0; j < 16; ++j) { o[j] *= al; sc[j] = EXP2(sc[j] - mn); }
            const u32x4 pa = {cvtpk(sc[0], sc[1]), cvtpk(sc[2], sc[3]), cvtpk(sc[4], sc[5]), cvtpk(sc[6], sc[7])};
            const u32x4 pb = {cvtpk(sc[8], sc[9]), cvtpk(sc[10], sc[11]), cvtpk(sc[12], sc[13]), cvtpk(sc[14], sc[15])};
            o = MFMA32(vf0, __builtin_bit_cast(bf16x8, pa), o);
            o = MFMA32(vf1, __builtin_bit_cast(bf16x8, pb), o);
          }
        }
        const int t = cur; cur = nxt; nxt = fre; fre = t;
      }
    }
    PIN(oa); PIN(ob);
#pragma unroll
    for (int qs = 0; qs < 2; ++qs) {
      const f32x16 o = qs ? ob : oa;
      const auto sw = __builtin_amdgcn_permlane32_swap(__float_as_uint(o[8]), __float_as_uint(o[8]), false, false);
      const float il = __builtin_amdgcn_rcpf(__uint_as_float(sw[1]));
      LAS char* os = (LAS char*)lds + O_STG + (qs * 32 + r) * 176 + w * 40 + 8 * h;
      *(LAS u32x2*)(os) = (u32x2){cvtpk(o[0] * il, o[1] * il), cvtpk(o[2] * il, o[3] * il)};
      *(LAS u32x2*)(os + 16) = (u32x2){cvtpk(o[4] * il, o[5] * il), cvtpk(o[6] * il, o[7] * il)};
      if (h == 0) *(LAS u32x2*)(os + 32) = (u32x2){cvtpk(o[8] * il, o[9] * il), cvtpk(o[10] * il, o[11] * il)};
    }
  }
  RAWBAR();
  const __amdgpu_buffer_rsrc_t orr = __builtin_amdgcn_make_buffer_rsrc(ws + WS_O, 0, S * D * 2, 0x00020000);
#pragma unroll
  for (int j = 0; j < 2; ++j) {
    const int ci = tid + 512 * j;
    if (ci < 640) {
      const int c = ci >> 6, cc = hg * 10 + c;
      const u32x4 v = *(const LAS u32x4*)((LAS char*)lds + O_STG + lane * 176 + c * 16);
      __builtin_amdgcn_raw_buffer_store_b128(v, orr, ((tt * 20 + (cc >> 2)) * 4 + (lane >> 4)) * 1024 + ((cc & 3) * 16 + (lane & 15)) * 16, 0, 16);
    }
  }
  vmwait<0>();
  RAWBAR();
  if (tid == 0) arrive(ws, 16 + tt);
}

struct Frag3 { bf16x8 x; bf16x8 w[3]; };
struct Frag5 { bf16x8 x; bf16x8 w[5]; };
DI void ffn_block(const u16* __restrict__ x, char* __restrict__ ws, u16* __restrict__ out, char* lds, int b) {
  const int tid = threadIdx.x, lane = tid & 63, w = __builtin_amdgcn_readfirstlane(tid >> 6);
  const int bi = b >> 3;
  const unsigned lds0 = (unsigned)(uintptr_t)(LAS char*)lds, lane16 = lane * 16;
  __builtin_amdgcn_s_setprio(GPRIO);
  {
    const int tp = ((b & 7) >> 2) * 8 + (bi & 7), cg = (b & 3) * 4 + (bi >> 3);
    const char* xsb = ws + WS_XN + (size_t)tp * 163840 + w * 1024;
    const char* wsrc = ws + WS_W13 + (size_t)cg * 245760 + w * 1536;
    auto piece = [&](int kt, int slot, int j) {
      if (j == 0) glds16s(xsb + kt * 8192, lane16, lds0 + slot * SLOT + w * 1024);
      else glds_lin<48>(wsrc + kt * 12288 + (j - 1) * 768, lane16, lds0 + slot * SLOT + 8192 + w * 1536 + (j - 1) * 768);
    };
#pragma unroll
    for (int i = 0; i < 4; ++i) { piece(i, i, 0); piece(i, i, 1); piece(i, i, 2); }
    const int tq = w & 3, hc = w >> 2, r = lane & 31, h = lane >> 5;
    const int xrow = tq * 32 + r;
    const LAS char* xb0 = (const LAS char*)lds + xrow * 64 + ((h ^ ((xrow >> 2) & 3)) * 16);
    const LAS char* xb1 = (const LAS char*)lds + xrow * 64 + (((2 + h) ^ ((xrow >> 2) & 3)) * 16);
    const LAS char* wb = (const LAS char*)lds + 8192 + hc * 3072 + lane * 16;
    f32x16 acc[3] = {};
    auto rd = [&](Frag3& f, int slot, int s) {
      f.x = *(const LAS bf16x8*)((s ? xb1 : xb0) + slot * SLOT);
#pragma unroll
      for (int c = 0; c < 3; ++c) f.w[c] = *(const LAS bf16x8*)(wb + slot * SLOT + s * 6144 + c * 1024);
    };
    auto mm = [&](const Frag3& f, int s, int dk, int dsl) {
#pragma unroll
      for (int c = 0; c < 3; ++c) {
        acc[c] = MFMA32(f.w[c], f.x, acc[c]);
        SBAR();
        if (dk >= 0) { if (s == 1 && c == 0) piece(dk, dsl, 0); if (s == 1 && c == 2) piece(dk, dsl, 1); if (s == 0 && c == 0) piece(dk, dsl, 2); }
        SBAR();
      }
    };
    Frag3 f0, f1;
    vmwait<9>();
    RAWBAR();
    rd(f0, 0, 0);
#define IT(kt, sl, VM, PREV, DMA, NEXT) do { \
      rd(f1, sl, 1); SBAR(); mm(f0, 0, PREV ? (kt) + 3 : -1, ((sl) + 3) & 3); SBAR(); \
      if (NEXT) { vmwait<VM>(); RAWBAR(); rd(f0, ((sl) + 1) & 3, 0); } \
      SBAR(); mm(f1, 1, DMA ? (kt) + 4 : -1, sl); SBAR(); } while (0)
    IT(0, 0, 6, false, true, true); IT(1, 1, 6, true, true, true); IT(2, 2, 6, true, true, true); IT(3, 3, 6, true, true, true);
#pragma unroll 1
    for (int kt = 4; kt < 16; kt += 4) {
      IT(kt, 0, 6, true, true, true); IT(kt + 1, 1, 6, true, true, true); IT(kt + 2, 2, 6, true, true, true); IT(kt + 3, 3, 6, true, true, true);
    }
    IT(16, 0, 6, true, false, true); IT(17, 1, 3, false, false, true); IT(18, 2, 0, false, false, true); IT(19, 3, 0, false, false, false);
#undef IT
    PIN(acc[0]); PIN(acc[1]); PIN(acc[2]);
    __builtin_amdgcn_s_setprio(3);
    u32x4 hv[3];
#pragma unroll
    for (int c = 0; c < 3; ++c) {
      float v[8];
#pragma unroll
      for (int j = 0; j < 8; ++j) {
        const float gt = acc[c][j], up = acc[c][j + 8];
        v[j] = gt * __builtin_amdgcn_rcpf(1.f + EXP2(-1.4426950408889634f * gt)) * up;
      }
      hv[c] = (u32x4){cvtpk(v[0], v[1]), cvtpk(v[2], v[3]), cvtpk(v[4], v[5]), cvtpk(v[6], v[7])};
    }
#pragma unroll
    for (int c = 0; c < 3; ++c) *(LAS u32x4*)((LAS char*)lds + xrow * 208 + hc * 96 + c * 32 + h * 16) = hv[c];
    RAWBAR();
    const __amdgpu_buffer_rsrc_t hr = __builtin_amdgcn_make_buffer_rsrc(ws + WS_H, 0, S * F * 2, 0x00020000);
#pragma unroll
    for (int j = 0; j < 3; ++j) {
      const int ci = tid + 512 * j, row = ci / 12, pos = ci - row * 12;
      const u32x4 v = *(const LAS u32x4*)((LAS char*)lds + row * 208 + pos * 16);
      __builtin_amdgcn_raw_buffer_store_b128(v, hr, ((tp * 128 + row) * F + cg * 96 + pos * 8) * 2, 0, 16);
    }
    vmwait<0>();
    RAWBAR();
    if (tid == 448) arrive(ws, tp);
  }

  const int tt = (b & 7) * 4 + (bi & 3), t0 = tt * 64, cg = bi >> 2, c0 = cg * 80;
  const int ts = w & 3, kh = w >> 2, r16 = lane & 15, gq = lane >> 4, xrow = ts * 16 + r16;
  f32x4 acc[5] = {};
  const char* wog = ws + WS_WO + (size_t)cg * 102400;
  bf16x8 wr[10];
  auto wop = [&](int j) { glds16s(wog + (j >= 5 ? 10240 : 0) + (j * 8 + w) * 1024, lane16, lds0 + (j * 8 + w) * 1024); };
  {
    const int srow = w * 8 + (lane >> 3);
    const unsigned hs = (srow * F + ((lane & 7) ^ ((srow >> 1) & 7)) * 8) * 2;
    const u16* hb = (const u16*)(ws + WS_H) + (size_t)t0 * F;
    const char* wsrc = ws + WS_W2 + (size_t)cg * 245760 + w * 1280;
    auto piece = [&](int kt, int slot, int j) {
      if (j == 0) glds16s_sc1(hb + kt * 64, hs, lds0 + slot * ST64 + w * 1024);
      else glds_lin<40>(wsrc + kt * 10240 + (j - 1) * 640, lane16, lds0 + slot * ST64 + 8192 + w * 1280 + (j - 1) * 640);
    };
#pragma unroll
    for (int i = 0; i < 4; ++i) { piece(i, i, 1); piece(i, i, 2); }
    if (kh == 0) {
#pragma unroll
      for (int c = 0; c < 5; ++c) {
        const u32x2 xv = *(const u32x2*)(x + (size_t)(t0 + xrow) * D + c0 + c * 16 + gq * 4);
        acc[c] = (f32x4){bflo(xv[0]), bfhi(xv[0]), bflo(xv[1]), bfhi(xv[1])};
        PIN(acc[c]);
      }
    }
    if (w == 0) wait_count(flagp(ws, tt >> 1), 16u);
    RAWBAR();
    __builtin_amdgcn_s_setprio(GPRIO);
#pragma unroll
    for (int i = 0; i < 4; ++i) piece(i, i, 0);
    const LAS char* xb = (const LAS char*)lds + xrow * 128 + (((kh * 4 + gq) ^ ((xrow >> 1) & 7)) * 16);
    const LAS char* wb = (const LAS char*)lds + 8192 + kh * 5120 + lane * 16;
    auto rd = [&](Frag5& f, int slot) {
      f.x = *(const LAS bf16x8*)(xb + slot * ST64);
#pragma unroll
      for (int c = 0; c < 5; ++c) f.w[c] = *(const LAS bf16x8*)(wb + slot * ST64 + c * 1024);
    };
    auto mm = [&](const Frag5& f, int dk, int dsl) {
#pragma unroll
      for (int c = 0; c < 5; ++c) { acc[c] = MFMA16(f.w[c], f.x, acc[c]); SBAR(); if (dk >= 0 && !(c & 1)) piece(dk, dsl, c >> 1); SBAR(); }
    };
    Frag5 fa, fb;
    vmwait<3>();
    RAWBAR();
    rd(fa, 0);
#define IT(kt, sl, FC, FN, VM, DMA) do { vmwait<VM>(); RAWBAR(); rd(FN, ((sl) + 1) & 3); SBAR(); mm(FC, DMA ? (kt) + 4 : -1, sl); SBAR(); } while (0)
    IT(0, 0, fa, fb, 2, true); IT(1, 1, fb, fa, 4, true); IT(2, 2, fa, fb, 6, true); IT(3, 3, fb, fa, 6, true);
#pragma unroll 1
    for (int kt = 4; kt < 20; kt += 4) {
      IT(kt, 0, fa, fb, 6, true); IT(kt + 1, 1, fb, fa, 6, true); IT(kt + 2, 2, fa, fb, 6, true); IT(kt + 3, 3, fb, fa, 6, true);
    }
    IT(20, 0, fa, fb, 6, false); wop(0); wop(1); wop(9);
    {
      const char* wrp = wog + kh * 51200 + 40960;
#pragma unroll
      for (int j = 0; j < 10; ++j) asm volatile("global_load_dwordx4 %0, %1, %2" : "=v"(wr[j]) : "v"(lane16), "s"(wrp + j * 1024) : "memory");
    }
    IT(21, 1, fb, fa, 16, false); wop(2); wop(3);
    IT(22, 2, fa, fb, 15, false); wop(4); wop(5);
    mm(fb, -1, 0);
#undef IT
  }
  PIN(acc[0]);
  RAWBAR();
  wop(6); wop(7); wop(8);
  asm volatile("s_waitcnt vmcnt(0)" : "+v"(wr[0]), "+v"(wr[1]), "+v"(wr[2]), "+v"(wr[3]), "+v"(wr[4]), "+v"(wr[5]), "+v"(wr[6]), "+v"(wr[7]), "+v"(wr[8]), "+v"(wr[9]) :: "memory");
  vmwait<0>();
  __builtin_amdgcn_s_setprio(3);
  const LAS char* wbt = (const LAS char*)lds + kh * 40960 + lane * 16;
  if (w == 0) wait_count(flagp(ws, 16 + tt), 8u);
  RAWBAR();

  {
    u32x4 of[10];
    const char* op = ws + WS_O + ((tt * 20 + kh * 10) * 4 + ts) * 1024;
#pragma unroll
    for (int s = 0; s < 10; ++s) asm volatile("global_load_dwordx4 %0, %1, %2 sc1" : "=v"(of[(s + 8) % 10]) : "v"(lane16), "s"(op + ((s + 8) % 10) * 4096) : "memory");
#define OSTEP(s, N, WC, WN) do { \
      if ((s) + 1 < 8) { _Pragma("unroll") for (int c = 0; c < 5; ++c) WN[c] = *(const LAS bf16x8*)(wbt + ((s) + 1) * 5120 + c * 1024); } \
      SBAR(); asm volatile("s_waitcnt vmcnt(" #N ")" : "+v"(of[s]) :: "memory"); SBAR(); \
      _Pragma("unroll") for (int c = 0; c < 5; ++c) acc[c] = MFMA16(WC[c], __builtin_bit_cast(bf16x8, of[s]), acc[c]); SBAR(); } while (0)
    asm volatile("s_waitcnt vmcnt(9)" : "+v"(of[8]) :: "memory");
#pragma unroll
    for (int c = 0; c < 5; ++c) acc[c] = MFMA16(wr[c], __builtin_bit_cast(bf16x8, of[8]), acc[c]);
    SBAR();
    bf16x8 w0[5];
#pragma unroll
    for (int c = 0; c < 5; ++c) w0[c] = *(const LAS bf16x8*)(wbt + c * 1024);
    SBAR(); asm volatile("s_waitcnt vmcnt(8)" : "+v"(of[9]) :: "memory"); SBAR();
#pragma unroll
    for (int c = 0; c < 5; ++c) acc[c] = MFMA16(wr[5 + c], __builtin_bit_cast(bf16x8, of[9]), acc[c]);
    SBAR();
    bf16x8 wn[5];
    OSTEP(0, 7, w0, wn); OSTEP(1, 6, wn, w0); OSTEP(2, 5, w0, wn); OSTEP(3, 4, wn, w0);
    OSTEP(4, 3, w0, wn); OSTEP(5, 2, wn, w0); OSTEP(6, 1, w0, wn); OSTEP(7, 0, wn, w0);
#undef OSTEP
  }
  RAWBAR();
#pragma unroll
  for (int c = 0; c < 5; ++c) *(LAS f32x4*)((LAS char*)lds + kh * 64 * 336 + xrow * 336 + c * 64 + gq * 16) = acc[c];
  RAWBAR();
#pragma unroll
  for (int j = 0; j < 2; ++j) {
    const int ci = tid + 512 * j;
    if (ci < 640) {
      const int row = ci / 10, c = ci - row * 10;
      const LAS f32x4* a0 = (const LAS f32x4*)((LAS char*)lds + row * 336 + c * 32);
      const LAS f32x4* a1 = (const LAS f32x4*)((LAS char*)lds + 64 * 336 + row * 336 + c * 32);
      const f32x4 lo = a0[0] + a1[0], hi = a0[1] + a1[1];
      const u32x4 res = {cvtpk(lo[0], lo[1]), cvtpk(lo[2], lo[3]), cvtpk(hi[0], hi[1]), cvtpk(hi[2], hi[3])};
      *(u32x4*)(out + (size_t)(t0 + row) * D + c0 + c * 8) = res;
    }
  }
}

__global__ __launch_bounds__(512, 4) void k_mid(const u16* __restrict__ x, char* __restrict__ ws, u16* __restrict__ out) {
  extern __shared__ __attribute__((aligned(16))) char lds[];
  if (blockIdx.x < 256) attn_block(ws, lds, blockIdx.x);
  else ffn_block(x, ws, out, lds, blockIdx.x - 256);
}

extern "C" void kernel_launch(void* const* d_in, const int* in_sizes, int n_in, void* d_out, int out_size, void* d_ws, size_t ws_size,
                              hipStream_t stream) {
  const u16* x = (const u16*)d_in[0];
  const u16* wq = (const u16*)d_in[1];
  const u16* wk = (const u16*)d_in[2];
  const u16* wv = (const u16*)d_in[3];
  const u16* wo = (const u16*)d_in[4];
  const u16* w1 = (const u16*)d_in[6];
  const u16* w3 = (const u16*)d_in[7];
  const u16* w2 = (const u16*)d_in[8];
  char* ws = (char*)d_ws;
  (void)hipFuncSetAttribute((const void*)k_mid, hipFuncAttributeMaxDynamicSharedMemorySize, LDS_BYTES);
  (void)hipFuncSetAttribute((const void*)k_pre, hipFuncAttributeMaxDynamicSharedMemorySize, LDS_BYTES);
  k_pre<<<512, 512, LDS_BYTES, stream>>>(x, wq, wk, wv, w1, w3, w2, wo, ws);
  k_mid<<<512, 512, LDS_BYTES, stream>>>(x, ws, (u16*)d_out);
}
```

```cpp
#include <hip/hip_runtime.h>
#include <hip/hip_bf16.h>
#include <stdint.h>

typedef short bf16x8 __attribute__((ext_vector_type(8)));
typedef short s16x4 __attribute__((ext_vector_type(4)));
typedef float f32x2 __attribute__((ext_vector_type(2)));
typedef float f32x4 __attribute__((ext_vector_type(4)));
typedef float f32x16 __attribute__((ext_vector_type(16)));
typedef unsigned u32x4 __attribute__((ext_vector_type(4)));
typedef unsigned u32x2 __attribute__((ext_vector_type(2)));
typedef __bf16 bf16x2_t __attribute__((ext_vector_type(2)));
typedef unsigned short u16;
#define LAS __attribute__((address_space(3)))
#define GAS __attribute__((address_space(1)))
#define RLX_AGENT __ATOMIC_RELAXED, __HIP_MEMORY_SCOPE_AGENT
#define DI __device__ __forceinline__
#define SBAR() __builtin_amdgcn_sched_barrier(0)
#define PIN(x) asm volatile("" : "+v"(x))

#define GPRIO 1
#define LADDER 1
#define WT 0
#define QPRIO 0
#define P2PRIO GPRIO
#define G1LATE 99
#define APRIO 2
constexpr int S = 2048, D = 640, H = 32, DH = 20, F = 1536;
constexpr int VR = 22;
constexpr int KV_K2 = 1024, KV_C = 1280, KV_V = 1296, KV_TILE = KV_V + 4 * VR * 16;
constexpr int KV_CHUNK = 8 * KV_TILE;
constexpr int KV_PIECES = (KV_CHUNK + 1023) / 1024, KV_SLOT = KV_PIECES * 1024;
constexpr size_t WS_CNT = 0;
constexpr size_t WS_KMAX = 12288;
constexpr size_t WS_KV = 16384;
constexpr size_t WS_XN = WS_KV + 192 * 1024;
constexpr size_t WS_O = WS_XN + (size_t)S * D * 2;
constexpr size_t WS_H = WS_O + (size_t)S * D * 2;
constexpr size_t WS_Q = WS_H + (size_t)S * F * 2;
constexpr size_t WS_WO = WS_Q + (size_t)S * D * 2;
constexpr size_t WS_W13 = WS_WO + (size_t)D * D * 2;
constexpr size_t WS_W2 = WS_W13 + (size_t)2 * D * F * 2;
constexpr float QSCALE = 0.22360679774997896f * 1.4426950408889634f;
constexpr int LDS_BYTES = 81920, SLOT = 20480;

DI unsigned cvtpk(float a, float b) { f32x2 v = {a, b}; bf16x2_t r = __builtin_convertvector(v, bf16x2_t); return __builtin_bit_cast(unsigned, r); }
DI float bflo(unsigned u) { return __uint_as_float(u << 16); }
DI float bfhi(unsigned u) { return __uint_as_float(u & 0xffff0000u); }
DI float rbf(float a) { return bflo(cvtpk(a, a)); }
DI float dot2(unsigned a, unsigned b, float c) { return __builtin_amdgcn_fdot2_f32_bf16(__builtin_bit_cast(bf16x2_t, a), __builtin_bit_cast(bf16x2_t, b), c, false); }
DI s16x4 trread(const LAS char* p) { return __builtin_bit_cast(s16x4, __builtin_amdgcn_ds_read_tr16_b64_v4i16((LAS s16x4*)p)); }
DI bf16x8 cat8(s16x4 a, s16x4 b) { return __builtin_shufflevector(a, b, 0, 1, 2, 3, 4, 5, 6, 7); }
DI void glds16s(const void* sbase, unsigned voff, unsigned lds_base) {
  unsigned sv;
  asm volatile("s_mov_b32 %0, m0\n\ts_mov_b32 m0, %3\n\ts_nop 0\n\tglobal_load_lds_dwordx4 %1, %2\n\ts_mov_b32 m0, %0" : "=&s"(sv) : "v"(voff), "s"(sbase), "s"(lds_base) : "memory");
}
DI void glds16s_sc1(const void* sbase, unsigned voff, unsigned lds_base) {
  unsigned sv;
  asm volatile("s_mov_b32 %0, m0\n\ts_mov_b32 m0, %3\n\ts_nop 0\n\tglobal_load_lds_dwordx4 %1, %2 sc1\n\ts_mov_b32 m0, %0" : "=&s"(sv) : "v"(voff), "s"(sbase), "s"(lds_base) : "memory");
}
#define ST_DECL
#define ST()
#define ST_OUT(a, b)
#define MFMA32(a, b, c) __builtin_amdgcn_mfma_f32_32x32x16_bf16(a, b, c, 0, 0, 0)
#define MFMA32K8(a, b, c) __builtin_amdgcn_mfma_f32_32x32x8bf16_1k(a, b, c, 0, 0, 0)
#define MFMA16(a, b, c) __builtin_amdgcn_mfma_f32_16x16x32_bf16(a, b, c, 0, 0, 0)
#define EXP2(v) __builtin_amdgcn_exp2f(v)
#define RAWBAR() asm volatile("s_waitcnt lgkmcnt(0)\n\ts_barrier" ::: "memory")
DI void touch(const void* p, unsigned& d) { asm volatile("global_load_dword %0, %1, off" : "+v"(d) : "v"(p) : "memory"); }
DI void st16wt(void* p, u32x4 v) {
  *(u32x4*)p = v;
}
template <int N> DI void vmwait() { asm volatile("s_waitcnt vmcnt(%0)" :: "n"(N) : "memory"); }
DI int wslot(int k) { return k ^ ((k >> 1) & 4); }
DI void ln_scalars(float sum, float sq, float& mean_b, float& rden) {
  const float mean = sum * (1.f / D);
  const float var = fmaxf(sq * (1.f / D) - mean * mean, 0.f);
  mean_b = rbf(mean);
  rden = __builtin_amdgcn_rcpf(rbf(__builtin_amdgcn_sqrtf(rbf(rbf(var) + 1e-5f))));
}

template <int NL> DI void glds_lin(const char* src, unsigned lane16, unsigned dst) {
  unsigned sv;
  asm volatile("s_mov_b32 %0, m0\n\ts_mov_b32 m0, %3\n\ts_mov_b32 exec_hi, %4\n\tglobal_load_lds_dwordx4 %1, %2\n\ts_mov_b32 exec_hi, -1\n\ts_mov_b32 m0, %0"
               : "=&s"(sv) : "v"(lane16), "s"(src), "s"(dst), "n"((1u << (NL - 32)) - 1u) : "memory");
}

constexpr int ST64 = 18432;

DI void retile_issue(const u16* __restrict__ wo, const u16* __restrict__ w1, const u16* __restrict__ w3, const u16* __restrict__ w2,
                     unsigned ldsu, int u, int lane) {
#pragma unroll
  for (int j = 0; j < 3; ++j) {
    const int ci = j * 64 + lane, rr = ci / 12, pos = ci - rr * 12;
    if (u < 640) {
      const int cg = u / 40, ks = u - cg * 40;
      glds16s(w1 + (size_t)ks * 16 * F + cg * 96, (rr * F + pos * 8) * 2, ldsu + j * 1024);
      glds16s(w3 + (size_t)ks * 16 * F + cg * 96, (rr * F + pos * 8) * 2, ldsu + 3072 + j * 1024);
    } else {
      const int i = u < 800 ? u - 640 : u - 800, nk = u < 800 ? 20 : 48, cg = i / nk, ks = i - cg * nk, c0 = cg * 80 - (cg & 1) * 16;
      const u16* src = (u < 800 ? wo : w2) + (size_t)ks * 32 * D + c0;
      const unsigned vo = (rr * D + (c0 + pos * 8 < D ? pos : 0) * 8) * 2;
      glds16s(src, vo, ldsu + j * 1024);
      glds16s(src + 16 * D, vo, ldsu + 3072 + j * 1024);
    }
  }
}
DI void retile_finish(char* __restrict__ ws, const LAS char* im, int u, int lane) {
  const int h = lane >> 5, q4 = (lane & 15) >> 2, p = lane & 3, blk = (lane >> 4) & 1;
  if (u < 640) {
    const LAS char* a = im + blk * 3072 + (8 * h + q4) * 192 + (((p & 1) << 1) | (p >> 1)) * 8;
    char* dst = ws + WS_W13 + (size_t)u * 6144 + lane * 16;
#pragma unroll
    for (int c = 0; c < 6; ++c) st16wt(dst + c * 1024, __builtin_bit_cast(u32x4, cat8(trread(a + c * 32), trread(a + c * 32 + 768))));
  } else {
    const int i = u < 800 ? u - 640 : u - 800, cg = i / (u < 800 ? 20 : 48);
    const LAS char* a = im + ((lane >> 4) * 8 + q4) * 192 + (cg & 1) * 32 + p * 8;
    char* dst = ws + (u < 800 ? WS_WO : WS_W2) + (size_t)i * 5120 + lane * 16;
#pragma unroll
    for (int c = 0; c < 5; ++c) st16wt(dst + c * 1024, __builtin_bit_cast(u32x4, cat8(trread(a + c * 32), trread(a + c * 32 + 768))));
  }
}

constexpr int LN_ST = 25600, LN_RED = LN_ST + 512, LN_HALF = LN_RED + 8192;
DI void ln_block(const u16* __restrict__ x, const u16* __restrict__ wk, const u16* __restrict__ wv, char* __restrict__ ws, char* ldsb, int b) {
  const int tid = threadIdx.x, lane = tid & 63, w8 = __builtin_amdgcn_readfirstlane(tid >> 6), w = w8 & 3, kv = w8 >> 2;
  const int t16 = (b & 7) * 16 + (b >> 3);
  const int r16 = lane & 15, gq = lane >> 4;
  LAS char* lds = (LAS char*)ldsb + kv * LN_HALF;
  const unsigned lds0 = (unsigned)(uintptr_t)lds;
  const int token = t16 * 16 + r16;
  ST_DECL ST();
  __builtin_amdgcn_s_setprio(3);
  u32x4 xf[5];
  const u16* xp = x + (size_t)token * D + w * 160 + gq * 8;
#pragma unroll
  for (int s = 0; s < 5; ++s) asm volatile("global_load_dwordx4 %0, %1, off offset:%2" : "=v"(xf[s]) : "v"(xp), "i"(s * 64) : "memory");
  const u16* wsrc = kv ? wv : wk;
#pragma unroll
  for (int j = 0; j < 7; ++j)
    if (j < 6 || w == 0) glds16s(wsrc + (w + 4 * j) * 512, lane * 16, lds0 + (w + 4 * j) * 1024);
  ST();
  asm volatile("s_waitcnt vmcnt(0)" : "+v"(xf[0]), "+v"(xf[1]), "+v"(xf[2]), "+v"(xf[3]), "+v"(xf[4]) :: "memory");
  float sum = 0.f, sq = 0.f, sum2 = 0.f, sq2 = 0.f;
#pragma unroll
  for (int s = 0; s < 5; ++s)
#pragma unroll
    for (int j = 0; j < 4; j += 2) {
      sum = dot2(xf[s][j], 0x3F803F80u, sum); sq = dot2(xf[s][j], xf[s][j], sq);
      sum2 = dot2(xf[s][j + 1], 0x3F803F80u, sum2); sq2 = dot2(xf[s][j + 1], xf[s][j + 1], sq2);
    }
  sum += sum2; sq += sq2;
  sum += __shfl_xor(sum, 16); sq += __shfl_xor(sq, 16);
  sum += __shfl_xor(sum, 32); sq += __shfl_xor(sq, 32);
  if (b == 0 && tid < 64) { ((unsigned*)(ws + WS_CNT))[tid * 16] = 0u; ((unsigned*)(ws + WS_CNT + 4096))[tid * 32] = 0u; }
  LAS f32x2* stp = (LAS f32x2*)(lds + LN_ST);
  if (gq == 0) stp[w * 16 + r16] = (f32x2){sum, sq};
  RAWBAR();
  { const f32x2 a = stp[r16], c = stp[16 + r16], d = stp[32 + r16], e = stp[48 + r16]; sum = (a[0] + c[0]) + (d[0] + e[0]); sq = (a[1] + c[1]) + (d[1] + e[1]); }
  float mean_b, rden;
  ln_scalars(sum, sq, mean_b, rden);
  ST();
#pragma unroll
  for (int s = 0; s < 5; ++s)
#pragma unroll
    for (int j = 0; j < 4; ++j) {
      const unsigned d = cvtpk(bflo(xf[s][j]) - mean_b, bfhi(xf[s][j]) - mean_b);
      xf[s][j] = cvtpk(bflo(d) * rden, bfhi(d) * rden);
    }
  if ((w >> 1) == kv) {
    const int row = token & 127;
    char* xn = ws + WS_XN + ((size_t)((token >> 7) * 20 + w * 5) * 128 + row) * 64 + ((gq ^ ((row >> 2) & 3)) * 16);
#pragma unroll
    for (int s = 0; s < 5; ++s) *(u32x4*)(xn + s * 8192) = xf[s];
  }
  const int q4 = (lane & 15) >> 2, p = lane & 3;
  const LAS char* e0 = lds + (w * 160 + gq * 8 + q4) * 40 + p * 8;
  const LAS char* e1 = lds + (w * 160 + gq * 8 + q4) * 40 + 32;
  f32x4 acc[2] = {};
  if (kv == 0) {
#pragma unroll
    for (int s = 0; s < 5; ++s) {
      acc[0] = MFMA16(cat8(trread(e0 + s * 1280), trread(e0 + s * 1280 + 160)), __builtin_bit_cast(bf16x8, xf[s]), acc[0]);
      acc[1] = MFMA16(cat8(trread(e1 + s * 1280), trread(e1 + s * 1280 + 160)), __builtin_bit_cast(bf16x8, xf[s]), acc[1]);
    }
  } else {
#pragma unroll
    for (int s = 0; s < 5; ++s) {
      acc[0] = MFMA16(__builtin_bit_cast(bf16x8, xf[s]), cat8(trread(e0 + s * 1280), trread(e0 + s * 1280 + 160)), acc[0]);
      acc[1] = MFMA16(__builtin_bit_cast(bf16x8, xf[s]), cat8(trread(e1 + s * 1280), trread(e1 + s * 1280 + 160)), acc[1]);
    }
  }
  LAS f32x4* red = (LAS f32x4*)(lds + LN_RED) + lane;
  red[(w * 2) * 64] = acc[0]; red[(w * 2 + 1) * 64] = acc[1];
  RAWBAR();
  ST();
  if (w < 2) {
    const f32x4 a = (red[w * 64] + red[(2 + w) * 64]) + (red[(4 + w) * 64] + red[(6 + w) * 64]);
    const u32x2 v = {cvtpk(a[0], a[1]), cvtpk(a[2], a[3])};
    char* kvp = ws + WS_KV + (t16 >> 1) * KV_TILE;
    if (kv == 0) {
      const int r = token & 31;
      float n2 = dot2(v[0], v[0], dot2(v[1], v[1], 0.f));
      if (w == 0) {
        *(u32x2*)(kvp + (gq >> 1) * 512 + r * 16 + (gq & 1) * 8) = v;
      } else {
        if (gq == 0) *(u32x2*)(kvp + KV_K2 + r * 8) = v;
        else n2 = 0.f;
        if (gq == 1 && r == 0) { const u32x4 c1 = {0x00003F80u, 0u, 0u, 0u}; *(u32x4*)(kvp + KV_C) = c1; }
      }
      n2 += __shfl_xor(n2, 16); n2 += __shfl_xor(n2, 32);
#pragma unroll
      for (int o = 1; o < 16; o <<= 1) n2 = fmaxf(n2, __shfl_xor(n2, o));
      if (lane == 0) ((float*)(ws + WS_KMAX))[w * 128 + t16] = n2;
    } else {
      char* vp = kvp + KV_V + (((t16 & 1) * 2 + (gq & 1)) * VR) * 16 + (gq >> 1) * 8;
      if (w == 0) *(u32x2*)(vp + r16 * 16) = v;
      else if (r16 < 6) {
        const u32x2 one = {0x3F803F80u, 0x3F803F80u}, zer = {0u, 0u};
        *(u32x2*)(vp + (16 + r16) * 16) = r16 < 4 ? v : (r16 == 4 ? one : zer);
      }
    }
  }
  ST(); ST_OUT(ws, 0);
}

struct FragT { bf16x8 x; s16x4 w[10]; };
DI void q_block(const u16* __restrict__ x, const u16* __restrict__ wq, char* __restrict__ ws, char* lds, int b) {
  const int tid = threadIdx.x, lane = tid & 63, w = __builtin_amdgcn_readfirstlane(tid >> 6);
  const int bi = b >> 3, tt = (b & 7) * 4 + (bi & 3), cg = bi >> 2, odd = cg & 1, c0 = cg * 80 - odd * 16;
  const int ts = w & 3, kh = w >> 2, r16 = lane & 15, gq = lane >> 4, q4 = (lane & 15) >> 2, p = lane & 3;
  const unsigned lds0 = (unsigned)(uintptr_t)(LAS char*)lds;
  ST_DECL ST();
  const int srow = w * 8 + (lane >> 3);
  const unsigned as = (srow * D + ((lane & 7) ^ ((srow >> 1) & 7)) * 8) * 2;
  const u16* ab = x + (size_t)tt * 64 * D;
  const int rr = lane / 12, ch = (lane - rr * 12) ^ ((w & 1) << 1);
  const unsigned bs = ((rr & 3) * D + (c0 + ch * 8 < D ? ch : 0) * 8) * 2;
  const u16* bb = wq + (size_t)w * 8 * D + c0;
  auto piece = [&](int kt, int j) {
    const int slot = kt & 3;
    if (j == 0) glds16s(ab + kt * 64, as, lds0 + slot * SLOT + w * 1024);
    else glds_lin<48>((const char*)(bb + (size_t)(kt * 64 + (j - 1) * 4) * D), bs, lds0 + slot * SLOT + 8192 + (2 * w + j - 1) * 768);
  };
  if (QPRIO) __builtin_amdgcn_s_setprio(QPRIO);
  unsigned pf = 0;
#pragma unroll
  for (int i = 0; i < 4; ++i) { piece(i, 0); piece(i, 1); piece(i, 2); }
  const int xrow = ts * 16 + r16;
  const LAS char* xb = (const LAS char*)lds + xrow * 128 + (((kh * 4 + gq) ^ ((xrow >> 1) & 7)) * 16);
  const LAS char* wb[2];
#pragma unroll
  for (int e = 0; e < 2; ++e) wb[e] = (const LAS char*)lds + 8192 + (kh * 32 + gq * 8 + q4) * 192 + (((e + odd) ^ (gq & 1)) * 32) + p * 8;
  const bf16x8 ones = {0x3F80, 0x3F80, 0x3F80, 0x3F80, 0x3F80, 0x3F80, 0x3F80, 0x3F80};
  f32x4 acc[5] = {}, sx = {}, sxx = {}, csa = {}, csb = {};
  auto rd = [&](FragT& f, int slot) {
    f.x = *(const LAS bf16x8*)(xb + slot * SLOT);
#pragma unroll
    for (int c = 0; c < 5; ++c) { f.w[2 * c] = trread(wb[c & 1] + slot * SLOT + (c >> 1) * 64); f.w[2 * c + 1] = trread(wb[c & 1] + slot * SLOT + (c >> 1) * 64 + 768); }
  };
  auto mm = [&](const FragT& f, int dk) {
#pragma unroll
    for (int c = 0; c < 5; ++c) {
      acc[c] = MFMA16(cat8(f.w[2 * c], f.w[2 * c + 1]), f.x, acc[c]);
      SBAR(); if (dk >= 0 && !(c & 1)) piece(dk, c >> 1); SBAR();
    }
    sx = MFMA16(ones, f.x, sx); sxx = MFMA16(f.x, f.x, sxx);
#pragma unroll
    for (int c = 0; c < 4; ++c) if (ts == c) csa = MFMA16(cat8(f.w[2 * c], f.w[2 * c + 1]), ones, csa);
    if (ts == 0) csb = MFMA16(cat8(f.w[8], f.w[9]), ones, csb);
  };
  FragT fa, fb;
  ST();
  vmwait<9>(); PIN(pf);
  RAWBAR();
  ST();
  rd(fa, 0);
#define IT(kt, FC, FN, VM, DMA) do { vmwait<VM>(); RAWBAR(); rd(FN, ((kt) + 1) & 3); SBAR(); mm(FC, DMA ? (kt) + 4 : -1); SBAR(); } while (0)
  IT(0, fa, fb, 6, true); IT(1, fb, fa, 6, true); IT(2, fa, fb, 6, true); ST(); IT(3, fb, fa, 6, true); IT(4, fa, fb, 6, true);
  IT(5, fb, fa, 6, true); ST(); IT(6, fa, fb, 6, false); IT(7, fb, fa, 3, false); IT(8, fa, fb, 0, false);
  mm(fb, -1);
#undef IT
  ST();
  LAS f32x4* red = (LAS f32x4*)((LAS char*)lds + 2 * SLOT) + ts * 5 * 64 + lane;
  LAS f32x2* stp = (LAS f32x2*)((LAS char*)lds + 3 * SLOT);
  LAS f32x4* csp = (LAS f32x4*)((LAS char*)lds + 3 * SLOT + 1024);
  if (kh == 0) { red[3 * 64] = acc[3]; red[4 * 64] = acc[4]; }
  else { red[0] = acc[0]; red[64] = acc[1]; red[128] = acc[2]; }
  if (gq == q4) stp[kh * 64 + xrow] = (f32x2){sx[0], p == 0 ? sxx[0] : p == 1 ? sxx[1] : p == 2 ? sxx[2] : sxx[3]};
  if (r16 == 0) { csp[(kh * 5 + ts) * 4 + gq] = csa; if (ts == 0) csp[(kh * 5 + 4) * 4 + gq] = csb; }
  RAWBAR();
  float mean_b, rden;
  { const f32x2 a = stp[xrow], c = stp[64 + xrow]; ln_scalars(a[0] + c[0], a[1] + c[1], mean_b, rden); }
  rden *= QSCALE;
  u16* qp = (u16*)(ws + WS_Q) + (size_t)(tt * 8 + cg) * 5120 + xrow * 80 + gq * 4;
#pragma unroll
  for (int c = 0; c < 5; ++c)
    if ((c < 3) == (kh == 0)) {
      const f32x4 a = (acc[c] + red[c * 64] - (csp[c * 4 + gq] + csp[(5 + c) * 4 + gq]) * mean_b) * rden;
      *(u32x2*)(qp + c * 16) = (u32x2){cvtpk(a[0], a[1]), cvtpk(a[2], a[3])};
    }
  ST(); ST_OUT(ws, 5);
}

__global__ __launch_bounds__(512, 4) void k_pre(const u16* __restrict__ x, const u16* __restrict__ wq, const u16* __restrict__ wk,
                                                 const u16* __restrict__ wv, const u16* __restrict__ w1, const u16* __restrict__ w3,
                                                 const u16* __restrict__ w2, const u16* __restrict__ wo, char* __restrict__ ws, const u16* __restrict__ out) {
  extern __shared__ __attribute__((aligned(16))) char lds[];
  const int b = blockIdx.x;
  if (b < 128) ln_block(x, wk, wv, ws, lds, b);
  else if (b < 384) q_block(x, wq, ws, lds, b - 128);
  else {
    const int lane = threadIdx.x & 63, w = __builtin_amdgcn_readfirstlane(threadIdx.x >> 6), rb = b - 384;
    const unsigned lds0 = (unsigned)(uintptr_t)(LAS char*)lds;
    const int u1 = rb * 8 + w, u2 = 1024 + rb * 2 + w;
    const bool two = w < 2 && u2 < 1184;
    ST_DECL ST();
    retile_issue(wo, w1, w3, w2, lds0 + w * 6144, u1, lane);
    if (two) retile_issue(wo, w1, w3, w2, lds0 + 49152 + w * 6144, u2, lane);
    vmwait<0>();
    retile_finish(ws, (const LAS char*)lds + w * 6144, u1, lane);
    if (two) retile_finish(ws, (const LAS char*)lds + 49152 + w * 6144, u2, lane);
    ST(); ST_OUT(ws, 6);
  }
}

DI void arrive(char* ws, int i, unsigned want) {
  __hip_atomic_fetch_add((GAS unsigned*)(ws + WS_CNT) + i * 16, 1u, RLX_AGENT);
}
DI GAS unsigned* flagp(char* ws, int i) {
  return (GAS unsigned*)(ws + WS_CNT) + i * 16;
}
DI void wait_count(GAS unsigned* cnt, unsigned want) {
  for (unsigned spins = 0; spins < (1u << 22); ++spins) {
    if ((unsigned)__builtin_amdgcn_readfirstlane(__hip_atomic_load(cnt, RLX_AGENT)) >= want) break;
    __builtin_amdgcn_s_sleep(1);
  }
}

constexpr int KV_A = LDS_BYTES - KV_SLOT, KV_B = 0, KV_Cs = KV_SLOT, O_STG = 2 * KV_SLOT;

DI void attn_block(char* __restrict__ ws, char* lds, int b) {
  const int tid = threadIdx.x, lane = tid & 63, w = __builtin_amdgcn_readfirstlane(tid >> 6);
  const int bi = b >> 3;
  const int tt = (b & 7) * 4 + (bi & 3), hg = bi >> 2;
  const int r = lane & 31, h = lane >> 5;
  const unsigned lds0 = (unsigned)(uintptr_t)(LAS char*)lds;
  const unsigned lane16 = lane * 16;
  ST_DECL ST();
  __builtin_amdgcn_s_setprio(3);
  {
    const char* qg = ws + WS_Q + (size_t)(tt * 8 + hg) * 10240;
    glds16s(qg + w * 1024, lane16, lds0 + O_STG + w * 1024);
    if (w < 3) glds16s(w < 2 ? qg + 8192 + w * 1024 : ws + WS_KMAX, lane16, lds0 + O_STG + 8192 + w * 1024);
#pragma unroll
    for (int j = 0; j < 3; ++j) {
      const int pc = (7 - w) + 8 * j;
      if (pc < KV_PIECES) glds16s(ws + WS_KV + pc * 1024, lane16, lds0 + KV_A + pc * 1024);
    }
  }
  if (w >= 4) {
    const int lw = w - 4;
    const char* kvg = ws + WS_KV + lw * 1024;
    auto dma = [&](int chunk, int slotoff) {
#pragma unroll
      for (int j = 0; j < 6; ++j)
        if (j < 5 || lw < KV_PIECES - 20) glds16s(kvg + chunk * KV_CHUNK + j * 4096, lane16, lds0 + slotoff + (lw + 4 * j) * 1024);
    };
    dma(1, KV_B);
    vmwait<5>();
    asm volatile("s_barrier" ::: "memory");
    int fre = KV_Cs, nx = KV_A, nn = KV_B;
#pragma unroll 1
    for (int c = 0; c < 8; ++c) {
      asm volatile("s_waitcnt vmcnt(0)\n\ts_barrier" ::: "memory");
      if (c + 2 < 8) dma(c + 2, fre);
      const int t = fre; fre = nx; nx = nn; nn = t;
    }
  } else {
    asm volatile("s_waitcnt vmcnt(0)\n\ts_barrier" ::: "memory");
    __builtin_amdgcn_s_setprio(APRIO);
    ST();
    u32x4 q1a, q1b; u32x2 q2a, q2b;
    {
      const LAS char* qs = (const LAS char*)lds + O_STG + r * 160 + w * 40;
      const u32x2 a0 = *(const LAS u32x2*)(qs + 16 * h), a1 = *(const LAS u32x2*)(qs + 16 * h + 8);
      const u32x2 b0 = *(const LAS u32x2*)(qs + 5120 + 16 * h), b1 = *(const LAS u32x2*)(qs + 5120 + 16 * h + 8);
      q1a = (u32x4){a0[0], a0[1], a1[0], a1[1]}; q1b = (u32x4){b0[0], b0[1], b1[0], b1[1]};
      q2a = *(const LAS u32x2*)(qs + 32); q2b = *(const LAS u32x2*)(qs + 5120 + 32);
    }
    float km;
    {
      const f32x4 km4 = *(const LAS f32x4*)((LAS char*)lds + O_STG + 10240 + lane * 16);
      int k = __float_as_int(fmaxf(fmaxf(km4[0], km4[1]), fmaxf(km4[2], km4[3])));
      k = max(k, __builtin_amdgcn_update_dpp(0, k, 0x128, 0xf, 0xf, false)); k = max(k, __builtin_amdgcn_update_dpp(0, k, 0x124, 0xf, 0xf, false));
      k = max(k, __builtin_amdgcn_update_dpp(0, k, 0x122, 0xf, 0xf, false)); k = max(k, __builtin_amdgcn_update_dpp(0, k, 0x121, 0xf, 0xf, false));
      km = __int_as_float(max(__builtin_amdgcn_readlane(k, 0), __builtin_amdgcn_readlane(k, 16))) +
           __int_as_float(max(__builtin_amdgcn_readlane(k, 32), __builtin_amdgcn_readlane(k, 48)));
    }
    float qna = dot2(q1a[0], q1a[0], dot2(q1a[1], q1a[1], dot2(q1a[2], q1a[2], dot2(q1a[3], q1a[3], 0.f))));
    float qnb = dot2(q1b[0], q1b[0], dot2(q1b[1], q1b[1], dot2(q1b[2], q1b[2], dot2(q1b[3], q1b[3], 0.f))));
    if (h == 0) { qna = dot2(q2a[0], q2a[0], dot2(q2a[1], q2a[1], qna)); qnb = dot2(q2b[0], q2b[0], dot2(q2b[1], q2b[1], qnb)); }
    { const auto sw = __builtin_amdgcn_permlane32_swap(__float_as_uint(qna), __float_as_uint(qna), false, false); qna = __uint_as_float(sw[0]) + __uint_as_float(sw[1]); }
    { const auto sw = __builtin_amdgcn_permlane32_swap(__float_as_uint(qnb), __float_as_uint(qnb), false, false); qnb = __uint_as_float(sw[0]) + __uint_as_float(sw[1]); }
    const float bda = __builtin_amdgcn_sqrtf(qna * km) * 1.02f + 1e-3f, bdb = __builtin_amdgcn_sqrtf(qnb * km) * 1.02f + 1e-3f;
    const bool fast = __all(bda <= 48.f && bdb <= 48.f);
    if (h == 1) {
      q2a[0] = fast ? (cvtpk(-bda, 0.f) & 0xffffu) : 0u; q2a[1] = 0u;
      q2b[0] = fast ? (cvtpk(-bdb, 0.f) & 0xffffu) : 0u; q2b[1] = 0u;
    }
    const bf16x8 QA1 = __builtin_bit_cast(bf16x8, q1a), QB1 = __builtin_bit_cast(bf16x8, q1b);
    const s16x4 QA2 = __builtin_bit_cast(s16x4, q2a), QB2 = __builtin_bit_cast(s16x4, q2b);
    const int dv = r <= 20 ? r : 21;
    const LAS char* k1b = (const LAS char*)lds + lane * 16;
    const LAS char* k2b = (const LAS char*)lds + (h ? KV_C : KV_K2 + r * 8);
    const LAS char* vb = (const LAS char*)lds + KV_V + (h * VR + dv) * 16;
    f32x16 oa = {}, ob = {};
    const f32x16 zero16 = {};
    ST();
    if (fast) {
      f32x16 sA, sB;
      bf16x8 k1f, vf0, vg0, vf1; s16x4 k2f;
      u32x4 pa, pb;
      float e14, e15;
      k1f = *(const LAS bf16x8*)(k1b + KV_A); k2f = *(const LAS s16x4*)(k2b + KV_A); vf0 = *(const LAS bf16x8*)(vb + KV_A);
      sA = MFMA32(k1f, QA1, zero16); sA = MFMA32K8(k2f, QA2, sA);
      vf1 = (bf16x8){0, 0, 0, 0, 0, 0, 0, 0}; pb = (u32x4){0u, 0u, 0u, 0u}; e14 = 0.f; e15 = 0.f;
      int cur = KV_A, nxt = KV_B, fre = KV_Cs;
#define STEP(C, N, Q1, Q2, OC, OP, V0, L1, L2, L3, L4) do { \
        N = MFMA32(k1f, Q1, zero16); SBAR(); L1; SBAR(); \
        C[0] = EXP2(C[0]); C[1] = EXP2(C[1]); SBAR(); pb[3] = cvtpk(e14, e15); PIN(pb); SBAR(); \
        C[2] = EXP2(C[2]); C[3] = EXP2(C[3]); SBAR(); pa[0] = cvtpk(C[0], C[1]); PIN(pa); SBAR(); \
        OP = MFMA32(vf1, __builtin_bit_cast(bf16x8, pb), OP); SBAR(); L2; SBAR(); \
        C[4] = EXP2(C[4]); C[5] = EXP2(C[5]); SBAR(); pa[1] = cvtpk(C[2], C[3]); PIN(pa); SBAR(); \
        C[6] = EXP2(C[6]); C[7] = EXP2(C[7]); SBAR(); pa[2] = cvtpk(C[4], C[5]); PIN(pa); SBAR(); \
        N = MFMA32K8(k2f, Q2, N); SBAR(); L3; SBAR(); \
        C[8] = EXP2(C[8]); C[9] = EXP2(C[9]); SBAR(); pa[3] = cvtpk(C[6], C[7]); PIN(pa); SBAR(); \
        C[10] = EXP2(C[10]); C[11] = EXP2(C[11]); SBAR(); pb[0] = cvtpk(C[8], C[9]); PIN(pb); SBAR(); \
        OC = MFMA32(V0, __builtin_bit_cast(bf16x8, pa), OC); SBAR(); L4; SBAR(); \
        C[12] = EXP2(C[12]); C[13] = EXP2(C[13]); SBAR(); pb[1] = cvtpk(C[10], C[11]); PIN(pb); SBAR(); \
        e14 = EXP2(C[14]); e15 = EXP2(C[15]); SBAR(); pb[2] = cvtpk(C[12], C[13]); PIN(pb); PIN(e14); PIN(e15); SBAR(); \
      } while (0)
#define KT(i, V0C, V0N) do { \
        STEP(sA, sB, QB1, QB2, oa, ob, vf0, k1f = *(const LAS bf16x8*)((i) < 7 ? kc1 + ((i) + 1) * KV_TILE : k1b + nxt), \
             vf1 = *(const LAS bf16x8*)(vc + (i) * KV_TILE + 2 * VR * 16), k2f = *(const LAS s16x4*)((i) < 7 ? kc2 + ((i) + 1) * KV_TILE : k2b + nxt), (void)0); \
        STEP(sB, sA, QA1, QA2, ob, oa, vf0, (void)0, (void)0, (void)0, vf0 = *(const LAS bf16x8*)((i) < 7 ? vc + ((i) + 1) * KV_TILE : vb + nxt)); \
      } while (0)
#pragma unroll 1
      for (int c = 0; c < 8; ++c) {
        const LAS char* kc1 = k1b + cur; const LAS char* kc2 = k2b + cur; const LAS char* vc = vb + cur;
        KT(0, vf0, vg0); KT(1, vg0, vf0); KT(2, vf0, vg0);
        asm volatile("s_barrier" ::: "memory");
        KT(3, vg0, vf0); KT(4, vf0, vg0); KT(5, vg0, vf0); KT(6, vf0, vg0); KT(7, vg0, vf0);
        const int t = cur; cur = nxt; nxt = fre; fre = t;
      }
      pb[3] = cvtpk(e14, e15);
      ob = MFMA32(vf1, __builtin_bit_cast(bf16x8, pb), ob);
#undef KT
#undef STEP
    } else {
      float ma = -INFINITY, mb = -INFINITY;
      int cur = KV_A, nxt = KV_B, fre = KV_Cs;
#pragma unroll 1
      for (int c = 0; c < 8; ++c) {
#pragma unroll 1
        for (int i = 0; i < 8; ++i) {
          if (i == 3) RAWBAR();
          const int off = cur + i * KV_TILE;
          const bf16x8 k1f = *(const LAS bf16x8*)(k1b + off); const s16x4 k2f = *(const LAS s16x4*)(k2b + off);
          const bf16x8 vf0 = *(const LAS bf16x8*)(vb + off), vf1 = *(const LAS bf16x8*)(vb + off + 2 * VR * 16);
#pragma unroll
          for (int qs = 0; qs < 2; ++qs) {
            f32x16 sc = MFMA32(k1f, qs ? QB1 : QA1, zero16); sc = MFMA32K8(k2f, qs ? QB2 : QA2, sc);
            float& m = qs ? mb : ma; f32x16& o = qs ? ob : oa;
            float mx = sc[0];
#pragma unroll
            for (int j = 1; j < 16; ++j) mx = fmaxf(mx, sc[j]);
            mx = fmaxf(mx, __shfl_xor(mx, 32));
            const float mn = fmaxf(m, mx);
            const float al = EXP2(m - mn);
            m = mn;
#pragma unroll
            for (int j = 0; j < 16; ++j) { o[j] *= al; sc[j] = EXP2(sc[j] - mn); }
            const u32x4 pa = {cvtpk(sc[0], sc[1]), cvtpk(sc[2], sc[3]), cvtpk(sc[4], sc[5]), cvtpk(sc[6], sc[7])};
            const u32x4 pb = {cvtpk(sc[8], sc[9]), cvtpk(sc[10], sc[11]), cvtpk(sc[12], sc[13]), cvtpk(sc[14], sc[15])};
            o = MFMA32(vf0, __builtin_bit_cast(bf16x8, pa), o);
            o = MFMA32(vf1, __builtin_bit_cast(bf16x8, pb), o);
          }
        }
        const int t = cur; cur = nxt; nxt = fre; fre = t;
      }
    }
    PIN(oa); PIN(ob); ST();
#pragma unroll
    for (int qs = 0; qs < 2; ++qs) {
      const f32x16 o = qs ? ob : oa;
      const auto sw = __builtin_amdgcn_permlane32_swap(__float_as_uint(o[8]), __float_as_uint(o[8]), false, false);
      const float il = __builtin_amdgcn_rcpf(__uint_as_float(sw[1]));
      LAS char* os = (LAS char*)lds + O_STG + (qs * 32 + r) * 176 + w * 40 + 8 * h;
      *(LAS u32x2*)(os) = (u32x2){cvtpk(o[0] * il, o[1] * il), cvtpk(o[2] * il, o[3] * il)};
      *(LAS u32x2*)(os + 16) = (u32x2){cvtpk(o[4] * il, o[5] * il), cvtpk(o[6] * il, o[7] * il)};
      if (h == 0) *(LAS u32x2*)(os + 32) = (u32x2){cvtpk(o[8] * il, o[9] * il), cvtpk(o[10] * il, o[11] * il)};
    }
  }
  RAWBAR();
  const __amdgpu_buffer_rsrc_t orr = __builtin_amdgcn_make_buffer_rsrc(ws + WS_O, 0, S * D * 2, 0x00020000);
#pragma unroll
  for (int j = 0; j < 2; ++j) {
    const int ci = tid + 512 * j;
    if (ci < 640) {
      const int c = ci >> 6, cc = hg * 10 + c;
      const u32x4 v = *(const LAS u32x4*)((LAS char*)lds + O_STG + lane * 176 + c * 16);
      __builtin_amdgcn_raw_buffer_store_b128(v, orr, ((tt * 20 + (cc >> 2)) * 4 + (lane >> 4)) * 1024 + ((cc & 3) * 16 + (lane & 15)) * 16, 0, 16);
    }
  }
  vmwait<0>();
  RAWBAR();
  if (tid == 0) arrive(ws, 16 + tt, 8u);
}

struct Frag3 { bf16x8 x; bf16x8 w[3]; };
struct Frag5 { bf16x8 x; bf16x8 w[5]; };
DI void ffn_block(const u16* __restrict__ x, char* __restrict__ ws, u16* __restrict__ out, char* lds, int b) {
  const int tid = threadIdx.x, lane = tid & 63, w = __builtin_amdgcn_readfirstlane(tid >> 6);
  const int bi = b >> 3;
  const unsigned lds0 = (unsigned)(uintptr_t)(LAS char*)lds, lane16 = lane * 16;
  ST_DECL ST();
  if (GPRIO) __builtin_amdgcn_s_setprio(GPRIO);
  {
    const int tp = ((b & 7) >> 2) * 8 + (bi & 7), cg = (b & 3) * 4 + (bi >> 3);
    const char* xsb = ws + WS_XN + (size_t)tp * 163840 + w * 1024;
    const char* wsrc = ws + WS_W13 + (size_t)cg * 245760 + w * 1536;
    auto piece = [&](int kt, int slot, int j) {
      if (j == 0) glds16s(xsb + kt * 8192, lane16, lds0 + slot * SLOT + w * 1024);
      else glds_lin<48>(wsrc + kt * 12288 + (j - 1) * 768, lane16, lds0 + slot * SLOT + 8192 + w * 1536 + (j - 1) * 768);
    };
#pragma unroll
    for (int i = 0; i < 4; ++i) { piece(i, i, 0); piece(i, i, 1); piece(i, i, 2); }
    const int tq = w & 3, hc = w >> 2, r = lane & 31, h = lane >> 5;
    const int xrow = tq * 32 + r;
    const LAS char* xb0 = (const LAS char*)lds + xrow * 64 + ((h ^ ((xrow >> 2) & 3)) * 16);
    const LAS char* xb1 = (const LAS char*)lds + xrow * 64 + (((2 + h) ^ ((xrow >> 2) & 3)) * 16);
    const LAS char* wb = (const LAS char*)lds + 8192 + hc * 3072 + lane * 16;
    f32x16 acc[3] = {};
    auto rd = [&](Frag3& f, int slot, int s) {
      f.x = *(const LAS bf16x8*)((s ? xb1 : xb0) + slot * SLOT);
#pragma unroll
      for (int c = 0; c < 3; ++c) f.w[c] = *(const LAS bf16x8*)(wb + slot * SLOT + s * 6144 + c * 1024);
    };
    auto mm = [&](const Frag3& f, int s, int dk, int dsl) {
#pragma unroll
      for (int c = 0; c < 3; ++c) {
        acc[c] = MFMA32(f.w[c], f.x, acc[c]);
        SBAR();
        if (dk >= 0) { if (s == 1 && c == 0) piece(dk, dsl, 0); if (s == 1 && c == 2) piece(dk, dsl, 1); if (s == 0 && c == 0) piece(dk, dsl, 2); }
        SBAR();
      }
    };
    Frag3 f0, f1;
    vmwait<9>();
    RAWBAR();
    rd(f0, 0, 0);
#define IT(kt, sl, VM, PREV, DMA, NEXT) do { \
      rd(f1, sl, 1); SBAR(); mm(f0, 0, PREV ? (kt) + 3 : -1, ((sl) + 3) & 3); SBAR(); \
      if (NEXT) { vmwait<VM>(); RAWBAR(); rd(f0, ((sl) + 1) & 3, 0); } \
      SBAR(); mm(f1, 1, DMA ? (kt) + 4 : -1, sl); SBAR(); } while (0)
    IT(0, 0, 6, false, true, true); IT(1, 1, 6, true, true, true); IT(2, 2, 6, true, true, true); IT(3, 3, 6, true, true, true);
#pragma unroll 1
    for (int kt = 4; kt < 16; kt += 4) {
      if (kt == G1LATE) __builtin_amdgcn_s_setprio(3);
      IT(kt, 0, 6, true, true, true); IT(kt + 1, 1, 6, true, true, true); IT(kt + 2, 2, 6, true, true, true); IT(kt + 3, 3, 6, true, true, true);
    }
    IT(16, 0, 6, true, false, true); IT(17, 1, 3, false, false, true); IT(18, 2, 0, false, false, true); IT(19, 3, 0, false, false, false);
#undef IT
    PIN(acc[0]); PIN(acc[1]); PIN(acc[2]); ST();
    __builtin_amdgcn_s_setprio(3);
    u32x4 hv[3];
#pragma unroll
    for (int c = 0; c < 3; ++c) {
      float v[8];
#pragma unroll
      for (int j = 0; j < 8; ++j) {
        const float gt = acc[c][j], up = acc[c][j + 8];
        v[j] = gt * __builtin_amdgcn_rcpf(1.f + EXP2(-1.4426950408889634f * gt)) * up;
      }
      hv[c] = (u32x4){cvtpk(v[0], v[1]), cvtpk(v[2], v[3]), cvtpk(v[4], v[5]), cvtpk(v[6], v[7])};
    }
#pragma unroll
    for (int c = 0; c < 3; ++c) *(LAS u32x4*)((LAS char*)lds + xrow * 208 + hc * 96 + c * 32 + h * 16) = hv[c];
    RAWBAR();
    const __amdgpu_buffer_rsrc_t hr = __builtin_amdgcn_make_buffer_rsrc(ws + WS_H, 0, S * F * 2, 0x00020000);
#pragma unroll
    for (int j = 0; j < 3; ++j) {
      const int ci = tid + 512 * j, row = ci / 12, pos = ci - row * 12;
      const u32x4 v = *(const LAS u32x4*)((LAS char*)lds + row * 208 + pos * 16);
      __builtin_amdgcn_raw_buffer_store_b128(v, hr, ((tp * 128 + row) * F + cg * 96 + pos * 8) * 2, 0, 16);
    }
    vmwait<0>();
    RAWBAR();
    if (tid == 448) arrive(ws, tp, 16u);
  }
  ST();
  const int tt = (b & 7) * 4 + (bi & 3), t0 = tt * 64, cg = bi >> 2, c0 = cg * 80;
  const int ts = w & 3, kh = w >> 2, r16 = lane & 15, gq = lane >> 4, xrow = ts * 16 + r16;
  f32x4 acc[5] = {};
  const char* wog = ws + WS_WO + (size_t)cg * 102400;
  bf16x8 wr[10];
  auto wop = [&](int j) { glds16s(wog + (j >= 5 ? 10240 : 0) + (j * 8 + w) * 1024, lane16, lds0 + (j * 8 + w) * 1024); };
  {
    const int srow = w * 8 + (lane >> 3);
    const unsigned hs = (srow * F + ((lane & 7) ^ ((srow >> 1) & 7)) * 8) * 2;
    const u16* hb = (const u16*)(ws + WS_H) + (size_t)t0 * F;
    const char* wsrc = ws + WS_W2 + (size_t)cg * 245760 + w * 1280;
    auto piece = [&](int kt, int slot, int j) {
      if (j == 0) glds16s_sc1(hb + kt * 64, hs, lds0 + slot * ST64 + w * 1024);
      else glds_lin<40>(wsrc + kt * 10240 + (j - 1) * 640, lane16, lds0 + slot * ST64 + 8192 + w * 1280 + (j - 1) * 640);
    };
#pragma unroll
    for (int i = 0; i < 4; ++i) { piece(i, i, 1); piece(i, i, 2); }
    if (kh == 0) {
#pragma unroll
      for (int c = 0; c < 5; ++c) {
        const u32x2 xv = *(const u32x2*)(x + (size_t)(t0 + xrow) * D + c0 + c * 16 + gq * 4);
        acc[c] = (f32x4){bflo(xv[0]), bfhi(xv[0]), bflo(xv[1]), bfhi(xv[1])};
        PIN(acc[c]);
      }
    }
    if (w == 0) wait_count(flagp(ws, tt >> 1), 16u);
    RAWBAR();
    __builtin_amdgcn_s_setprio(P2PRIO);
    ST();
#pragma unroll
    for (int i = 0; i < 4; ++i) piece(i, i, 0);
    const LAS char* xb = (const LAS char*)lds + xrow * 128 + (((kh * 4 + gq) ^ ((xrow >> 1) & 7)) * 16);
    const LAS char* wb = (const LAS char*)lds + 8192 + kh * 5120 + lane * 16;
    auto rd = [&](Frag5& f, int slot) {
      f.x = *(const LAS bf16x8*)(xb + slot * ST64);
#pragma unroll
      for (int c = 0; c < 5; ++c) f.w[c] = *(const LAS bf16x8*)(wb + slot * ST64 + c * 1024);
    };
    auto mm = [&](const Frag5& f, int dk, int dsl) {
#pragma unroll
      for (int c = 0; c < 5; ++c) { acc[c] = MFMA16(f.w[c], f.x, acc[c]); SBAR(); if (dk >= 0 && !(c & 1)) piece(dk, dsl, c >> 1); SBAR(); }
    };
    Frag5 fa, fb;
    vmwait<3>();
    RAWBAR();
    rd(fa, 0);
#define IT(kt, sl, FC, FN, VM, DMA) do { vmwait<VM>(); RAWBAR(); rd(FN, ((sl) + 1) & 3); SBAR(); mm(FC, DMA ? (kt) + 4 : -1, sl); SBAR(); } while (0)
    IT(0, 0, fa, fb, 2, true); IT(1, 1, fb, fa, 4, true); IT(2, 2, fa, fb, 6, true); IT(3, 3, fb, fa, 6, true);
#pragma unroll 1
    for (int kt = 4; kt < 20; kt += 4) {
      IT(kt, 0, fa, fb, 6, true); IT(kt + 1, 1, fb, fa, 6, true); IT(kt + 2, 2, fa, fb, 6, true); IT(kt + 3, 3, fb, fa, 6, true);
    }
    IT(20, 0, fa, fb, 6, false); wop(0); wop(1); wop(9);
    {
      const char* wrp = wog + kh * 51200 + 40960;
#pragma unroll
      for (int j = 0; j < 10; ++j) asm volatile("global_load_dwordx4 %0, %1, %2" : "=v"(wr[j]) : "v"(lane16), "s"(wrp + j * 1024) : "memory");
    }
    IT(21, 1, fb, fa, 16, false); wop(2); wop(3);
    IT(22, 2, fa, fb, 15, false); wop(4); wop(5);
    mm(fb, -1, 0);
#undef IT
  }
  PIN(acc[0]); ST();
  RAWBAR();
  wop(6); wop(7); wop(8);
  asm volatile("s_waitcnt vmcnt(0)" : "+v"(wr[0]), "+v"(wr[1]), "+v"(wr[2]), "+v"(wr[3]), "+v"(wr[4]), "+v"(wr[5]), "+v"(wr[6]), "+v"(wr[7]), "+v"(wr[8]), "+v"(wr[9]) :: "memory");
  vmwait<0>();
  __builtin_amdgcn_s_setprio(3);
  ST();
  const LAS char* wbt = (const LAS char*)lds + kh * 40960 + lane * 16;
  if (w == 0) wait_count(flagp(ws, 16 + tt), 8u);
  RAWBAR();
  ST();
  {
    u32x4 of[10];
    const char* op = ws + WS_O + ((tt * 20 + kh * 10) * 4 + ts) * 1024;
#pragma unroll
    for (int s = 0; s < 10; ++s) asm volatile("global_load_dwordx4 %0, %1, %2 sc1" : "=v"(of[(s + 8) % 10]) : "v"(lane16), "s"(op + ((s + 8) % 10) * 4096) : "memory");
#define OSTEP(s, N, WC, WN) do { \
      if ((s) + 1 < 8) { _Pragma("unroll") for (int c = 0; c < 5; ++c) WN[c] = *(const LAS bf16x8*)(wbt + ((s) + 1) * 5120 + c * 1024); } \
      SBAR(); asm volatile("s_waitcnt vmcnt(" #N ")" : "+v"(of[s]) :: "memory"); SBAR(); \
      _Pragma("unroll") for (int c = 0; c < 5; ++c) acc[c] = MFMA16(WC[c], __builtin_bit_cast(bf16x8, of[s]), acc[c]); SBAR(); } while (0)
    asm volatile("s_waitcnt vmcnt(9)" : "+v"(of[8]) :: "memory");
#pragma unroll
    for (int c = 0; c < 5; ++c) acc[c] = MFMA16(wr[c], __builtin_bit_cast(bf16x8, of[8]), acc[c]);
    SBAR();
    bf16x8 w0[5];
#pragma unroll
    for (int c = 0; c < 5; ++c) w0[c] = *(const LAS bf16x8*)(wbt + c * 1024);
    SBAR(); asm volatile("s_waitcnt vmcnt(8)" : "+v"(of[9]) :: "memory"); SBAR();
#pragma unroll
    for (int c = 0; c < 5; ++c) acc[c] = MFMA16(wr[5 + c], __builtin_bit_cast(bf16x8, of[9]), acc[c]);
    SBAR();
    bf16x8 wn[5];
    OSTEP(0, 7, w0, wn); OSTEP(1, 6, wn, w0); OSTEP(2, 5, w0, wn); OSTEP(3, 4, wn, w0);
    OSTEP(4, 3, w0, wn); OSTEP(5, 2, wn, w0); OSTEP(6, 1, w0, wn); OSTEP(7, 0, wn, w0);
#undef OSTEP
  }
  RAWBAR();
#pragma unroll
  for (int c = 0; c < 5; ++c) *(LAS f32x4*)((LAS char*)lds + kh * 64 * 336 + xrow * 336 + c * 64 + gq * 16) = acc[c];
  RAWBAR();
#pragma unroll
  for (int j = 0; j < 2; ++j) {
    const int ci = tid + 512 * j;
    if (ci < 640) {
      const int row = ci / 10, c = ci - row * 10;
      const LAS f32x4* a0 = (const LAS f32x4*)((LAS char*)lds + row * 336 + c * 32);
      const LAS f32x4* a1 = (const LAS f32x4*)((LAS char*)lds + 64 * 336 + row * 336 + c * 32);
      const f32x4 lo = a0[0] + a1[0], hi = a0[1] + a1[1];
      const u32x4 res = {cvtpk(lo[0], lo[1]), cvtpk(lo[2], lo[3]), cvtpk(hi[0], hi[1]), cvtpk(hi[2], hi[3])};
      *(u32x4*)(out + (size_t)(t0 + row) * D + c0 + c * 8) = res;
    }
  }
  ST(); ST_OUT(ws, 2);
}

__global__ __launch_bounds__(512, 4) void k_mid(const u16* __restrict__ x, char* __restrict__ ws, u16* __restrict__ out) {
  extern __shared__ __attribute__((aligned(16))) char lds[];
  if (blockIdx.x < 256) attn_block(ws, lds, blockIdx.x);
  else ffn_block(x, ws, out, lds, blockIdx.x - 256);
}

extern "C" void kernel_launch(void* const* d_in, const int* in_sizes, int n_in, void* d_out, int out_size, void* d_ws, size_t ws_size,
                              hipStream_t stream) {
  const u16* x = (const u16*)d_in[0];
  const u16* wq = (const u16*)d_in[1];
  const u16* wk = (const u16*)d_in[2];
  const u16* wv = (const u16*)d_in[3];
  const u16* wo = (const u16*)d_in[4];
  const u16* w1 = (const u16*)d_in[6];
  const u16* w3 = (const u16*)d_in[7];
  const u16* w2 = (const u16*)d_in[8];
  char* ws = (char*)d_ws;
  (void)hipFuncSetAttribute((const void*)k_mid, hipFuncAttributeMaxDynamicSharedMemorySize, LDS_BYTES);
  (void)hipFuncSetAttribute((const void*)k_pre, hipFuncAttributeMaxDynamicSharedMemorySize, LDS_BYTES);
  k_pre<<<512, 512, LDS_BYTES, stream>>>(x, wq, wk, wv, w1, w3, w2, wo, ws, (const u16*)d_out);
  k_mid<<<512, 512, LDS_BYTES, stream>>>(x, ws, (u16*)d_out);
}
```

```cpp
#include <hip/hip_runtime.h>
#include <hip/hip_bf16.h>
#include <stdint.h>

typedef short bf16x8 __attribute__((ext_vector_type(8)));
typedef short s16x4 __attribute__((ext_vector_type(4)));
typedef float f32x2 __attribute__((ext_vector_type(2)));
typedef float f32x4 __attribute__((ext_vector_type(4)));
typedef float f32x16 __attribute__((ext_vector_type(16)));
typedef unsigned u32x4 __attribute__((ext_vector_type(4)));
typedef unsigned u32x2 __attribute__((ext_vector_type(2)));
typedef __bf16 bf16x2_t __attribute__((ext_vector_type(2)));
typedef unsigned short u16;
#define LAS __attribute__((address_space(3)))
#define GAS __attribute__((address_space(1)))
#define RLX_AGENT __ATOMIC_RELAXED, __HIP_MEMORY_SCOPE_AGENT
#define DI __device__ __forceinline__
#define SBAR() __builtin_amdgcn_sched_barrier(0)
#define PIN(x) asm volatile("" : "+v"(x))

#define GPRIO 1
#define LADDER 1
#define WT 0
#define QPRIO 0
#define P2PRIO GPRIO
#define G1LATE 99
#define APRIO 2
constexpr int S = 2048, D = 640, H = 32, DH = 20, F = 1536;
constexpr int VR = 22;
constexpr int KV_K2 = 1024, KV_C = 1280, KV_V = 1296, KV_TILE = KV_V + 4 * VR * 16;
constexpr int KV_CHUNK = 8 * KV_TILE;
constexpr int KV_PIECES = (KV_CHUNK + 1023) / 1024, KV_SLOT = KV_PIECES * 1024;
constexpr size_t WS_CNT = 0;
constexpr size_t WS_KMAX = 12288;
constexpr size_t WS_KV = 16384;
constexpr size_t WS_XN = WS_KV + 192 * 1024;
constexpr size_t WS_O = WS_XN + (size_t)S * D * 2;
constexpr size_t WS_H = WS_O + (size_t)S * D * 2;
constexpr size_t WS_Q = WS_H + (size_t)S * F * 2;
constexpr size_t WS_WO = WS_Q + (size_t)S * D * 2;
constexpr size_t WS_W13 = WS_WO + (size_t)D * D * 2;
constexpr size_t WS_W2 = WS_W13 + (size_t)2 * D * F * 2;
constexpr float QSCALE = 0.22360679774997896f * 1.4426950408889634f;
constexpr int LDS_BYTES = 81920, SLOT = 20480;

DI unsigned cvtpk(float a, float b) { f32x2 v = {a, b}; bf16x2_t r = __builtin_convertvector(v, bf16x2_t); return __builtin_bit_cast(unsigned, r); }
DI float bflo(unsigned u) { return __uint_as_float(u << 16); }
DI float bfhi(unsigned u) { return __uint_as_float(u & 0xffff0000u); }
DI float rbf(float a) { return bflo(cvtpk(a, a)); }
DI float dot2(unsigned a, unsigned b, float c) { return __builtin_amdgcn_fdot2_f32_bf16(__builtin_bit_cast(bf16x2_t, a), __builtin_bit_cast(bf16x2_t, b), c, false); }
DI s16x4 trread(const LAS char* p) { return __builtin_bit_cast(s16x4, __builtin_amdgcn_ds_read_tr16_b64_v4i16((LAS s16x4*)p)); }
DI bf16x8 cat8(s16x4 a, s16x4 b) { return __builtin_shufflevector(a, b, 0, 1, 2, 3, 4, 5, 6, 7); }
DI void glds16s(const void* sbase, unsigned voff, unsigned lds_base) {
  unsigned sv;
  asm volatile("s_mov_b32 %0, m0\n\ts_mov_b32 m0, %3\n\ts_nop 0\n\tglobal_load_lds_dwordx4 %1, %2\n\ts_mov_b32 m0, %0" : "=&s"(sv) : "v"(voff), "s"(sbase), "s"(lds_base) : "memory");
}
DI void glds16s_sc1(const void* sbase, unsigned voff, unsigned lds_base) {
  unsigned sv;
  asm volatile("s_mov_b32 %0, m0\n\ts_mov_b32 m0, %3\n\ts_nop 0\n\tglobal_load_lds_dwordx4 %1, %2 sc1\n\ts_mov_b32 m0, %0" : "=&s"(sv) : "v"(voff), "s"(sbase), "s"(lds_base) : "memory");
}
#define ST_DECL
#define ST()
#define ST_OUT(a, b)
#define MFMA32(a, b, c) __builtin_amdgcn_mfma_f32_32x32x16_bf16(a, b, c, 0, 0, 0)
#define MFMA32K8(a, b, c) __builtin_amdgcn_mfma_f32_32x32x8bf16_1k(a, b, c, 0, 0, 0)
#define MFMA16(a, b, c) __builtin_amdgcn_mfma_f32_16x16x32_bf16(a, b, c, 0, 0, 0)
#define EXP2(v) __builtin_amdgcn_exp2f(v)
#define RAWBAR() asm volatile("s_waitcnt lgkmcnt(0)\n\ts_barrier" ::: "memory")
DI void touch(const void* p, unsigned& d) { asm volatile("global_load_dword %0, %1, off" : "+v"(d) : "v"(p) : "memory"); }
DI void st16wt(void* p, u32x4 v) {
  *(u32x4*)p = v;
}
template <int N> DI void vmwait() { asm volatile("s_waitcnt vmcnt(%0)" :: "n"(N) : "memory"); }
DI int wslot(int k) { return k ^ ((k >> 1) & 4); }
DI void ln_scalars(float sum, float sq, float& mean_b, float& rden) {
  const float mean = sum * (1.f / D);
  const float var = fmaxf(sq * (1.f / D) - mean * mean, 0.f);
  mean_b = rbf(mean);
  rden = __builtin_amdgcn_rcpf(rbf(__builtin_amdgcn_sqrtf(rbf(rbf(var) + 1e-5f))));
}

template <int NL> DI void glds_lin(const char* src, unsigned lane16, unsigned dst) {
  unsigned sv;
  asm volatile("s_mov_b32 %0, m0\n\ts_mov_b32 m0, %3\n\ts_mov_b32 exec_hi, %4\n\tglobal_load_lds_dwordx4 %1, %2\n\ts_mov_b32 exec_hi, -1\n\ts_mov_b32 m0, %0"
               : "=&s"(sv) : "v"(lane16), "s"(src), "s"(dst), "n"((1u << (NL - 32)) - 1u) : "memory");
}

constexpr int ST64 = 18432;

DI void retile_issue(const u16* __restrict__ wo, const u16* __restrict__ w1, const u16* __restrict__ w3, const u16* __restrict__ w2,
                     unsigned ldsu, int u, int lane) {
#pragma unroll
  for (int j = 0; j < 3; ++j) {
    const int ci = j * 64 + lane, rr = ci / 12, pos = ci - rr * 12;
    if (u < 640) {
      const int cg = u / 40, ks = u - cg * 40;
      glds16s(w1 + (size_t)ks * 16 * F + cg * 96, (rr * F + pos * 8) * 2, ldsu + j * 1024);
      glds16s(w3 + (size_t)ks * 16 * F + cg * 96, (rr * F + pos * 8) * 2, ldsu + 3072 + j * 1024);
    } else {
      const int i = u < 800 ? u - 640 : u - 800, nk = u < 800 ? 20 : 48, cg = i / nk, ks = i - cg * nk, c0 = cg * 80 - (cg & 1) * 16;
      const u16* src = (u < 800 ? wo : w2) + (size_t)ks * 32 * D + c0;
      const unsigned vo = (rr * D + (c0 + pos * 8 < D ? pos : 0) * 8) * 2;
      glds16s(src, vo, ldsu + j * 1024);
      glds16s(src + 16 * D, vo, ldsu + 3072 + j * 1024);
    }
  }
}
DI void retile_finish(char* __restrict__ ws, const LAS char* im, int u, int lane) {
  const int h = lane >> 5, q4 = (lane & 15) >> 2, p = lane & 3, blk = (lane >> 4) & 1;
  if (u < 640) {
    const LAS char* a = im + blk * 3072 + (8 * h + q4) * 192 + (((p & 1) << 1) | (p >> 1)) * 8;
    char* dst = ws + WS_W13 + (size_t)u * 6144 + lane * 16;
#pragma unroll
    for (int c = 0; c < 6; ++c) st16wt(dst + c * 1024, __builtin_bit_cast(u32x4, cat8(trread(a + c * 32), trread(a + c * 32 + 768))));
  } else {
    const int i = u < 800 ? u - 640 : u - 800, cg = i / (u < 800 ? 20 : 48);
    const LAS char* a = im + ((lane >> 4) * 8 + q4) * 192 + (cg & 1) * 32 + p * 8;
    char* dst = ws + (u < 800 ? WS_WO : WS_W2) + (size_t)i * 5120 + lane * 16;
#pragma unroll
    for (int c = 0; c < 5; ++c) st16wt(dst + c * 1024, __builtin_bit_cast(u32x4, cat8(trread(a + c * 32), trread(a + c * 32 + 768))));
  }
}

constexpr int LN_ST = 25600, LN_RED = LN_ST + 512, LN_HALF = LN_RED + 8192;
DI void ln_block(const u16* __restrict__ x, const u16* __restrict__ wk, const u16* __restrict__ wv, char* __restrict__ ws, char* ldsb, int b) {
  const int tid = threadIdx.x, lane = tid & 63, w8 = __builtin_amdgcn_readfirstlane(tid >> 6), w = w8 & 3, kv = w8 >> 2;
  const int t16 = (b & 7) * 16 + (b >> 3);
  const int r16 = lane & 15, gq = lane >> 4;
  LAS char* lds = (LAS char*)ldsb + kv * LN_HALF;
  const unsigned lds0 = (unsigned)(uintptr_t)lds;
  const int token = t16 * 16 + r16;
  ST_DECL ST();
  __builtin_amdgcn_s_setprio(3);
  u32x4 xf[5];
  const u16* xp = x + (size_t)token * D + w * 160 + gq * 8;
#pragma unroll
  for (int s = 0; s < 5; ++s) asm volatile("global_load_dwordx4 %0, %1, off offset:%2" : "=v"(xf[s]) : "v"(xp), "i"(s * 64) : "memory");
  const u16* wsrc = kv ? wv : wk;
#pragma unroll
  for (int j = 0; j < 7; ++j)
    if (j < 6 || w == 0) glds16s(wsrc + (w + 4 * j) * 512, lane * 16, lds0 + (w + 4 * j) * 1024);
  ST();
  asm volatile("s_waitcnt vmcnt(0)" : "+v"(xf[0]), "+v"(xf[1]), "+v"(xf[2]), "+v"(xf[3]), "+v"(xf[4]) :: "memory");
  float sum = 0.f, sq = 0.f, sum2 = 0.f, sq2 = 0.f;
#pragma unroll
  for (int s = 0; s < 5; ++s)
#pragma unroll
    for (int j = 0; j < 4; j += 2) {
      sum = dot2(xf[s][j], 0x3F803F80u, sum); sq = dot2(xf[s][j], xf[s][j], sq);
      sum2 = dot2(xf[s][j + 1], 0x3F803F80u, sum2); sq2 = dot2(xf[s][j + 1], xf[s][j + 1], sq2);
    }
  sum += sum2; sq += sq2;
  sum += __shfl_xor(sum, 16); sq += __shfl_xor(sq, 16);
  sum += __shfl_xor(sum, 32); sq += __shfl_xor(sq, 32);
  if (b == 0 && tid < 64) { ((unsigned*)(ws + WS_CNT))[tid * 16] = 0u; ((unsigned*)(ws + WS_CNT + 4096))[tid * 32] = 0u; }
  LAS f32x2* stp = (LAS f32x2*)(lds + LN_ST);
  if (gq == 0) stp[w * 16 + r16] = (f32x2){sum, sq};
  RAWBAR();
  { const f32x2 a = stp[r16], c = stp[16 + r16], d = stp[32 + r16], e = stp[48 + r16]; sum = (a[0] + c[0]) + (d[0] + e[0]); sq = (a[1] + c[1]) + (d[1] + e[1]); }
  float mean_b, rden;
  ln_scalars(sum, sq, mean_b, rden);
  ST();
#pragma unroll
  for (int s = 0; s < 5; ++s)
#pragma unroll
    for (int j = 0; j < 4; ++j) {
      const unsigned d = cvtpk(bflo(xf[s][j]) - mean_b, bfhi(xf[s][j]) - mean_b);
      xf[s][j] = cvtpk(bflo(d) * rden, bfhi(d) * rden);
    }
  if ((w >> 1) == kv) {
    const int row = token & 127;
    char* xn = ws + WS_XN + ((size_t)((token >> 7) * 20 + w * 5) * 128 + row) * 64 + ((gq ^ ((row >> 2) & 3)) * 16);
#pragma unroll
    for (int s = 0; s < 5; ++s) *(u32x4*)(xn + s * 8192) = xf[s];
  }
  const int q4 = (lane & 15) >> 2, p = lane & 3;
  const LAS char* e0 = lds + (w * 160 + gq * 8 + q4) * 40 + p * 8;
  const LAS char* e1 = lds + (w * 160 + gq * 8 + q4) * 40 + 32;
  f32x4 acc[2] = {};
  if (kv == 0) {
#pragma unroll
    for (int s = 0; s < 5; ++s) {
      acc[0] = MFMA16(cat8(trread(e0 + s * 1280), trread(e0 + s * 1280 + 160)), __builtin_bit_cast(bf16x8, xf[s]), acc[0]);
      acc[1] = MFMA16(cat8(trread(e1 + s * 1280), trread(e1 + s * 1280 + 160)), __builtin_bit_cast(bf16x8, xf[s]), acc[1]);
    }
  } else {
#pragma unroll
    for (int s = 0; s < 5; ++s) {
      acc[0] = MFMA16(__builtin_bit_cast(bf16x8, xf[s]), cat8(trread(e0 + s * 1280), trread(e0 + s * 1280 + 160)), acc[0]);
      acc[1] = MFMA16(__builtin_bit_cast(bf16x8, xf[s]), cat8(trread(e1 + s * 1280), trread(e1 + s * 1280 + 160)), acc[1]);
    }
  }
  LAS f32x4* red = (LAS f32x4*)(lds + LN_RED) + lane;
  red[(w * 2) * 64] = acc[0]; red[(w * 2 + 1) * 64] = acc[1];
  RAWBAR();
  ST();
  if (w < 2) {
    const f32x4 a = (red[w * 64] + red[(2 + w) * 64]) + (red[(4 + w) * 64] + red[(6 + w) * 64]);
    const u32x2 v = {cvtpk(a[0], a[1]), cvtpk(a[2], a[3])};
    char* kvp = ws + WS_KV + (t16 >> 1) * KV_TILE;
    if (kv == 0) {
      const int r = token & 31;
      float n2 = dot2(v[0], v[0], dot2(v[1], v[1], 0.f));
      if (w == 0) {
        *(u32x2*)(kvp + (gq >> 1) * 512 + r * 16 + (gq & 1) * 8) = v;
      } else {
        if (gq == 0) *(u32x2*)(kvp + KV_K2 + r * 8) = v;
        else n2 = 0.f;
        if (gq == 1 && r == 0) { const u32x4 c1 = {0x00003F80u, 0u, 0u, 0u}; *(u32x4*)(kvp + KV_C) = c1; }
      }
      n2 += __shfl_xor(n2, 16); n2 += __shfl_xor(n2, 32);
#pragma unroll
      for (int o = 1; o < 16; o <<= 1) n2 = fmaxf(n2, __shfl_xor(n2, o));
      if (lane == 0) ((float*)(ws + WS_KMAX))[w * 128 + t16] = n2;
    } else {
      char* vp = kvp + KV_V + (((t16 & 1) * 2 + (gq & 1)) * VR) * 16 + (gq >> 1) * 8;
      if (w == 0) *(u32x2*)(vp + r16 * 16) = v;
      else if (r16 < 6) {
        const u32x2 one = {0x3F803F80u, 0x3F803F80u}, zer = {0u, 0u};
        *(u32x2*)(vp + (16 + r16) * 16) = r16 < 4 ? v : (r16 == 4 ? one : zer);
      }
    }
  }
  ST(); ST_OUT(ws, 0);
}

struct FragT { bf16x8 x; s16x4 w[10]; };
DI void q_block(const u16* __restrict__ x, const u16* __restrict__ wq, char* __restrict__ ws, char* lds, int b) {
  const int tid = threadIdx.x, lane = tid & 63, w = __builtin_amdgcn_readfirstlane(tid >> 6);
  const int bi = b >> 3, tt = (b & 7) * 4 + (bi & 3), cg = bi >> 2, odd = cg & 1, c0 = cg * 80 - odd * 16;
  const int ts = w & 3, kh = w >> 2, r16 = lane & 15, gq = lane >> 4, q4 = (lane & 15) >> 2, p = lane & 3;
  const unsigned lds0 = (unsigned)(uintptr_t)(LAS char*)lds;
  ST_DECL ST();
  const int srow = w * 8 + (lane >> 3);
  const unsigned as = (srow * D + ((lane & 7) ^ ((srow >> 1) & 7)) * 8) * 2;
  const u16* ab = x + (size_t)tt * 64 * D;
  const int rr = lane / 12, ch = (lane - rr * 12) ^ ((w & 1) << 1);
  const unsigned bs = ((rr & 3) * D + (c0 + ch * 8 < D ? ch : 0) * 8) * 2;
  const u16* bb = wq + (size_t)w * 8 * D + c0;
  auto piece = [&](int kt, int j) {
    const int slot = kt & 3;
    if (j == 0) glds16s(ab + kt * 64, as, lds0 + slot * SLOT + w * 1024);
    else glds_lin<48>((const char*)(bb + (size_t)(kt * 64 + (j - 1) * 4) * D), bs, lds0 + slot * SLOT + 8192 + (2 * w + j - 1) * 768);
  };
  if (QPRIO) __builtin_amdgcn_s_setprio(QPRIO);
  unsigned pf = 0;
#pragma unroll
  for (int i = 0; i < 4; ++i) { piece(i, 0); piece(i, 1); piece(i, 2); }
  const int xrow = ts * 16 + r16;
  const LAS char* xb = (const LAS char*)lds + xrow * 128 + (((kh * 4 + gq) ^ ((xrow >> 1) & 7)) * 16);
  const LAS char* wb[2];
#pragma unroll
  for (int e = 0; e < 2; ++e) wb[e] = (const LAS char*)lds + 8192 + (kh * 32 + gq * 8 + q4) * 192 + (((e + odd) ^ (gq & 1)) * 32) + p * 8;
  const bf16x8 ones = {0x3F80, 0x3F80, 0x3F80, 0x3F80, 0x3F80, 0x3F80, 0x3F80, 0x3F80};
  f32x4 acc[5] = {}, sx = {}, sxx = {}, csa = {}, csb = {};
  auto rd = [&](FragT& f, int slot) {
    f.x = *(const LAS bf16x8*)(xb + slot * SLOT);
#pragma unroll
    for (int c = 0; c < 5; ++c) { f.w[2 * c] = trread(wb[c & 1] + slot * SLOT + (c >> 1) * 64); f.w[2 * c + 1] = trread(wb[c & 1] + slot * SLOT + (c >> 1) * 64 + 768); }
  };
  auto mm = [&](const FragT& f, int dk) {
#pragma unroll
    for (int c = 0; c < 5; ++c) {
      acc[c] = MFMA16(cat8(f.w[2 * c], f.w[2 * c + 1]), f.x, acc[c]);
      SBAR(); if (dk >= 0 && !(c & 1)) piece(dk, c >> 1); SBAR();
    }
    sx = MFMA16(ones, f.x, sx); sxx = MFMA16(f.x, f.x, sxx);
#pragma unroll
    for (int c = 0; c < 4; ++c) if (ts == c) csa = MFMA16(cat8(f.w[2 * c], f.w[2 * c + 1]), ones, csa);
    if (ts == 0) csb = MFMA16(cat8(f.w[8], f.w[9]), ones, csb);
  };
  FragT fa, fb;
  ST();
  vmwait<9>(); PIN(pf);
  RAWBAR();
  ST();
  rd(fa, 0);
#define IT(kt, FC, FN, VM, DMA) do { vmwait<VM>(); RAWBAR(); rd(FN, ((kt) + 1) & 3); SBAR(); mm(FC, DMA ? (kt) + 4 : -1); SBAR(); } while (0)
  IT(0, fa, fb, 6, true); IT(1, fb, fa, 6, true); IT(2, fa, fb, 6, true); ST(); IT(3, fb, fa, 6, true); IT(4, fa, fb, 6, true);
  IT(5, fb, fa, 6, true); ST(); IT(6, fa, fb, 6, false); IT(7, fb, fa, 3, false); IT(8, fa, fb, 0, false);
  mm(fb, -1);
#undef IT
  ST();
  LAS f32x4* red = (LAS f32x4*)((LAS char*)lds + 2 * SLOT) + ts * 5 * 64 + lane;
  LAS f32x2* stp = (LAS f32x2*)((LAS char*)lds + 3 * SLOT);
  LAS f32x4* csp = (LAS f32x4*)((LAS char*)lds + 3 * SLOT + 1024);
  if (kh == 0) { red[3 * 64] = acc[3]; red[4 * 64] = acc[4]; }
  else { red[0] = acc[0]; red[64] = acc[1]; red[128] = acc[2]; }
  if (gq == q4) stp[kh * 64 + xrow] = (f32x2){sx[0], p == 0 ? sxx[0] : p == 1 ? sxx[1] : p == 2 ? sxx[2] : sxx[3]};
  if (r16 == 0) { csp[(kh * 5 + ts) * 4 + gq] = csa; if (ts == 0) csp[(kh * 5 + 4) * 4 + gq] = csb; }
  RAWBAR();
  float mean_b, rden;
  { const f32x2 a = stp[xrow], c = stp[64 + xrow]; ln_scalars(a[0] + c[0], a[1] + c[1], mean_b, rden); }
  rden *= QSCALE;
  u16* qp = (u16*)(ws + WS_Q) + (size_t)(tt * 8 + cg) * 5120 + xrow * 80 + gq * 4;
#pragma unroll
  for (int c = 0; c < 5; ++c)
    if ((c < 3) == (kh == 0)) {
      const f32x4 a = (acc[c] + red[c * 64] - (csp[c * 4 + gq] + csp[(5 + c) * 4 + gq]) * mean_b) * rden;
      *(u32x2*)(qp + c * 16) = (u32x2){cvtpk(a[0], a[1]), cvtpk(a[2], a[3])};
    }
  ST(); ST_OUT(ws, 5);
}

__global__ __launch_bounds__(512, 4) void k_pre(const u16* __restrict__ x, const u16* __restrict__ wq, const u16* __restrict__ wk,
                                                 const u16* __restrict__ wv, const u16* __restrict__ w1, const u16* __restrict__ w3,
                                                 const u16* __restrict__ w2, const u16* __restrict__ wo, char* __restrict__ ws, const u16* __restrict__ out) {
  extern __shared__ __attribute__((aligned(16))) char lds[];
  const int b = blockIdx.x;
  if (b < 128) ln_block(x, wk, wv, ws, lds, b);
  else if (b < 384) q_block(x, wq, ws, lds, b - 128);
  else {
    const int lane = threadIdx.x & 63, w = __builtin_amdgcn_readfirstlane(threadIdx.x >> 6), rb = b - 384;
    const unsigned lds0 = (unsigned)(uintptr_t)(LAS char*)lds;
    const int u1 = 640 + rb * 8 + w;
    ST_DECL ST();
    unsigned pf = 0;
    if (u1 < 1184) retile_issue(wo, w1, w3, w2, lds0 + w * 6144, u1, lane);
    {
      const int i = (rb >> 3) * 480 + w * 60 + min(lane, 59), row = i / 12, e = i - row * 12;
      touch(((e & 1) ? w3 : w1) + (size_t)row * F + (rb & 3) * 384 + (e >> 1) * 64, pf);
      vmwait<1>();
    }
    if (u1 < 1184) retile_finish(ws, (const LAS char*)lds + w * 6144, u1, lane);
    vmwait<0>(); PIN(pf);
    ST(); ST_OUT(ws, 6);
  }
}

DI void arrive(char* ws, int i, unsigned want) {
  __hip_atomic_fetch_add((GAS unsigned*)(ws + WS_CNT) + i * 16, 1u, RLX_AGENT);
}
DI GAS unsigned* flagp(char* ws, int i) {
  return (GAS unsigned*)(ws + WS_CNT) + i * 16;
}
DI void wait_count(GAS unsigned* cnt, unsigned want) {
  for (unsigned spins = 0; spins < (1u << 22); ++spins) {
    if ((unsigned)__builtin_amdgcn_readfirstlane(__hip_atomic_load(cnt, RLX_AGENT)) >= want) break;
    __builtin_amdgcn_s_sleep(1);
  }
}

constexpr int KV_A = LDS_BYTES - KV_SLOT, KV_B = 0, KV_Cs = KV_SLOT, O_STG = 2 * KV_SLOT;

DI void attn_block(char* __restrict__ ws, char* lds, int b) {
  const int tid = threadIdx.x, lane = tid & 63, w = __builtin_amdgcn_readfirstlane(tid >> 6);
  const int bi = b >> 3;
  const int tt = (b & 7) * 4 + (bi & 3), hg = bi >> 2;
  const int r = lane & 31, h = lane >> 5;
  const unsigned lds0 = (unsigned)(uintptr_t)(LAS char*)lds;
  const unsigned lane16 = lane * 16;
  ST_DECL ST();
  __builtin_amdgcn_s_setprio(3);
  {
    const char* qg = ws + WS_Q + (size_t)(tt * 8 + hg) * 10240;
    glds16s(qg + w * 1024, lane16, lds0 + O_STG + w * 1024);
    if (w < 3) glds16s(w < 2 ? qg + 8192 + w * 1024 : ws + WS_KMAX, lane16, lds0 + O_STG + 8192 + w * 1024);
#pragma unroll
    for (int j = 0; j < 3; ++j) {
      const int pc = (7 - w) + 8 * j;
      if (pc < KV_PIECES) glds16s(ws + WS_KV + pc * 1024, lane16, lds0 + KV_A + pc * 1024);
    }
  }
  if (w >= 4) {
    const int lw = w - 4;
    const char* kvg = ws + WS_KV + lw * 1024;
    auto dma = [&](int chunk, int slotoff) {
#pragma unroll
      for (int j = 0; j < 6; ++j)
        if (j < 5 || lw < KV_PIECES - 20) glds16s(kvg + chunk * KV_CHUNK + j * 4096, lane16, lds0 + slotoff + (lw + 4 * j) * 1024);
    };
    dma(1, KV_B);
    vmwait<5>();
    asm volatile("s_barrier" ::: "memory");
    int fre = KV_Cs, nx = KV_A, nn = KV_B;
#pragma unroll 1
    for (int c = 0; c < 8; ++c) {
      asm volatile("s_waitcnt vmcnt(0)\n\ts_barrier" ::: "memory");
      if (c + 2 < 8) dma(c + 2, fre);
      const int t = fre; fre = nx; nx = nn; nn = t;
    }
  } else {
    asm volatile("s_waitcnt vmcnt(0)\n\ts_barrier" ::: "memory");
    __builtin_amdgcn_s_setprio(APRIO);
    ST();
    u32x4 q1a, q1b; u32x2 q2a, q2b;
    {
      const LAS char* qs = (const LAS char*)lds + O_STG + r * 160 + w * 40;
      const u32x2 a0 = *(const LAS u32x2*)(qs + 16 * h), a1 = *(const LAS u32x2*)(qs + 16 * h + 8);
      const u32x2 b0 = *(const LAS u32x2*)(qs + 5120 + 16 * h), b1 = *(const LAS u32x2*)(qs + 5120 + 16 * h + 8);
      q1a = (u32x4){a0[0], a0[1], a1[0], a1[1]}; q1b = (u32x4){b0[0], b0[1], b1[0], b1[1]};
      q2a = *(const LAS u32x2*)(qs + 32); q2b = *(const LAS u32x2*)(qs + 5120 + 32);
    }
    float km;
    {
      const f32x4 km4 = *(const LAS f32x4*)((LAS char*)lds + O_STG + 10240 + lane * 16);
      int k = __float_as_int(fmaxf(fmaxf(km4[0], km4[1]), fmaxf(km4[2], km4[3])));
      k = max(k, __builtin_amdgcn_update_dpp(0, k, 0x128, 0xf, 0xf, false)); k = max(k, __builtin_amdgcn_update_dpp(0, k, 0x124, 0xf, 0xf, false));
      k = max(k, __builtin_amdgcn_update_dpp(0, k, 0x122, 0xf, 0xf, false)); k = max(k, __builtin_amdgcn_update_dpp(0, k, 0x121, 0xf, 0xf, false));
      km = __int_as_float(max(__builtin_amdgcn_readlane(k, 0), __builtin_amdgcn_readlane(k, 16))) +
           __int_as_float(max(__builtin_amdgcn_readlane(k, 32), __builtin_amdgcn_readlane(k, 48)));
    }
    float qna = dot2(q1a[0], q1a[0], dot2(q1a[1], q1a[1], dot2(q1a[2], q1a[2], dot2(q1a[3], q1a[3], 0.f))));
    float qnb = dot2(q1b[0], q1b[0], dot2(q1b[1], q1b[1], dot2(q1b[2], q1b[2], dot2(q1b[3], q1b[3], 0.f))));
    if (h == 0) { qna = dot2(q2a[0], q2a[0], dot2(q2a[1], q2a[1], qna)); qnb = dot2(q2b[0], q2b[0], dot2(q2b[1], q2b[1], qnb)); }
    { const auto sw = __builtin_amdgcn_permlane32_swap(__float_as_uint(qna), __float_as_uint(qna), false, false); qna = __uint_as_float(sw[0]) + __uint_as_float(sw[1]); }
    { const auto sw = __builtin_amdgcn_permlane32_swap(__float_as_uint(qnb), __float_as_uint(qnb), false, false); qnb = __uint_as_float(sw[0]) + __uint_as_float(sw[1]); }
    const float bda = __builtin_amdgcn_sqrtf(qna * km) * 1.02f + 1e-3f, bdb = __builtin_amdgcn_sqrtf(qnb * km) * 1.02f + 1e-3f;
    const bool fast = __all(bda <= 48.f && bdb <= 48.f);
    if (h == 1) {
      q2a[0] = fast ? (cvtpk(-bda, 0.f) & 0xffffu) : 0u; q2a[1] = 0u;
      q2b[0] = fast ? (cvtpk(-bdb, 0.f) & 0xffffu) : 0u; q2b[1] = 0u;
    }
    const bf16x8 QA1 = __builtin_bit_cast(bf16x8, q1a), QB1 = __builtin_bit_cast(bf16x8, q1b);
    const s16x4 QA2 = __builtin_bit_cast(s16x4, q2a), QB2 = __builtin_bit_cast(s16x4, q2b);
    const int dv = r <= 20 ? r : 21;
    const LAS char* k1b = (const LAS char*)lds + lane * 16;
    const LAS char* k2b = (const LAS char*)lds + (h ? KV_C : KV_K2 + r * 8);
    const LAS char* vb = (const LAS char*)lds + KV_V + (h * VR + dv) * 16;
    f32x16 oa = {}, ob = {};
    const f32x16 zero16 = {};
    ST();
    if (fast) {
      f32x16 sA, sB;
      bf16x8 k1f, vf0, vg0, vf1; s16x4 k2f;
      u32x4 pa, pb;
      float e14, e15;
      k1f = *(const LAS bf16x8*)(k1b + KV_A); k2f = *(const LAS s16x4*)(k2b + KV_A); vf0 = *(const LAS bf16x8*)(vb + KV_A);
      sA = MFMA32(k1f, QA1, zero16); sA = MFMA32K8(k2f, QA2, sA);
      vf1 = (bf16x8){0, 0, 0, 0, 0, 0, 0, 0}; pb = (u32x4){0u, 0u, 0u, 0u}; e14 = 0.f; e15 = 0.f;
      int cur = KV_A, nxt = KV_B, fre = KV_Cs;
#define STEP(C, N, Q1, Q2, OC, OP, V0, L1, L2, L3, L4) do { \
        N = MFMA32(k1f, Q1, zero16); SBAR(); L1; SBAR(); \
        C[0] = EXP2(C[0]); C[1] = EXP2(C[1]); SBAR(); pb[3] = cvtpk(e14, e15); PIN(pb); SBAR(); \
        C[2] = EXP2(C[2]); C[3] = EXP2(C[3]); SBAR(); pa[0] = cvtpk(C[0], C[1]); PIN(pa); SBAR(); \
        OP = MFMA32(vf1, __builtin_bit_cast(bf16x8, pb), OP); SBAR(); L2; SBAR(); \
        C[4] = EXP2(C[4]); C[5] = EXP2(C[5]); SBAR(); pa[1] = cvtpk(C[2], C[3]); PIN(pa); SBAR(); \
        C[6] = EXP2(C[6]); C[7] = EXP2(C[7]); SBAR(); pa[2] = cvtpk(C[4], C[5]); PIN(pa); SBAR(); \
        N = MFMA32K8(k2f, Q2, N); SBAR(); L3; SBAR(); \
        C[8] = EXP2(C[8]); C[9] = EXP2(C[9]); SBAR(); pa[3] = cvtpk(C[6], C[7]); PIN(pa); SBAR(); \
        C[10] = EXP2(C[10]); C[11] = EXP2(C[11]); SBAR(); pb[0] = cvtpk(C[8], C[9]); PIN(pb); SBAR(); \
        OC = MFMA32(V0, __builtin_bit_cast(bf16x8, pa), OC); SBAR(); L4; SBAR(); \
        C[12] = EXP2(C[12]); C[13] = EXP2(C[13]); SBAR(); pb[1] = cvtpk(C[10], C[11]); PIN(pb); SBAR(); \
        e14 = EXP2(C[14]); e15 = EXP2(C[15]); SBAR(); pb[2] = cvtpk(C[12], C[13]); PIN(pb); PIN(e14); PIN(e15); SBAR(); \
      } while (0)
#define KT(i, V0C, V0N) do { \
        STEP(sA, sB, QB1, QB2, oa, ob, vf0, k1f = *(const LAS bf16x8*)((i) < 7 ? kc1 + ((i) + 1) * KV_TILE : k1b + nxt), \
             vf1 = *(const LAS bf16x8*)(vc + (i) * KV_TILE + 2 * VR * 16), k2f = *(const LAS s16x4*)((i) < 7 ? kc2 + ((i) + 1) * KV_TILE : k2b + nxt), (void)0); \
        STEP(sB, sA, QA1, QA2, ob, oa, vf0, (void)0, (void)0, (void)0, vf0 = *(const LAS bf16x8*)((i) < 7 ? vc + ((i) + 1) * KV_TILE : vb + nxt)); \
      } while (0)
#pragma unroll 1
      for (int c = 0; c < 8; ++c) {
        const LAS char* kc1 = k1b + cur; const LAS char* kc2 = k2b + cur; const LAS char* vc = vb + cur;
        KT(0, vf0, vg0); KT(1, vg0, vf0); KT(2, vf0, vg0);
        asm volatile("s_barrier" ::: "memory");
        KT(3, vg0, vf0); KT(4, vf0, vg0); KT(5, vg0, vf0); KT(6, vf0, vg0); KT(7, vg0, vf0);
        const int t = cur; cur = nxt; nxt = fre; fre = t;
      }
      pb[3] = cvtpk(e14, e15);
      ob = MFMA32(vf1, __builtin_bit_cast(bf16x8, pb), ob);
#undef KT
#undef STEP
    } else {
      float ma = -INFINITY, mb = -INFINITY;
      int cur = KV_A, nxt = KV_B, fre = KV_Cs;
#pragma unroll 1
      for (int c = 0; c < 8; ++c) {
#pragma unroll 1
        for (int i = 0; i < 8; ++i) {
          if (i == 3) RAWBAR();
          const int off = cur + i * KV_TILE;
          const bf16x8 k1f = *(const LAS bf16x8*)(k1b + off); const s16x4 k2f = *(const LAS s16x4*)(k2b + off);
          const bf16x8 vf0 = *(const LAS bf16x8*)(vb + off), vf1 = *(const LAS bf16x8*)(vb + off + 2 * VR * 16);
#pragma unroll
          for (int qs = 0; qs < 2; ++qs) {
            f32x16 sc = MFMA32(k1f, qs ? QB1 : QA1, zero16); sc = MFMA32K8(k2f, qs ? QB2 : QA2, sc);
            float& m = qs ? mb : ma; f32x16& o = qs ? ob : oa;
            float mx = sc[0];
#pragma unroll
            for (int j = 1; j < 16; ++j) mx = fmaxf(mx, sc[j]);
            mx = fmaxf(mx, __shfl_xor(mx, 32));
            const float mn = fmaxf(m, mx);
            const float al = EXP2(m - mn);
            m = mn;
#pragma unroll
            for (int j = 0; j < 16; ++j) { o[j] *= al; sc[j] = EXP2(sc[j] - mn); }
            const u32x4 pa = {cvtpk(sc[0], sc[1]), cvtpk(sc[2], sc[3]), cvtpk(sc[4], sc[5]), cvtpk(sc[6], sc[7])};
            const u32x4 pb = {cvtpk(sc[8], sc[9]), cvtpk(sc[10], sc[11]), cvtpk(sc[12], sc[13]), cvtpk(sc[14], sc[15])};
            o = MFMA32(vf0, __builtin_bit_cast(bf16x8, pa), o);
            o = MFMA32(vf1, __builtin_bit_cast(bf16x8, pb), o);
          }
        }
        const int t = cur; cur = nxt; nxt = fre; fre = t;
      }
    }
    PIN(oa); PIN(ob); ST();
#pragma unroll
    for (int qs = 0; qs < 2; ++qs) {
      const f32x16 o = qs ? ob : oa;
      const auto sw = __builtin_amdgcn_permlane32_swap(__float_as_uint(o[8]), __float_as_uint(o[8]), false, false);
      const float il = __builtin_amdgcn_rcpf(__uint_as_float(sw[1]));
      LAS char* os = (LAS char*)lds + O_STG + (qs * 32 + r) * 176 + w * 40 + 8 * h;
      *(LAS u32x2*)(os) = (u32x2){cvtpk(o[0] * il, o[1] * il), cvtpk(o[2] * il, o[3] * il)};
      *(LAS u32x2*)(os + 16) = (u32x2){cvtpk(o[4] * il, o[5] * il), cvtpk(o[6] * il, o[7] * il)};
      if (h == 0) *(LAS u32x2*)(os + 32) = (u32x2){cvtpk(o[8] * il, o[9] * il), cvtpk(o[10] * il, o[11] * il)};
    }
  }
  RAWBAR();
  const __amdgpu_buffer_rsrc_t orr = __builtin_amdgcn_make_buffer_rsrc(ws + WS_O, 0, S * D * 2, 0x00020000);
#pragma unroll
  for (int j = 0; j < 2; ++j) {
    const int ci = tid + 512 * j;
    if (ci < 640) {
      const int c = ci >> 6, cc = hg * 10 + c;
      const u32x4 v = *(const LAS u32x4*)((LAS char*)lds + O_STG + lane * 176 + c * 16);
      __builtin_amdgcn_raw_buffer_store_b128(v, orr, ((tt * 20 + (cc >> 2)) * 4 + (lane >> 4)) * 1024 + ((cc & 3) * 16 + (lane & 15)) * 16, 0, 16);
    }
  }
  vmwait<0>();
  RAWBAR();
  if (tid == 0) arrive(ws, 16 + tt, 8u);
}

struct Frag3 { bf16x8 x; s16x4 w[6]; };
struct Frag5 { bf16x8 x; bf16x8 w[5]; };
DI void ffn_block(const u16* __restrict__ x, const u16* __restrict__ w1, const u16* __restrict__ w3, char* __restrict__ ws, u16* __restrict__ out, char* lds, int b) {
  const int tid = threadIdx.x, lane = tid & 63, w = __builtin_amdgcn_readfirstlane(tid >> 6);
  const int bi = b >> 3;
  const unsigned lds0 = (unsigned)(uintptr_t)(LAS char*)lds, lane16 = lane * 16;
  ST_DECL ST();
  if (GPRIO) __builtin_amdgcn_s_setprio(GPRIO);
  {
    const int tp = ((b & 7) >> 2) * 8 + (bi & 7), cg = (b & 3) * 4 + (bi >> 3);
    const char* xsb = ws + WS_XN + (size_t)tp * 163840 + w * 1024;
    const int rr = lane / 12, pos = lane - rr * 12;
    const unsigned bs = ((rr & 3) * F + (pos ^ ((w >> 2) << 1)) * 8) * 2;
    const u16* bb = (w < 4 ? w1 : w3) + (size_t)(w & 3) * 8 * F + cg * 96;
    auto piece = [&](int kt, int slot, int j) {
      if (j == 0) glds16s(xsb + kt * 8192, lane16, lds0 + slot * SLOT + w * 1024);
      else glds_lin<48>((const char*)(bb + (size_t)(kt * 32 + (j - 1) * 4) * F), bs, lds0 + slot * SLOT + 8192 + (2 * w + j - 1) * 768);
    };
#pragma unroll
    for (int i = 0; i < 4; ++i) { piece(i, i, 0); piece(i, i, 1); piece(i, i, 2); }
    const int tq = w & 3, hc = w >> 2, r = lane & 31, h = lane >> 5;
    const int xrow = tq * 32 + r;
    const LAS char* xb0 = (const LAS char*)lds + xrow * 64 + ((h ^ ((xrow >> 2) & 3)) * 16);
    const LAS char* xb1 = (const LAS char*)lds + xrow * 64 + (((2 + h) ^ ((xrow >> 2) & 3)) * 16);
    const int gu = (lane >> 4) & 1, q4 = (lane & 15) >> 2, p = lane & 3, pp = ((p & 1) << 1) | (p >> 1);
    const LAS char* wb[3];
#pragma unroll
    for (int c = 0; c < 3; ++c) wb[c] = (const LAS char*)lds + 8192 + gu * 6144 + (8 * h + q4) * 192 + (((hc * 3 + c) ^ gu) * 32) + pp * 8;
    f32x16 acc[3] = {};
    auto rd = [&](Frag3& f, int slot, int s) {
      f.x = *(const LAS bf16x8*)((s ? xb1 : xb0) + slot * SLOT);
#pragma unroll
      for (int c = 0; c < 3; ++c) { f.w[2 * c] = trread(wb[c] + slot * SLOT + s * 3072); f.w[2 * c + 1] = trread(wb[c] + slot * SLOT + s * 3072 + 768); }
    };
    auto mm = [&](const Frag3& f, int s, int dk, int dsl) {
#pragma unroll
      for (int c = 0; c < 3; ++c) {
        acc[c] = MFMA32(cat8(f.w[2 * c], f.w[2 * c + 1]), f.x, acc[c]);
        SBAR();
        if (dk >= 0) { if (s == 1 && c == 0) piece(dk, dsl, 0); if (s == 1 && c == 2) piece(dk, dsl, 1); if (s == 0 && c == 0) piece(dk, dsl, 2); }
        SBAR();
      }
    };
    Frag3 f0, f1;
    vmwait<9>();
    RAWBAR();
    rd(f0, 0, 0);
#define IT(kt, sl, VM, PREV, DMA, NEXT) do { \
      rd(f1, sl, 1); SBAR(); mm(f0, 0, PREV ? (kt) + 3 : -1, ((sl) + 3) & 3); SBAR(); \
      if (NEXT) { vmwait<VM>(); RAWBAR(); rd(f0, ((sl) + 1) & 3, 0); } \
      SBAR(); mm(f1, 1, DMA ? (kt) + 4 : -1, sl); SBAR(); } while (0)
    IT(0, 0, 6, false, true, true); IT(1, 1, 6, true, true, true); IT(2, 2, 6, true, true, true); IT(3, 3, 6, true, true, true);
#pragma unroll 1
    for (int kt = 4; kt < 16; kt += 4) {
      if (kt == G1LATE) __builtin_amdgcn_s_setprio(3);
      IT(kt, 0, 6, true, true, true); IT(kt + 1, 1, 6, true, true, true); IT(kt + 2, 2, 6, true, true, true); IT(kt + 3, 3, 6, true, true, true);
    }
    IT(16, 0, 6, true, false, true); IT(17, 1, 3, false, false, true); IT(18, 2, 0, false, false, true); IT(19, 3, 0, false, false, false);
#undef IT
    PIN(acc[0]); PIN(acc[1]); PIN(acc[2]); ST();
    __builtin_amdgcn_s_setprio(3);
    u32x4 hv[3];
#pragma unroll
    for (int c = 0; c < 3; ++c) {
      float v[8];
#pragma unroll
      for (int j = 0; j < 8; ++j) {
        const float gt = acc[c][j], up = acc[c][j + 8];
        v[j] = gt * __builtin_amdgcn_rcpf(1.f + EXP2(-1.4426950408889634f * gt)) * up;
      }
      hv[c] = (u32x4){cvtpk(v[0], v[1]), cvtpk(v[2], v[3]), cvtpk(v[4], v[5]), cvtpk(v[6], v[7])};
    }
#pragma unroll
    for (int c = 0; c < 3; ++c) *(LAS u32x4*)((LAS char*)lds + xrow * 208 + hc * 96 + c * 32 + h * 16) = hv[c];
    RAWBAR();
    const __amdgpu_buffer_rsrc_t hr = __builtin_amdgcn_make_buffer_rsrc(ws + WS_H, 0, S * F * 2, 0x00020000);
#pragma unroll
    for (int j = 0; j < 3; ++j) {
      const int ci = tid + 512 * j, row = ci / 12, pos = ci - row * 12;
      const u32x4 v = *(const LAS u32x4*)((LAS char*)lds + row * 208 + pos * 16);
      __builtin_amdgcn_raw_buffer_store_b128(v, hr, ((tp * 128 + row) * F + cg * 96 + pos * 8) * 2, 0, 16);
    }
    vmwait<0>();
    RAWBAR();
    if (tid == 448) arrive(ws, tp, 16u);
  }
  ST();
  const int tt = (b & 7) * 4 + (bi & 3), t0 = tt * 64, cg = bi >> 2, c0 = cg * 80;
  const int ts = w & 3, kh = w >> 2, r16 = lane & 15, gq = lane >> 4, xrow = ts * 16 + r16;
  f32x4 acc[5] = {};
  const char* wog = ws + WS_WO + (size_t)cg * 102400;
  bf16x8 wr[10];
  auto wop = [&](int j) { glds16s(wog + (j >= 5 ? 10240 : 0) + (j * 8 + w) * 1024, lane16, lds0 + (j * 8 + w) * 1024); };
  {
    const int srow = w * 8 + (lane >> 3);
    const unsigned hs = (srow * F + ((lane & 7) ^ ((srow >> 1) & 7)) * 8) * 2;
    const u16* hb = (const u16*)(ws + WS_H) + (size_t)t0 * F;
    const char* wsrc = ws + WS_W2 + (size_t)cg * 245760 + w * 1280;
    auto piece = [&](int kt, int slot, int j) {
      if (j == 0) glds16s_sc1(hb + kt * 64, hs, lds0 + slot * ST64 + w * 1024);
      else glds_lin<40>(wsrc + kt * 10240 + (j - 1) * 640, lane16, lds0 + slot * ST64 + 8192 + w * 1280 + (j - 1) * 640);
    };
#pragma unroll
    for (int i = 0; i < 4; ++i) { piece(i, i, 1); piece(i, i, 2); }
    if (kh == 0) {
#pragma unroll
      for (int c = 0; c < 5; ++c) {
        const u32x2 xv = *(const u32x2*)(x + (size_t)(t0 + xrow) * D + c0 + c * 16 + gq * 4);
        acc[c] = (f32x4){bflo(xv[0]), bfhi(xv[0]), bflo(xv[1]), bfhi(xv[1])};
        PIN(acc[c]);
      }
    }
    if (w == 0) wait_count(flagp(ws, tt >> 1), 16u);
    RAWBAR();
    __builtin_amdgcn_s_setprio(P2PRIO);
    ST();
#pragma unroll
    for (int i = 0; i < 4; ++i) piece(i, i, 0);
    const LAS char* xb = (const LAS char*)lds + xrow * 128 + (((kh * 4 + gq) ^ ((xrow >> 1) & 7)) * 16);
    const LAS char* wb = (const LAS char*)lds + 8192 + kh * 5120 + lane * 16;
    auto rd = [&](Frag5& f, int slot) {
      f.x = *(const LAS bf16x8*)(xb + slot * ST64);
#pragma unroll
      for (int c = 0; c < 5; ++c) f.w[c] = *(const LAS bf16x8*)(wb + slot * ST64 + c * 1024);
    };
    auto mm = [&](const Frag5& f, int dk, int dsl) {
#pragma unroll
      for (int c = 0; c < 5; ++c) { acc[c] = MFMA16(f.w[c], f.x, acc[c]); SBAR(); if (dk >= 0 && !(c & 1)) piece(dk, dsl, c >> 1); SBAR(); }
    };
    Frag5 fa, fb;
    vmwait<3>();
    RAWBAR();
    rd(fa, 0);
#define IT(kt, sl, FC, FN, VM, DMA) do { vmwait<VM>(); RAWBAR(); rd(FN, ((sl) + 1) & 3); SBAR(); mm(FC, DMA ? (kt) + 4 : -1, sl); SBAR(); } while (0)
    IT(0, 0, fa, fb, 2, true); IT(1, 1, fb, fa, 4, true); IT(2, 2, fa, fb, 6, true); IT(3, 3, fb, fa, 6, true);
#pragma unroll 1
    for (int kt = 4; kt < 20; kt += 4) {
      IT(kt, 0, fa, fb, 6, true); IT(kt + 1, 1, fb, fa, 6, true); IT(kt + 2, 2, fa, fb, 6, true); IT(kt + 3, 3, fb, fa, 6, true);
    }
    IT(20, 0, fa, fb, 6, false); wop(0); wop(1); wop(9);
    {
      const char* wrp = wog + kh * 51200 + 40960;
#pragma unroll
      for (int j = 0; j < 10; ++j) asm volatile("global_load_dwordx4 %0, %1, %2" : "=v"(wr[j]) : "v"(lane16), "s"(wrp + j * 1024) : "memory");
    }
    IT(21, 1, fb, fa, 16, false); wop(2); wop(3);
    IT(22, 2, fa, fb, 15, false); wop(4); wop(5);
    mm(fb, -1, 0);
#undef IT
  }
  PIN(acc[0]); ST();
  RAWBAR();
  wop(6); wop(7); wop(8);
  asm volatile("s_waitcnt vmcnt(0)" : "+v"(wr[0]), "+v"(wr[1]), "+v"(wr[2]), "+v"(wr[3]), "+v"(wr[4]), "+v"(wr[5]), "+v"(wr[6]), "+v"(wr[7]), "+v"(wr[8]), "+v"(wr[9]) :: "memory");
  vmwait<0>();
  __builtin_amdgcn_s_setprio(3);
  ST();
  const LAS char* wbt = (const LAS char*)lds + kh * 40960 + lane * 16;
  if (w == 0) wait_count(flagp(ws, 16 + tt), 8u);
  RAWBAR();
  ST();
  {
    u32x4 of[10];
    const char* op = ws + WS_O + ((tt * 20 + kh * 10) * 4 + ts) * 1024;
#pragma unroll
    for (int s = 0; s < 10; ++s) asm volatile("global_load_dwordx4 %0, %1, %2 sc1" : "=v"(of[(s + 8) % 10]) : "v"(lane16), "s"(op + ((s + 8) % 10) * 4096) : "memory");
#define OSTEP(s, N, WC, WN) do { \
      if ((s) + 1 < 8) { _Pragma("unroll") for (int c = 0; c < 5; ++c) WN[c] = *(const LAS bf16x8*)(wbt + ((s) + 1) * 5120 + c * 1024); } \
      SBAR(); asm volatile("s_waitcnt vmcnt(" #N ")" : "+v"(of[s]) :: "memory"); SBAR(); \
      _Pragma("unroll") for (int c = 0; c < 5; ++c) acc[c] = MFMA16(WC[c], __builtin_bit_cast(bf16x8, of[s]), acc[c]); SBAR(); } while (0)
    asm volatile("s_waitcnt vmcnt(9)" : "+v"(of[8]) :: "memory");
#pragma unroll
    for (int c = 0; c < 5; ++c) acc[c] = MFMA16(wr[c], __builtin_bit_cast(bf16x8, of[8]), acc[c]);
    SBAR();
    bf16x8 w0[5];
#pragma unroll
    for (int c = 0; c < 5; ++c) w0[c] = *(const LAS bf16x8*)(wbt + c * 1024);
    SBAR(); asm volatile("s_waitcnt vmcnt(8)" : "+v"(of[9]) :: "memory"); SBAR();
#pragma unroll
    for (int c = 0; c < 5; ++c) acc[c] = MFMA16(wr[5 + c], __builtin_bit_cast(bf16x8, of[9]), acc[c]);
    SBAR();
    bf16x8 wn[5];
    OSTEP(0, 7, w0, wn); OSTEP(1, 6, wn, w0); OSTEP(2, 5, w0, wn); OSTEP(3, 4, wn, w0);
    OSTEP(4, 3, w0, wn); OSTEP(5, 2, wn, w0); OSTEP(6, 1, w0, wn); OSTEP(7, 0, wn, w0);
#undef OSTEP
  }
  RAWBAR();
#pragma unroll
  for (int c = 0; c < 5; ++c) *(LAS f32x4*)((LAS char*)lds + kh * 64 * 336 + xrow * 336 + c * 64 + gq * 16) = acc[c];
  RAWBAR();
#pragma unroll
  for (int j = 0; j < 2; ++j) {
    const int ci = tid + 512 * j;
    if (ci < 640) {
      const int row = ci / 10, c = ci - row * 10;
      const LAS f32x4* a0 = (const LAS f32x4*)((LAS char*)lds + row * 336 + c * 32);
      const LAS f32x4* a1 = (const LAS f32x4*)((LAS char*)lds + 64 * 336 + row * 336 + c * 32);
      const f32x4 lo = a0[0] + a1[0], hi = a0[1] + a1[1];
      const u32x4 res = {cvtpk(lo[0], lo[1]), cvtpk(lo[2], lo[3]), cvtpk(hi[0], hi[1]), cvtpk(hi[2], hi[3])};
      *(u32x4*)(out + (size_t)(t0 + row) * D + c0 + c * 8) = res;
    }
  }
  ST(); ST_OUT(ws, 2);
}

__global__ __launch_bounds__(512, 4) void k_mid(const u16* __restrict__ x, const u16* __restrict__ w1, const u16* __restrict__ w3, char* __restrict__ ws, u16* __restrict__ out) {
  extern __shared__ __attribute__((aligned(16))) char lds[];
  if (blockIdx.x < 256) attn_block(ws, lds, blockIdx.x);
  else ffn_block(x, w1, w3, ws, out, lds, blockIdx.x - 256);
}

extern "C" void kernel_launch(void* const* d_in, const int* in_sizes, int n_in, void* d_out, int out_size, void* d_ws, size_t ws_size,
                              hipStream_t stream) {
  const u16* x = (const u16*)d_in[0];
  const u16* wq = (const u16*)d_in[1];
  const u16* wk = (const u16*)d_in[2];
  const u16* wv = (const u16*)d_in[3];
  const u16* wo = (const u16*)d_in[4];
  const u16* w1 = (const u16*)d_in[6];
  const u16* w3 = (const u16*)d_in[7];
  const u16* w2 = (const u16*)d_in[8];
  char* ws = (char*)d_ws;
  (void)hipFuncSetAttribute((const void*)k_mid, hipFuncAttributeMaxDynamicSharedMemorySize, LDS_BYTES);
  (void)hipFuncSetAttribute((const void*)k_pre, hipFuncAttributeMaxDynamicSharedMemorySize, LDS_BYTES);
  k_pre<<<512, 512, LDS_BYTES, stream>>>(x, wq, wk, wv, w1, w3, w2, wo, ws, (const u16*)d_out);
  k_mid<<<512, 512, LDS_BYTES, stream>>>(x, w1, w3, ws, (u16*)d_out);
}
```

```cpp
#include <hip/hip_runtime.h>
#include <hip/hip_bf16.h>
#include <stdint.h>

typedef short bf16x8 __attribute__((ext_vector_type(8)));
typedef short s16x4 __attribute__((ext_vector_type(4)));
typedef float f32x2 __attribute__((ext_vector_type(2)));
typedef float f32x4 __attribute__((ext_vector_type(4)));
typedef float f32x16 __attribute__((ext_vector_type(16)));
typedef unsigned u32x4 __attribute__((ext_vector_type(4)));
typedef unsigned u32x2 __attribute__((ext_vector_type(2)));
typedef __bf16 bf16x2_t __attribute__((ext_vector_type(2)));
typedef unsigned short u16;
#define LAS __attribute__((address_space(3)))
#define GAS __attribute__((address_space(1)))
#define RLX_AGENT __ATOMIC_RELAXED, __HIP_MEMORY_SCOPE_AGENT
#define DI __device__ __forceinline__
#define SBAR() __builtin_amdgcn_sched_barrier(0)
#define PIN(x) asm volatile("" : "+v"(x))

constexpr int GPRIO = 1, APRIO = 2;
constexpr int S = 2048, D = 640, H = 32, DH = 20, F = 1536;
constexpr int VR = 22;
constexpr int KV_K2 = 1024, KV_C = 1280, KV_V = 1296, KV_TILE = KV_V + 4 * VR * 16;
constexpr int KV_CHUNK = 8 * KV_TILE;
constexpr int KV_PIECES = (KV_CHUNK + 1023) / 1024, KV_SLOT = KV_PIECES * 1024;
constexpr size_t WS_CNT = 0;
constexpr size_t WS_KMAX = 12288;
constexpr size_t WS_KV = 16384;
constexpr size_t WS_XN = WS_KV + 192 * 1024;
constexpr size_t WS_O = WS_XN + (size_t)S * D * 2;
constexpr size_t WS_H = WS_O + (size_t)S * D * 2;
constexpr size_t WS_Q = WS_H + (size_t)S * F * 2;
constexpr size_t WS_WO = WS_Q + (size_t)S * D * 2;
constexpr size_t WS_W2 = WS_WO + (size_t)D * D * 2;
constexpr float QSCALE = 0.22360679774997896f * 1.4426950408889634f;
constexpr int LDS_BYTES = 81920, SLOT = 20480;

DI unsigned cvtpk(float a, float b) { f32x2 v = {a, b}; bf16x2_t r = __builtin_convertvector(v, bf16x2_t); return __builtin_bit_cast(unsigned, r); }
DI float bflo(unsigned u) { return __uint_as_float(u << 16); }
DI float bfhi(unsigned u) { return __uint_as_float(u & 0xffff0000u); }
DI float rbf(float a) { return bflo(cvtpk(a, a)); }
DI float dot2(unsigned a, unsigned b, float c) { return __builtin_amdgcn_fdot2_f32_bf16(__builtin_bit_cast(bf16x2_t, a), __builtin_bit_cast(bf16x2_t, b), c, false); }
DI s16x4 trread(const LAS char* p) { return __builtin_bit_cast(s16x4, __builtin_amdgcn_ds_read_tr16_b64_v4i16((LAS s16x4*)p)); }
DI bf16x8 cat8(s16x4 a, s16x4 b) { return __builtin_shufflevector(a, b, 0, 1, 2, 3, 4, 5, 6, 7); }
DI void glds16s(const void* sbase, unsigned voff, unsigned lds_base) {
  unsigned sv;
  asm volatile("s_mov_b32 %0, m0\n\ts_mov_b32 m0, %3\n\ts_nop 0\n\tglobal_load_lds_dwordx4 %1, %2\n\ts_mov_b32 m0, %0" : "=&s"(sv) : "v"(voff), "s"(sbase), "s"(lds_base) : "memory");
}
DI void glds16s_sc1(const void* sbase, unsigned voff, unsigned lds_base) {
  unsigned sv;
  asm volatile("s_mov_b32 %0, m0\n\ts_mov_b32 m0, %3\n\ts_nop 0\n\tglobal_load_lds_dwordx4 %1, %2 sc1\n\ts_mov_b32 m0, %0" : "=&s"(sv) : "v"(voff), "s"(sbase), "s"(lds_base) : "memory");
}
#define MFMA32(a, b, c) __builtin_amdgcn_mfma_f32_32x32x16_bf16(a, b, c, 0, 0, 0)
#define MFMA32K8(a, b, c) __builtin_amdgcn_mfma_f32_32x32x8bf16_1k(a, b, c, 0, 0, 0)
#define MFMA16(a, b, c) __builtin_amdgcn_mfma_f32_16x16x32_bf16(a, b, c, 0, 0, 0)
#define EXP2(v) __builtin_amdgcn_exp2f(v)
#define RAWBAR() asm volatile("s_waitcnt lgkmcnt(0)\n\ts_barrier" ::: "memory")
DI void touch(const void* p, unsigned& d) { asm volatile("global_load_dword %0, %1, off" : "+v"(d) : "v"(p) : "memory"); }
template <int N> DI void vmwait() { asm volatile("s_waitcnt vmcnt(%0)" :: "n"(N) : "memory"); }
DI void ln_scalars(float sum, float sq, float& mean_b, float& rden) {
  const float mean = sum * (1.f / D);
  const float var = fmaxf(sq * (1.f / D) - mean * mean, 0.f);
  mean_b = rbf(mean);
  rden = __builtin_amdgcn_rcpf(rbf(__builtin_amdgcn_sqrtf(rbf(rbf(var) + 1e-5f))));
}

template <int NL> DI void glds_lin(const char* src, unsigned lane16, unsigned dst) {
  unsigned sv;
  asm volatile("s_mov_b32 %0, m0\n\ts_mov_b32 m0, %3\n\ts_mov_b32 exec_hi, %4\n\tglobal_load_lds_dwordx4 %1, %2\n\ts_mov_b32 exec_hi, -1\n\ts_mov_b32 m0, %0"
               : "=&s"(sv) : "v"(lane16), "s"(src), "s"(dst), "n"((1u << (NL - 32)) - 1u) : "memory");
}

constexpr int ST64 = 18432;

DI void retile_issue(const u16* __restrict__ wo, const u16* __restrict__ w2, unsigned ldsu, int u, int lane) {
  const int i = u < 160 ? u : u - 160, nk = u < 160 ? 20 : 48, cg = i / nk, ks = i - cg * nk, c0 = cg * 80 - (cg & 1) * 16;
  const u16* src = (u < 160 ? wo : w2) + (size_t)ks * 32 * D + c0;
#pragma unroll
  for (int j = 0; j < 3; ++j) {
    const int ci = j * 64 + lane, rr = ci / 12, pos = ci - rr * 12;
    const unsigned vo = (rr * D + (c0 + pos * 8 < D ? pos : 0) * 8) * 2;
    glds16s(src, vo, ldsu + j * 1024);
    glds16s(src + 16 * D, vo, ldsu + 3072 + j * 1024);
  }
}
DI void retile_finish(char* __restrict__ ws, const LAS char* im, int u, int lane) {
  const int i = u < 160 ? u : u - 160, cg = i / (u < 160 ? 20 : 48);
  const LAS char* a = im + ((lane >> 4) * 8 + ((lane & 15) >> 2)) * 192 + (cg & 1) * 32 + (lane & 3) * 8;
  char* dst = ws + (u < 160 ? WS_WO : WS_W2) + (size_t)i * 5120 + lane * 16;
#pragma unroll
  for (int c = 0; c < 5; ++c) *(bf16x8*)(dst + c * 1024) = cat8(trread(a + c * 32), trread(a + c * 32 + 768));
}

constexpr int LN_ST = 25600, LN_RED = LN_ST + 512, LN_HALF = LN_RED + 8192;
DI void ln_block(const u16* __restrict__ x, const u16* __restrict__ wk, const u16* __restrict__ wv, char* __restrict__ ws, char* ldsb, int b) {
  const int tid = threadIdx.x, lane = tid & 63, w8 = __builtin_amdgcn_readfirstlane(tid >> 6), w = w8 & 3, kv = w8 >> 2;
  const int t16 = (b & 7) * 16 + (b >> 3);
  const int r16 = lane & 15, gq = lane >> 4;
  LAS char* lds = (LAS char*)ldsb + kv * LN_HALF;
  const unsigned lds0 = (unsigned)(uintptr_t)lds;
  const int token = t16 * 16 + r16;
  __builtin_amdgcn_s_setprio(3);
  u32x4 xf[5];
  const u16* xp = x + (size_t)token * D + w * 160 + gq * 8;
#pragma unroll
  for (int s = 0; s < 5; ++s) asm volatile("global_load_dwordx4 %0, %1, off offset:%2" : "=v"(xf[s]) : "v"(xp), "i"(s * 64) : "memory");
  const u16* wsrc = kv ? wv : wk;
#pragma unroll
  for (int j = 0; j < 7; ++j)
    if (j < 6 || w == 0) glds16s(wsrc + (w + 4 * j) * 512, lane * 16, lds0 + (w + 4 * j) * 1024);
  asm volatile("s_waitcnt vmcnt(0)" : "+v"(xf[0]), "+v"(xf[1]), "+v"(xf[2]), "+v"(xf[3]), "+v"(xf[4]) :: "memory");
  float sum = 0.f, sq = 0.f, sum2 = 0.f, sq2 = 0.f;
#pragma unroll
  for (int s = 0; s < 5; ++s)
#pragma unroll
    for (int j = 0; j < 4; j += 2) {
      sum = dot2(xf[s][j], 0x3F803F80u, sum); sq = dot2(xf[s][j], xf[s][j], sq);
      sum2 = dot2(xf[s][j + 1], 0x3F803F80u, sum2); sq2 = dot2(xf[s][j + 1], xf[s][j + 1], sq2);
    }
  sum += sum2; sq += sq2;
  sum += __shfl_xor(sum, 16); sq += __shfl_xor(sq, 16);
  sum += __shfl_xor(sum, 32); sq += __shfl_xor(sq, 32);
  if (b == 0 && tid < 48) ((unsigned*)(ws + WS_CNT))[tid * 16] = 0u;
  LAS f32x2* stp = (LAS f32x2*)(lds + LN_ST);
  if (gq == 0) stp[w * 16 + r16] = (f32x2){sum, sq};
  RAWBAR();
  { const f32x2 a = stp[r16], c = stp[16 + r16], d = stp[32 + r16], e = stp[48 + r16]; sum = (a[0] + c[0]) + (d[0] + e[0]); sq = (a[1] + c[1]) + (d[1] + e[1]); }
  float mean_b, rden;
  ln_scalars(sum, sq, mean_b, rden);

#pragma unroll
  for (int s = 0; s < 5; ++s)
#pragma unroll
    for (int j = 0; j < 4; ++j) {
      const unsigned d = cvtpk(bflo(xf[s][j]) - mean_b, bfhi(xf[s][j]) - mean_b);
      xf[s][j] = cvtpk(bflo(d) * rden, bfhi(d) * rden);
    }
  if ((w >> 1) == kv) {
    const int row = token & 127;
    char* xn = ws + WS_XN + ((size_t)((token >> 7) * 20 + w * 5) * 128 + row) * 64 + ((gq ^ ((row >> 2) & 3)) * 16);
#pragma unroll
    for (int s = 0; s < 5; ++s) *(u32x4*)(xn + s * 8192) = xf[s];
  }
  const int q4 = (lane & 15) >> 2, p = lane & 3;
  const LAS char* e0 = lds + (w * 160 + gq * 8 + q4) * 40 + p * 8;
  const LAS char* e1 = lds + (w * 160 + gq * 8 + q4) * 40 + 32;
  f32x4 acc[2] = {};
  if (kv == 0) {
#pragma unroll
    for (int s = 0; s < 5; ++s) {
      acc[0] = MFMA16(cat8(trread(e0 + s * 1280), trread(e0 + s * 1280 + 160)), __builtin_bit_cast(bf16x8, xf[s]), acc[0]);
      acc[1] = MFMA16(cat8(trread(e1 + s * 1280), trread(e1 + s * 1280 + 160)), __builtin_bit_cast(bf16x8, xf[s]), acc[1]);
    }
  } else {
#pragma unroll
    for (int s = 0; s < 5; ++s) {
      acc[0] = MFMA16(__builtin_bit_cast(bf16x8, xf[s]), cat8(trread(e0 + s * 1280), trread(e0 + s * 1280 + 160)), acc[0]);
      acc[1] = MFMA16(__builtin_bit_cast(bf16x8, xf[s]), cat8(trread(e1 + s * 1280), trread(e1 + s * 1280 + 160)), acc[1]);
    }
  }
  LAS f32x4* red = (LAS f32x4*)(lds + LN_RED) + lane;
  red[(w * 2) * 64] = acc[0]; red[(w * 2 + 1) * 64] = acc[1];
  RAWBAR();
  if (w < 2) {
    const f32x4 a = (red[w * 64] + red[(2 + w) * 64]) + (red[(4 + w) * 64] + red[(6 + w) * 64]);
    const u32x2 v = {cvtpk(a[0], a[1]), cvtpk(a[2], a[3])};
    char* kvp = ws + WS_KV + (t16 >> 1) * KV_TILE;
    if (kv == 0) {
      const int r = token & 31;
      float n2 = dot2(v[0], v[0], dot2(v[1], v[1], 0.f));
      if (w == 0) {
        *(u32x2*)(kvp + (gq >> 1) * 512 + r * 16 + (gq & 1) * 8) = v;
      } else {
        if (gq == 0) *(u32x2*)(kvp + KV_K2 + r * 8) = v;
        else n2 = 0.f;
        if (gq == 1 && r == 0) { const u32x4 c1 = {0x00003F80u, 0u, 0u, 0u}; *(u32x4*)(kvp + KV_C) = c1; }
      }
      n2 += __shfl_xor(n2, 16); n2 += __shfl_xor(n2, 32);
#pragma unroll
      for (int o = 1; o < 16; o <<= 1) n2 = fmaxf(n2, __shfl_xor(n2, o));
      if (lane == 0) ((float*)(ws + WS_KMAX))[w * 128 + t16] = n2;
    } else {
      char* vp = kvp + KV_V + (((t16 & 1) * 2 + (gq & 1)) * VR) * 16 + (gq >> 1) * 8;
      if (w == 0) *(u32x2*)(vp + r16 * 16) = v;
      else if (r16 < 6) {
        const u32x2 one = {0x3F803F80u, 0x3F803F80u}, zer = {0u, 0u};
        *(u32x2*)(vp + (16 + r16) * 16) = r16 < 4 ? v : (r16 == 4 ? one : zer);
      }
    }
  }
}

struct FragT { bf16x8 x; s16x4 w[10]; };
DI void q_block(const u16* __restrict__ x, const u16* __restrict__ wq, char* __restrict__ ws, char* lds, int b) {
  const int tid = threadIdx.x, lane = tid & 63, w = __builtin_amdgcn_readfirstlane(tid >> 6);
  const int bi = b >> 3, tt = (b & 7) * 4 + (bi & 3), cg = bi >> 2, odd = cg & 1, c0 = cg * 80 - odd * 16;
  const int ts = w & 3, kh = w >> 2, r16 = lane & 15, gq = lane >> 4, q4 = (lane & 15) >> 2, p = lane & 3;
  const unsigned lds0 = (unsigned)(uintptr_t)(LAS char*)lds;
  const int srow = w * 8 + (lane >> 3);
  const unsigned as = (srow * D + ((lane & 7) ^ ((srow >> 1) & 7)) * 8) * 2;
  const u16* ab = x + (size_t)tt * 64 * D;
  const int rr = lane / 12, ch = (lane - rr * 12) ^ ((w & 1) << 1);
  const unsigned bs = ((rr & 3) * D + (c0 + ch * 8 < D ? ch : 0) * 8) * 2;
  const u16* bb = wq + (size_t)w * 8 * D + c0;
  auto piece = [&](int kt, int j) {
    const int slot = kt & 3;
    if (j == 0) glds16s(ab + kt * 64, as, lds0 + slot * SLOT + w * 1024);
    else glds_lin<48>((const char*)(bb + (size_t)(kt * 64 + (j - 1) * 4) * D), bs, lds0 + slot * SLOT + 8192 + (2 * w + j - 1) * 768);
  };
#pragma unroll
  for (int i = 0; i < 4; ++i) { piece(i, 0); piece(i, 1); piece(i, 2); }
  const int xrow = ts * 16 + r16;
  const LAS char* xb = (const LAS char*)lds + xrow * 128 + (((kh * 4 + gq) ^ ((xrow >> 1) & 7)) * 16);
  const LAS char* wb[2];
#pragma unroll
  for (int e = 0; e < 2; ++e) wb[e] = (const LAS char*)lds + 8192 + (kh * 32 + gq * 8 + q4) * 192 + (((e + odd) ^ (gq & 1)) * 32) + p * 8;
  const bf16x8 ones = {0x3F80, 0x3F80, 0x3F80, 0x3F80, 0x3F80, 0x3F80, 0x3F80, 0x3F80};
  f32x4 acc[5] = {}, sx = {}, sxx = {}, csa = {}, csb = {};
  auto rd = [&](FragT& f, int slot) {
    f.x = *(const LAS bf16x8*)(xb + slot * SLOT);
#pragma unroll
    for (int c = 0; c < 5; ++c) { f.w[2 * c] = trread(wb[c & 1] + slot * SLOT + (c >> 1) * 64); f.w[2 * c + 1] = trread(wb[c & 1] + slot * SLOT + (c >> 1) * 64 + 768); }
  };
  auto mm = [&](const FragT& f, int dk) {
#pragma unroll
    for (int c = 0; c < 5; ++c) {
      acc[c] = MFMA16(cat8(f.w[2 * c], f.w[2 * c + 1]), f.x, acc[c]);
      SBAR(); if (dk >= 0 && !(c & 1)) piece(dk, c >> 1); SBAR();
    }
    sx = MFMA16(ones, f.x, sx); sxx = MFMA16(f.x, f.x, sxx);
#pragma unroll
    for (int c = 0; c < 4; ++c) if (ts == c) csa = MFMA16(cat8(f.w[2 * c], f.w[2 * c + 1]), ones, csa);
    if (ts == 0) csb = MFMA16(cat8(f.w[8], f.w[9]), ones, csb);
  };
  FragT fa, fb;
  vmwait<9>();
  RAWBAR();
  rd(fa, 0);
#define IT(kt, FC, FN, VM, DMA) do { vmwait<VM>(); RAWBAR(); rd(FN, ((kt) + 1) & 3); SBAR(); mm(FC, DMA ? (kt) + 4 : -1); SBAR(); } while (0)
  IT(0, fa, fb, 6, true); IT(1, fb, fa, 6, true); IT(2, fa, fb, 6, true);  IT(3, fb, fa, 6, true); IT(4, fa, fb, 6, true);
  IT(5, fb, fa, 6, true);  IT(6, fa, fb, 6, false); IT(7, fb, fa, 3, false); IT(8, fa, fb, 0, false);
  mm(fb, -1);
#undef IT

  LAS f32x4* red = (LAS f32x4*)((LAS char*)lds + 2 * SLOT) + ts * 5 * 64 + lane;
  LAS f32x2* stp = (LAS f32x2*)((LAS char*)lds + 3 * SLOT);
  LAS f32x4* csp = (LAS f32x4*)((LAS char*)lds + 3 * SLOT + 1024);
  if (kh == 0) { red[3 * 64] = acc[3]; red[4 * 64] = acc[4]; }
  else { red[0] = acc[0]; red[64] = acc[1]; red[128] = acc[2]; }
  if (gq == q4) stp[kh * 64 + xrow] = (f32x2){sx[0], p == 0 ? sxx[0] : p == 1 ? sxx[1] : p == 2 ? sxx[2] : sxx[3]};
  if (r16 == 0) { csp[(kh * 5 + ts) * 4 + gq] = csa; if (ts == 0) csp[(kh * 5 + 4) * 4 + gq] = csb; }
  RAWBAR();
  float mean_b, rden;
  { const f32x2 a = stp[xrow], c = stp[64 + xrow]; ln_scalars(a[0] + c[0], a[1] + c[1], mean_b, rden); }
  rden *= QSCALE;
  u16* qp = (u16*)(ws + WS_Q) + (size_t)(tt * 8 + cg) * 5120 + xrow * 80 + gq * 4;
#pragma unroll
  for (int c = 0; c < 5; ++c)
    if ((c < 3) == (kh == 0)) {
      const f32x4 a = (acc[c] + red[c * 64] - (csp[c * 4 + gq] + csp[(5 + c) * 4 + gq]) * mean_b) * rden;
      *(u32x2*)(qp + c * 16) = (u32x2){cvtpk(a[0], a[1]), cvtpk(a[2], a[3])};
    }
}

__global__ __launch_bounds__(512, 4) void k_pre(const u16* __restrict__ x, const u16* __restrict__ wq, const u16* __restrict__ wk,
                                                 const u16* __restrict__ wv, const u16* __restrict__ w1, const u16* __restrict__ w3,
                                                 const u16* __restrict__ w2, const u16* __restrict__ wo, char* __restrict__ ws) {
  extern __shared__ __attribute__((aligned(16))) char lds[];
  const int b = blockIdx.x;
  if (b < 128) ln_block(x, wk, wv, ws, lds, b);
  else if (b < 384) q_block(x, wq, ws, lds, b - 128);
  else {
    const int lane = threadIdx.x & 63, w = __builtin_amdgcn_readfirstlane(threadIdx.x >> 6), rb = b - 384;
    const unsigned lds0 = (unsigned)(uintptr_t)(LAS char*)lds;
    const int u1 = rb * 8 + w;
    unsigned pf = 0;
    if (u1 < 544) retile_issue(wo, w2, lds0 + w * 6144, u1, lane);
    {
      const int i = (rb >> 3) * 480 + w * 60 + min(lane, 59), row = i / 12, e = i - row * 12;
      touch(((e & 1) ? w3 : w1) + (size_t)row * F + (rb & 3) * 384 + (e >> 1) * 64, pf);
      vmwait<1>();
    }
    if (u1 < 544) retile_finish(ws, (const LAS char*)lds + w * 6144, u1, lane);
    vmwait<0>(); PIN(pf);
  }
}

DI void arrive(char* ws, int i) {
  __hip_atomic_fetch_add((GAS unsigned*)(ws + WS_CNT) + i * 16, 1u, RLX_AGENT);
}
DI GAS unsigned* flagp(char* ws, int i) {
  return (GAS unsigned*)(ws + WS_CNT) + i * 16;
}
DI void wait_count(GAS unsigned* cnt, unsigned want) {
  for (unsigned spins = 0; spins < (1u << 22); ++spins) {
    if ((unsigned)__builtin_amdgcn_readfirstlane(__hip_atomic_load(cnt, RLX_AGENT)) >= want) break;
    __builtin_amdgcn_s_sleep(1);
  }
}

constexpr int KV_A = LDS_BYTES - KV_SLOT, KV_B = 0, KV_Cs = KV_SLOT, O_STG = 2 * KV_SLOT;

DI void attn_block(char* __restrict__ ws, char* lds, int b) {
  const int tid = threadIdx.x, lane = tid & 63, w = __builtin_amdgcn_readfirstlane(tid >> 6);
  const int bi = b >> 3;
  const int tt = (b & 7) * 4 + (bi & 3), hg = bi >> 2;
  const int r = lane & 31, h = lane >> 5;
  const unsigned lds0 = (unsigned)(uintptr_t)(LAS char*)lds;
  const unsigned lane16 = lane * 16;
  __builtin_amdgcn_s_setprio(3);
  {
    const char* qg = ws + WS_Q + (size_t)(tt * 8 + hg) * 10240;
    glds16s(qg + w * 1024, lane16, lds0 + O_STG + w * 1024);
    if (w < 3) glds16s(w < 2 ? qg + 8192 + w * 1024 : ws + WS_KMAX, lane16, lds0 + O_STG + 8192 + w * 1024);
#pragma unroll
    for (int j = 0; j < 3; ++j) {
      const int pc = (7 - w) + 8 * j;
      if (pc < KV_PIECES) glds16s(ws + WS_KV + pc * 1024, lane16, lds0 + KV_A + pc * 1024);
    }
  }
  if (w >= 4) {
    const int lw = w - 4;
    const char* kvg = ws + WS_KV + lw * 1024;
    auto dma = [&](int chunk, int slotoff) {
#pragma unroll
      for (int j = 0; j < 6; ++j)
        if (j < 5 || lw < KV_PIECES - 20) glds16s(kvg + chunk * KV_CHUNK + j * 4096, lane16, lds0 + slotoff + (lw + 4 * j) * 1024);
    };
    dma(1, KV_B);
    vmwait<5>();
    asm volatile("s_barrier" ::: "memory");
    int fre = KV_Cs, nx = KV_A, nn = KV_B;
#pragma unroll 1
    for (int c = 0; c < 8; ++c) {
      asm volatile("s_waitcnt vmcnt(0)\n\ts_barrier" ::: "memory");
      if (c + 2 < 8) dma(c + 2, fre);
      const int t = fre; fre = nx; nx = nn; nn = t;
    }
  } else {
    asm volatile("s_waitcnt vmcnt(0)\n\ts_barrier" ::: "memory");
    __builtin_amdgcn_s_setprio(APRIO);
    u32x4 q1a, q1b; u32x2 q2a, q2b;
    {
      const LAS char* qs = (const LAS char*)lds + O_STG + r * 160 + w * 40;
      const u32x2 a0 = *(const LAS u32x2*)(qs + 16 * h), a1 = *(const LAS u32x2*)(qs + 16 * h + 8);
      const u32x2 b0 = *(const LAS u32x2*)(qs + 5120 + 16 * h), b1 = *(const LAS u32x2*)(qs + 5120 + 16 * h + 8);
      q1a = (u32x4){a0[0], a0[1], a1[0], a1[1]}; q1b = (u32x4){b0[0], b0[1], b1[0], b1[1]};
      q2a = *(const LAS u32x2*)(qs + 32); q2b = *(const LAS u32x2*)(qs + 5120 + 32);
    }
    float km;
    {
      const f32x4 km4 = *(const LAS f32x4*)((LAS char*)lds + O_STG + 10240 + lane * 16);
      int k = __float_as_int(fmaxf(fmaxf(km4[0], km4[1]), fmaxf(km4[2], km4[3])));
      k = max(k, __builtin_amdgcn_update_dpp(0, k, 0x128, 0xf, 0xf, false)); k = max(k, __builtin_amdgcn_update_dpp(0, k, 0x124, 0xf, 0xf, false));
      k = max(k, __builtin_amdgcn_update_dpp(0, k, 0x122, 0xf, 0xf, false)); k = max(k, __builtin_amdgcn_update_dpp(0, k, 0x121, 0xf, 0xf, false));
      km = __int_as_float(max(__builtin_amdgcn_readlane(k, 0), __builtin_amdgcn_readlane(k, 16))) +
           __int_as_float(max(__builtin_amdgcn_readlane(k, 32), __builtin_amdgcn_readlane(k, 48)));
    }
    float qna = dot2(q1a[0], q1a[0], dot2(q1a[1], q1a[1], dot2(q1a[2], q1a[2], dot2(q1a[3], q1a[3], 0.f))));
    float qnb = dot2(q1b[0], q1b[0], dot2(q1b[1], q1b[1], dot2(q1b[2], q1b[2], dot2(q1b[3], q1b[3], 0.f))));
    if (h == 0) { qna = dot2(q2a[0], q2a[0], dot2(q2a[1], q2a[1], qna)); qnb = dot2(q2b[0], q2b[0], dot2(q2b[1], q2b[1], qnb)); }
    { const auto sw = __builtin_amdgcn_permlane32_swap(__float_as_uint(qna), __float_as_uint(qna), false, false); qna = __uint_as_float(sw[0]) + __uint_as_float(sw[1]); }
    { const auto sw = __builtin_amdgcn_permlane32_swap(__float_as_uint(qnb), __float_as_uint(qnb), false, false); qnb = __uint_as_float(sw[0]) + __uint_as_float(sw[1]); }
    const float bda = __builtin_amdgcn_sqrtf(qna * km) * 1.02f + 1e-3f, bdb = __builtin_amdgcn_sqrtf(qnb * km) * 1.02f + 1e-3f;
    const bool fast = __all(bda <= 48.f && bdb <= 48.f);
    if (h == 1) {
      q2a[0] = fast ? (cvtpk(-bda, 0.f) & 0xffffu) : 0u; q2a[1] = 0u;
      q2b[0] = fast ? (cvtpk(-bdb, 0.f) & 0xffffu) : 0u; q2b[1] = 0u;
    }
    const bf16x8 QA1 = __builtin_bit_cast(bf16x8, q1a), QB1 = __builtin_bit_cast(bf16x8, q1b);
    const s16x4 QA2 = __builtin_bit_cast(s16x4, q2a), QB2 = __builtin_bit_cast(s16x4, q2b);
    const int dv = r <= 20 ? r : 21;
    const LAS char* k1b = (const LAS char*)lds + lane * 16;
    const LAS char* k2b = (const LAS char*)lds + (h ? KV_C : KV_K2 + r * 8);
    const LAS char* vb = (const LAS char*)lds + KV_V + (h * VR + dv) * 16;
    f32x16 oa = {}, ob = {};
    const f32x16 zero16 = {};
    if (fast) {
      f32x16 sA, sB;
      bf16x8 k1f, vf0, vf1; s16x4 k2f;
      u32x4 pa, pb;
      float e14, e15;
      k1f = *(const LAS bf16x8*)(k1b + KV_A); k2f = *(const LAS s16x4*)(k2b + KV_A); vf0 = *(const LAS bf16x8*)(vb + KV_A);
      sA = MFMA32(k1f, QA1, zero16); sA = MFMA32K8(k2f, QA2, sA);
      vf1 = (bf16x8){0, 0, 0, 0, 0, 0, 0, 0}; pb = (u32x4){0u, 0u, 0u, 0u}; e14 = 0.f; e15 = 0.f;
      int cur = KV_A, nxt = KV_B, fre = KV_Cs;
#define STEP(C, N, Q1, Q2, OC, OP, V0, L1, L2, L3, L4) do { \
        N = MFMA32(k1f, Q1, zero16); SBAR(); L1; SBAR(); \
        C[0] = EXP2(C[0]); C[1] = EXP2(C[1]); SBAR(); pb[3] = cvtpk(e14, e15); PIN(pb); SBAR(); \
        C[2] = EXP2(C[2]); C[3] = EXP2(C[3]); SBAR(); pa[0] = cvtpk(C[0], C[1]); PIN(pa); SBAR(); \
        OP = MFMA32(vf1, __builtin_bit_cast(bf16x8, pb), OP); SBAR(); L2; SBAR(); \
        C[4] = EXP2(C[4]); C[5] = EXP2(C[5]); SBAR(); pa[1] = cvtpk(C[2], C[3]); PIN(pa); SBAR(); \
        C[6] = EXP2(C[6]); C[7] = EXP2(C[7]); SBAR(); pa[2] = cvtpk(C[4], C[5]); PIN(pa); SBAR(); \
        N = MFMA32K8(k2f, Q2, N); SBAR(); L3; SBAR(); \
        C[8] = EXP2(C[8]); C[9] = EXP2(C[9]); SBAR(); pa[3] = cvtpk(C[6], C[7]); PIN(pa); SBAR(); \
        C[10] = EXP2(C[10]); C[11] = EXP2(C[11]); SBAR(); pb[0] = cvtpk(C[8], C[9]); PIN(pb); SBAR(); \
        OC = MFMA32(V0, __builtin_bit_cast(bf16x8, pa), OC); SBAR(); L4; SBAR(); \
        C[12] = EXP2(C[12]); C[13] = EXP2(C[13]); SBAR(); pb[1] = cvtpk(C[10], C[11]); PIN(pb); SBAR(); \
        e14 = EXP2(C[14]); e15 = EXP2(C[15]); SBAR(); pb[2] = cvtpk(C[12], C[13]); PIN(pb); PIN(e14); PIN(e15); SBAR(); \
      } while (0)
#define KT(i) do { \
        STEP(sA, sB, QB1, QB2, oa, ob, vf0, k1f = *(const LAS bf16x8*)((i) < 7 ? kc1 + ((i) + 1) * KV_TILE : k1b + nxt), \
             vf1 = *(const LAS bf16x8*)(vc + (i) * KV_TILE + 2 * VR * 16), k2f = *(const LAS s16x4*)((i) < 7 ? kc2 + ((i) + 1) * KV_TILE : k2b + nxt), (void)0); \
        STEP(sB, sA, QA1, QA2, ob, oa, vf0, (void)0, (void)0, (void)0, vf0 = *(const LAS bf16x8*)((i) < 7 ? vc + ((i) + 1) * KV_TILE : vb + nxt)); \
      } while (0)
#pragma unroll 1
      for (int c = 0; c < 8; ++c) {
        const LAS char* kc1 = k1b + cur; const LAS char* kc2 = k2b + cur; const LAS char* vc = vb + cur;
        KT(0); KT(1); KT(2);
        asm volatile("s_barrier" ::: "memory");
        KT(3); KT(4); KT(5); KT(6); KT(7);
        const int t = cur; cur = nxt; nxt = fre; fre = t;
      }
      pb[3] = cvtpk(e14, e15);
      ob = MFMA32(vf1, __builtin_bit_cast(bf16x8, pb), ob);
#undef KT
#undef STEP
    } else {
      float ma = -INFINITY, mb = -INFINITY;
      int cur = KV_A, nxt = KV_B, fre = KV_Cs;
#pragma unroll 1
      for (int c = 0; c < 8; ++c) {
#pragma unroll 1
        for (int i = 0; i < 8; ++i) {
          if (i == 3) RAWBAR();
          const int off = cur + i * KV_TILE;
          const bf16x8 k1f = *(const LAS bf16x8*)(k1b + off); const s16x4 k2f = *(const LAS s16x4*)(k2b + off);
          const bf16x8 vf0 = *(const LAS bf16x8*)(vb + off), vf1 = *(const LAS bf16x8*)(vb + off + 2 * VR * 16);
#pragma unroll
          for (int qs = 0; qs < 2; ++qs) {
            f32x16 sc = MFMA32(k1f, qs ? QB1 : QA1, zero16); sc = MFMA32K8(k2f, qs ? QB2 : QA2, sc);
            float& m = qs ? mb : ma; f32x16& o = qs ? ob : oa;
            float mx = sc[0];
#pragma unroll
            for (int j = 1; j < 16; ++j) mx = fmaxf(mx, sc[j]);
            mx = fmaxf(mx, __shfl_xor(mx, 32));
            const float mn = fmaxf(m, mx);
            const float al = EXP2(m - mn);
            m = mn;
#pragma unroll
            for (int j = 0; j < 16; ++j) { o[j] *= al; sc[j] = EXP2(sc[j] - mn); }
            const u32x4 pa = {cvtpk(sc[0], sc[1]), cvtpk(sc[2], sc[3]), cvtpk(sc[4], sc[5]), cvtpk(sc[6], sc[7])};
            const u32x4 pb = {cvtpk(sc[8], sc[9]), cvtpk(sc[10], sc[11]), cvtpk(sc[12], sc[13]), cvtpk(sc[14], sc[15])};
            o = MFMA32(vf0, __builtin_bit_cast(bf16x8, pa), o);
            o = MFMA32(vf1, __builtin_bit_cast(bf16x8, pb), o);
          }
        }
        const int t = cur; cur = nxt; nxt = fre; fre = t;
      }
    }
    PIN(oa); PIN(ob);
#pragma unroll
    for (int qs = 0; qs < 2; ++qs) {
      const f32x16 o = qs ? ob : oa;
      const auto sw = __builtin_amdgcn_permlane32_swap(__float_as_uint(o[8]), __float_as_uint(o[8]), false, false);
      const float il = __builtin_amdgcn_rcpf(__uint_as_float(sw[1]));
      LAS char* os = (LAS char*)lds + O_STG + (qs * 32 + r) * 176 + w * 40 + 8 * h;
      *(LAS u32x2*)(os) = (u32x2){cvtpk(o[0] * il, o[1] * il), cvtpk(o[2] * il, o[3] * il)};
      *(LAS u32x2*)(os + 16) = (u32x2){cvtpk(o[4] * il, o[5] * il), cvtpk(o[6] * il, o[7] * il)};
      if (h == 0) *(LAS u32x2*)(os + 32) = (u32x2){cvtpk(o[8] * il, o[9] * il), cvtpk(o[10] * il, o[11] * il)};
    }
  }
  RAWBAR();
  const __amdgpu_buffer_rsrc_t orr = __builtin_amdgcn_make_buffer_rsrc(ws + WS_O, 0, S * D * 2, 0x00020000);
#pragma unroll
  for (int j = 0; j < 2; ++j) {
    const int ci = tid + 512 * j;
    if (ci < 640) {
      const int c = ci >> 6, cc = hg * 10 + c;
      const u32x4 v = *(const LAS u32x4*)((LAS char*)lds + O_STG + lane * 176 + c * 16);
      __builtin_amdgcn_raw_buffer_store_b128(v, orr, ((tt * 20 + (cc >> 2)) * 4 + (lane >> 4)) * 1024 + ((cc & 3) * 16 + (lane & 15)) * 16, 0, 16);
    }
  }
  vmwait<0>();
  RAWBAR();
  if (tid == 0) arrive(ws, 16 + tt);
}

struct Frag3 { bf16x8 x; s16x4 w[6]; };
struct Frag5 { bf16x8 x; bf16x8 w[5]; };
DI void ffn_block(const u16* __restrict__ x, const u16* __restrict__ w1, const u16* __restrict__ w3, char* __restrict__ ws, u16* __restrict__ out, char* lds, int b) {
  const int tid = threadIdx.x, lane = tid & 63, w = __builtin_amdgcn_readfirstlane(tid >> 6);
  const int bi = b >> 3;
  const unsigned lds0 = (unsigned)(uintptr_t)(LAS char*)lds, lane16 = lane * 16;
  __builtin_amdgcn_s_setprio(GPRIO);
  {
    const int tp = ((b & 7) >> 2) * 8 + (bi & 7), cg = (b & 3) * 4 + (bi >> 3);
    const char* xsb = ws + WS_XN + (size_t)tp * 163840 + w * 1024;
    const int rr = lane / 12, pos = lane - rr * 12;
    const unsigned bs = ((rr & 3) * F + (pos ^ ((w >> 2) << 1)) * 8) * 2;
    const u16* bb = (w < 4 ? w1 : w3) + (size_t)(w & 3) * 8 * F + cg * 96;
    auto piece = [&](int kt, int slot, int j) {
      if (j == 0) glds16s(xsb + kt * 8192, lane16, lds0 + slot * SLOT + w * 1024);
      else glds_lin<48>((const char*)(bb + (size_t)(kt * 32 + (j - 1) * 4) * F), bs, lds0 + slot * SLOT + 8192 + (2 * w + j - 1) * 768);
    };
#pragma unroll
    for (int i = 0; i < 4; ++i) { piece(i, i, 0); piece(i, i, 1); piece(i, i, 2); }
    const int tq = w & 3, hc = w >> 2, r = lane & 31, h = lane >> 5;
    const int xrow = tq * 32 + r;
    const LAS char* xb0 = (const LAS char*)lds + xrow * 64 + ((h ^ ((xrow >> 2) & 3)) * 16);
    const LAS char* xb1 = (const LAS char*)lds + xrow * 64 + (((2 + h) ^ ((xrow >> 2) & 3)) * 16);
    const int gu = (lane >> 4) & 1, q4 = (lane & 15) >> 2, p = lane & 3, pp = ((p & 1) << 1) | (p >> 1);
    const LAS char* wb[3];
#pragma unroll
    for (int c = 0; c < 3; ++c) wb[c] = (const LAS char*)lds + 8192 + gu * 6144 + (8 * h + q4) * 192 + (((hc * 3 + c) ^ gu) * 32) + pp * 8;
    f32x16 acc[3] = {};
    auto rd = [&](Frag3& f, int slot, int s) {
      f.x = *(const LAS bf16x8*)((s ? xb1 : xb0) + slot * SLOT);
#pragma unroll
      for (int c = 0; c < 3; ++c) { f.w[2 * c] = trread(wb[c] + slot * SLOT + s * 3072); f.w[2 * c + 1] = trread(wb[c] + slot * SLOT + s * 3072 + 768); }
    };
    auto mm = [&](const Frag3& f, int s, int dk, int dsl) {
#pragma unroll
      for (int c = 0; c < 3; ++c) {
        acc[c] = MFMA32(cat8(f.w[2 * c], f.w[2 * c + 1]), f.x, acc[c]);
        SBAR();
        if (dk >= 0) { if (s == 1 && c == 0) piece(dk, dsl, 0); if (s == 1 && c == 2) piece(dk, dsl, 1); if (s == 0 && c == 0) piece(dk, dsl, 2); }
        SBAR();
      }
    };
    Frag3 f0, f1;
    vmwait<9>();
    RAWBAR();
    rd(f0, 0, 0);
#define IT(kt, sl, VM, PREV, DMA, NEXT) do { \
      rd(f1, sl, 1); SBAR(); mm(f0, 0, PREV ? (kt) + 3 : -1, ((sl) + 3) & 3); SBAR(); \
      if (NEXT) { vmwait<VM>(); RAWBAR(); rd(f0, ((sl) + 1) & 3, 0); } \
      SBAR(); mm(f1, 1, DMA ? (kt) + 4 : -1, sl); SBAR(); } while (0)
    IT(0, 0, 6, false, true, true); IT(1, 1, 6, true, true, true); IT(2, 2, 6, true, true, true); IT(3, 3, 6, true, true, true);
#pragma unroll 1
    for (int kt = 4; kt < 16; kt += 4) {
      IT(kt, 0, 6, true, true, true); IT(kt + 1, 1, 6, true, true, true); IT(kt + 2, 2, 6, true, true, true); IT(kt + 3, 3, 6, true, true, true);
    }
    IT(16, 0, 6, true, false, true); IT(17, 1, 3, false, false, true); IT(18, 2, 0, false, false, true); IT(19, 3, 0, false, false, false);
#undef IT
    PIN(acc[0]); PIN(acc[1]); PIN(acc[2]);
    __builtin_amdgcn_s_setprio(3);
    u32x4 hv[3];
#pragma unroll
    for (int c = 0; c < 3; ++c) {
      float v[8];
#pragma unroll
      for (int j = 0; j < 8; ++j) {
        const float gt = acc[c][j], up = acc[c][j + 8];
        v[j] = gt * __builtin_amdgcn_rcpf(1.f + EXP2(-1.4426950408889634f * gt)) * up;
      }
      hv[c] = (u32x4){cvtpk(v[0], v[1]), cvtpk(v[2], v[3]), cvtpk(v[4], v[5]), cvtpk(v[6], v[7])};
    }
#pragma unroll
    for (int c = 0; c < 3; ++c) *(LAS u32x4*)((LAS char*)lds + xrow * 208 + hc * 96 + c * 32 + h * 16) = hv[c];
    RAWBAR();
    const __amdgpu_buffer_rsrc_t hr = __builtin_amdgcn_make_buffer_rsrc(ws + WS_H, 0, S * F * 2, 0x00020000);
#pragma unroll
    for (int j = 0; j < 3; ++j) {
      const int ci = tid + 512 * j, row = ci / 12, pos = ci - row * 12;
      const u32x4 v = *(const LAS u32x4*)((LAS char*)lds + row * 208 + pos * 16);
      __builtin_amdgcn_raw_buffer_store_b128(v, hr, ((tp * 128 + row) * F + cg * 96 + pos * 8) * 2, 0, 16);
    }
    vmwait<0>();
    RAWBAR();
    if (tid == 448) arrive(ws, tp);
  }

  const int tt = (b & 7) * 4 + (bi & 3), t0 = tt * 64, cg = bi >> 2, c0 = cg * 80;
  const int ts = w & 3, kh = w >> 2, r16 = lane & 15, gq = lane >> 4, xrow = ts * 16 + r16;
  f32x4 acc[5] = {};
  const char* wog = ws + WS_WO + (size_t)cg * 102400;
  bf16x8 wr[10];
  auto wop = [&](int j) { glds16s(wog + (j >= 5 ? 10240 : 0) + (j * 8 + w) * 1024, lane16, lds0 + (j * 8 + w) * 1024); };
  {
    const int srow = w * 8 + (lane >> 3);
    const unsigned hs = (srow * F + ((lane & 7) ^ ((srow >> 1) & 7)) * 8) * 2;
    const u16* hb = (const u16*)(ws + WS_H) + (size_t)t0 * F;
    const char* wsrc = ws + WS_W2 + (size_t)cg * 245760 + w * 1280;
    auto piece = [&](int kt, int slot, int j) {
      if (j == 0) glds16s_sc1(hb + kt * 64, hs, lds0 + slot * ST64 + w * 1024);
      else glds_lin<40>(wsrc + kt * 10240 + (j - 1) * 640, lane16, lds0 + slot * ST64 + 8192 + w * 1280 + (j - 1) * 640);
    };
#pragma unroll
    for (int i = 0; i < 4; ++i) { piece(i, i, 1); piece(i, i, 2); }
    if (kh == 0) {
#pragma unroll
      for (int c = 0; c < 5; ++c) {
        const u32x2 xv = *(const u32x2*)(x + (size_t)(t0 + xrow) * D + c0 + c * 16 + gq * 4);
        acc[c] = (f32x4){bflo(xv[0]), bfhi(xv[0]), bflo(xv[1]), bfhi(xv[1])};
        PIN(acc[c]);
      }
    }
    if (w == 0) wait_count(flagp(ws, tt >> 1), 16u);
    RAWBAR();
    __builtin_amdgcn_s_setprio(GPRIO);

#pragma unroll
    for (int i = 0; i < 4; ++i) piece(i, i, 0);
    const LAS char* xb = (const LAS char*)lds + xrow * 128 + (((kh * 4 + gq) ^ ((xrow >> 1) & 7)) * 16);
    const LAS char* wb = (const LAS char*)lds + 8192 + kh * 5120 + lane * 16;
    auto rd = [&](Frag5& f, int slot) {
      f.x = *(const LAS bf16x8*)(xb + slot * ST64);
#pragma unroll
      for (int c = 0; c < 5; ++c) f.w[c] = *(const LAS bf16x8*)(wb + slot * ST64 + c * 1024);
    };
    auto mm = [&](const Frag5& f, int dk, int dsl) {
#pragma unroll
      for (int c = 0; c < 5; ++c) { acc[c] = MFMA16(f.w[c], f.x, acc[c]); SBAR(); if (dk >= 0 && !(c & 1)) piece(dk, dsl, c >> 1); SBAR(); }
    };
    Frag5 fa, fb;
    vmwait<3>();
    RAWBAR();
    rd(fa, 0);
#define IT(kt, sl, FC, FN, VM, DMA) do { vmwait<VM>(); RAWBAR(); rd(FN, ((sl) + 1) & 3); SBAR(); mm(FC, DMA ? (kt) + 4 : -1, sl); SBAR(); } while (0)
    IT(0, 0, fa, fb, 2, true); IT(1, 1, fb, fa, 4, true); IT(2, 2, fa, fb, 6, true); IT(3, 3, fb, fa, 6, true);
#pragma unroll 1
    for (int kt = 4; kt < 20; kt += 4) {
      IT(kt, 0, fa, fb, 6, true); IT(kt + 1, 1, fb, fa, 6, true); IT(kt + 2, 2, fa, fb, 6, true); IT(kt + 3, 3, fb, fa, 6, true);
    }
    IT(20, 0, fa, fb, 6, false); wop(0); wop(1); wop(9);
    {
      const char* wrp = wog + kh * 51200 + 40960;
#pragma unroll
      for (int j = 0; j < 10; ++j) asm volatile("global_load_dwordx4 %0, %1, %2" : "=v"(wr[j]) : "v"(lane16), "s"(wrp + j * 1024) : "memory");
    }
    IT(21, 1, fb, fa, 16, false); wop(2); wop(3);
    IT(22, 2, fa, fb, 15, false); wop(4); wop(5);
    mm(fb, -1, 0);
#undef IT
  }
  PIN(acc[0]);
  RAWBAR();
  wop(6); wop(7); wop(8);
  asm volatile("s_waitcnt vmcnt(0)" : "+v"(wr[0]), "+v"(wr[1]), "+v"(wr[2]), "+v"(wr[3]), "+v"(wr[4]), "+v"(wr[5]), "+v"(wr[6]), "+v"(wr[7]), "+v"(wr[8]), "+v"(wr[9]) :: "memory");
  vmwait<0>();
  __builtin_amdgcn_s_setprio(3);
  const LAS char* wbt = (const LAS char*)lds + kh * 40960 + lane * 16;
  if (w == 0) wait_count(flagp(ws, 16 + tt), 8u);
  RAWBAR();

  {
    u32x4 of[10];
    const char* op = ws + WS_O + ((tt * 20 + kh * 10) * 4 + ts) * 1024;
#pragma unroll
    for (int s = 0; s < 10; ++s) asm volatile("global_load_dwordx4 %0, %1, %2 sc1" : "=v"(of[(s + 8) % 10]) : "v"(lane16), "s"(op + ((s + 8) % 10) * 4096) : "memory");
#define OSTEP(s, N, WC, WN) do { \
      if ((s) + 1 < 8) { _Pragma("unroll") for (int c = 0; c < 5; ++c) WN[c] = *(const LAS bf16x8*)(wbt + ((s) + 1) * 5120 + c * 1024); } \
      SBAR(); asm volatile("s_waitcnt vmcnt(" #N ")" : "+v"(of[s]) :: "memory"); SBAR(); \
      _Pragma("unroll") for (int c = 0; c < 5; ++c) acc[c] = MFMA16(WC[c], __builtin_bit_cast(bf16x8, of[s]), acc[c]); SBAR(); } while (0)
    asm volatile("s_waitcnt vmcnt(9)" : "+v"(of[8]) :: "memory");
#pragma unroll
    for (int c = 0; c < 5; ++c) acc[c] = MFMA16(wr[c], __builtin_bit_cast(bf16x8, of[8]), acc[c]);
    SBAR();
    bf16x8 w0[5];
#pragma unroll
    for (int c = 0; c < 5; ++c) w0[c] = *(const LAS bf16x8*)(wbt + c * 1024);
    SBAR(); asm volatile("s_waitcnt vmcnt(8)" : "+v"(of[9]) :: "memory"); SBAR();
#pragma unroll
    for (int c = 0; c < 5; ++c) acc[c] = MFMA16(wr[5 + c], __builtin_bit_cast(bf16x8, of[9]), acc[c]);
    SBAR();
    bf16x8 wn[5];
    OSTEP(0, 7, w0, wn); OSTEP(1, 6, wn, w0); OSTEP(2, 5, w0, wn); OSTEP(3, 4, wn, w0);
    OSTEP(4, 3, w0, wn); OSTEP(5, 2, wn, w0); OSTEP(6, 1, w0, wn); OSTEP(7, 0, wn, w0);
#undef OSTEP
  }
  RAWBAR();
#pragma unroll
  for (int c = 0; c < 5; ++c) *(LAS f32x4*)((LAS char*)lds + kh * 64 * 336 + xrow * 336 + c * 64 + gq * 16) = acc[c];
  RAWBAR();
#pragma unroll
  for (int j = 0; j < 2; ++j) {
    const int ci = tid + 512 * j;
    if (ci < 640) {
      const int row = ci / 10, c = ci - row * 10;
      const LAS f32x4* a0 = (const LAS f32x4*)((LAS char*)lds + row * 336 + c * 32);
      const LAS f32x4* a1 = (const LAS f32x4*)((LAS char*)lds + 64 * 336 + row * 336 + c * 32);
      const f32x4 lo = a0[0] + a1[0], hi = a0[1] + a1[1];
      const u32x4 res = {cvtpk(lo[0], lo[1]), cvtpk(lo[2], lo[3]), cvtpk(hi[0], hi[1]), cvtpk(hi[2], hi[3])};
      *(u32x4*)(out + (size_t)(t0 + row) * D + c0 + c * 8) = res;
    }
  }
}

__global__ __launch_bounds__(512, 4) void k_mid(const u16* __restrict__ x, const u16* __restrict__ w1, const u16* __restrict__ w3, char* __restrict__ ws, u16* __restrict__ out) {
  extern __shared__ __attribute__((aligned(16))) char lds[];
  if (blockIdx.x < 256) attn_block(ws, lds, blockIdx.x);
  else ffn_block(x, w1, w3, ws, out, lds, blockIdx.x - 256);
}

extern "C" void kernel_launch(void* const* d_in, const int* in_sizes, int n_in, void* d_out, int out_size, void* d_ws, size_t ws_size,
                              hipStream_t stream) {
  const u16* x = (const u16*)d_in[0];
  const u16* wq = (const u16*)d_in[1];
  const u16* wk = (const u16*)d_in[2];
  const u16* wv = (const u16*)d_in[3];
  const u16* wo = (const u16*)d_in[4];
  const u16* w1 = (const u16*)d_in[6];
  const u16* w3 = (const u16*)d_in[7];
  const u16* w2 = (const u16*)d_in[8];
  char* ws = (char*)d_ws;
  (void)hipFuncSetAttribute((const void*)k_mid, hipFuncAttributeMaxDynamicSharedMemorySize, LDS_BYTES);
  (void)hipFuncSetAttribute((const void*)k_pre, hipFuncAttributeMaxDynamicSharedMemorySize, LDS_BYTES);
  k_pre<<<512, 512, LDS_BYTES, stream>>>(x, wq, wk, wv, w1, w3, w2, wo, ws);
  k_mid<<<512, 512, LDS_BYTES, stream>>>(x, w1, w3, ws, (u16*)d_out);
}
```

```cpp
#include <hip/hip_runtime.h>
#include <hip/hip_bf16.h>
#include <stdint.h>

typedef short bf16x8 __attribute__((ext_vector_type(8)));
typedef short s16x4 __attribute__((ext_vector_type(4)));
typedef float f32x2 __attribute__((ext_vector_type(2)));
typedef float f32x4 __attribute__((ext_vector_type(4)));
typedef float f32x16 __attribute__((ext_vector_type(16)));
typedef unsigned u32x4 __attribute__((ext_vector_type(4)));
typedef unsigned u32x2 __attribute__((ext_vector_type(2)));
typedef __bf16 bf16x2_t __attribute__((ext_vector_type(2)));
typedef unsigned short u16;
#define LAS __attribute__((address_space(3)))
#define GAS __attribute__((address_space(1)))
#define RLX_AGENT __ATOMIC_RELAXED, __HIP_MEMORY_SCOPE_AGENT
#define DI __device__ __forceinline__
#define SBAR() __builtin_amdgcn_sched_barrier(0)
#define PIN(x) asm volatile("" : "+v"(x))

#define GPRIO 1
#define LADDER 1
#define WT 0
#define QPRIO 0
#define P2PRIO GPRIO
#define G1LATE 99
#define KSL 1
#define GSL 16
#define BSL 12
#define APRIO 2
constexpr int S = 2048, D = 640, H = 32, DH = 20, F = 1536;
constexpr int VR = 22;
constexpr int KV_K2 = 1024, KV_C = 1280, KV_V = 1296, KV_TILE = KV_V + 4 * VR * 16;
constexpr int KV_CHUNK = 8 * KV_TILE;
constexpr int KV_PIECES = (KV_CHUNK + 1023) / 1024, KV_SLOT = KV_PIECES * 1024;
constexpr size_t WS_CNT = 0;
constexpr size_t WS_KVF = 12288;
constexpr size_t WS_KVC = 16384;
constexpr size_t WS_ACK = 17408;
constexpr size_t WS_ZEND = 20480;
constexpr size_t WS_BEACON = 20480;
constexpr size_t WS_KMAX = 24576;
constexpr size_t WS_STAT = 28672;
constexpr size_t WS_KV = 49152;
constexpr size_t WS_XN = WS_KV + 192 * 1024;
constexpr size_t WS_O = WS_XN + (size_t)S * D * 2;
constexpr size_t WS_H = WS_O + (size_t)S * D * 2;
constexpr size_t WS_Q = WS_H + (size_t)S * F * 2;
constexpr size_t WS_WO = WS_Q + (size_t)S * D * 2;
constexpr size_t WS_W13 = WS_WO + (size_t)D * D * 2;
constexpr size_t WS_W2 = WS_W13 + (size_t)2 * D * F * 2;
constexpr float QSCALE = 0.22360679774997896f * 1.4426950408889634f;
constexpr int LDS_BYTES = 81920, SLOT = 20480;

DI unsigned cvtpk(float a, float b) { f32x2 v = {a, b}; bf16x2_t r = __builtin_convertvector(v, bf16x2_t); return __builtin_bit_cast(unsigned, r); }
DI float bflo(unsigned u) { return __uint_as_float(u << 16); }
DI float bfhi(unsigned u) { return __uint_as_float(u & 0xffff0000u); }
DI float rbf(float a) { return bflo(cvtpk(a, a)); }
DI float dot2(unsigned a, unsigned b, float c) { return __builtin_amdgcn_fdot2_f32_bf16(__builtin_bit_cast(bf16x2_t, a), __builtin_bit_cast(bf16x2_t, b), c, false); }
DI s16x4 trread(const LAS char* p) { return __builtin_bit_cast(s16x4, __builtin_amdgcn_ds_read_tr16_b64_v4i16((LAS s16x4*)p)); }
DI bf16x8 cat8(s16x4 a, s16x4 b) { return __builtin_shufflevector(a, b, 0, 1, 2, 3, 4, 5, 6, 7); }
DI void glds16s(const void* sbase, unsigned voff, unsigned lds_base) {
  unsigned sv;
  asm volatile("s_mov_b32 %0, m0\n\ts_mov_b32 m0, %3\n\ts_nop 0\n\tglobal_load_lds_dwordx4 %1, %2\n\ts_mov_b32 m0, %0" : "=&s"(sv) : "v"(voff), "s"(sbase), "s"(lds_base) : "memory");
}
DI void glds16s_sc1(const void* sbase, unsigned voff, unsigned lds_base) {
  unsigned sv;
  asm volatile("s_mov_b32 %0, m0\n\ts_mov_b32 m0, %3\n\ts_nop 0\n\tglobal_load_lds_dwordx4 %1, %2 sc1\n\ts_mov_b32 m0, %0" : "=&s"(sv) : "v"(voff), "s"(sbase), "s"(lds_base) : "memory");
}
#define ST_DECL
#define ST()
#define ST_OUT(a, b)
#define MFMA32(a, b, c) __builtin_amdgcn_mfma_f32_32x32x16_bf16(a, b, c, 0, 0, 0)
#define MFMA32K8(a, b, c) __builtin_amdgcn_mfma_f32_32x32x8bf16_1k(a, b, c, 0, 0, 0)
#define MFMA16(a, b, c) __builtin_amdgcn_mfma_f32_16x16x32_bf16(a, b, c, 0, 0, 0)
#define EXP2(v) __builtin_amdgcn_exp2f(v)
#define RAWBAR() asm volatile("s_waitcnt lgkmcnt(0)\n\ts_barrier" ::: "memory")
DI void touch(const void* p, unsigned& d) { asm volatile("global_load_dword %0, %1, off" : "+v"(d) : "v"(p) : "memory"); }
DI void st16wt(void* p, u32x4 v) {
  *(u32x4*)p = v;
}
DI void st8wt(void* p, u32x2 v) { asm volatile("global_store_dwordx2 %0, %1, off sc1" :: "v"(p), "v"(v) : "memory"); }
DI void st4wt(void* p, unsigned v) { asm volatile("global_store_dword %0, %1, off sc1" :: "v"(p), "v"(v) : "memory"); }
DI void st16s(void* p, u32x4 v) { asm volatile("global_store_dwordx4 %0, %1, off sc1\n\ts_nop 1" :: "v"(p), "v"(v) : "memory"); }
template <int N> DI void vmwait() { asm volatile("s_waitcnt vmcnt(%0)" :: "n"(N) : "memory"); }
DI int wslot(int k) { return k ^ ((k >> 1) & 4); }
DI void ln_scalars(float sum, float sq, float& mean_b, float& rden) {
  const float mean = sum * (1.f / D);
  const float var = fmaxf(sq * (1.f / D) - mean * mean, 0.f);
  mean_b = rbf(mean);
  rden = __builtin_amdgcn_rcpf(rbf(__builtin_amdgcn_sqrtf(rbf(rbf(var) + 1e-5f))));
}

template <int NL> DI void glds_lin_sc1(const char* src, unsigned lane16, unsigned dst) {
  unsigned sv;
  asm volatile("s_mov_b32 %0, m0\n\ts_mov_b32 m0, %3\n\ts_mov_b32 exec_hi, %4\n\tglobal_load_lds_dwordx4 %1, %2 sc1\n\ts_mov_b32 exec_hi, -1\n\ts_mov_b32 m0, %0"
               : "=&s"(sv) : "v"(lane16), "s"(src), "s"(dst), "n"((1u << (NL - 32)) - 1u) : "memory");
}
template <int NL> DI void glds_lin(const char* src, unsigned lane16, unsigned dst) {
  unsigned sv;
  asm volatile("s_mov_b32 %0, m0\n\ts_mov_b32 m0, %3\n\ts_mov_b32 exec_hi, %4\n\tglobal_load_lds_dwordx4 %1, %2\n\ts_mov_b32 exec_hi, -1\n\ts_mov_b32 m0, %0"
               : "=&s"(sv) : "v"(lane16), "s"(src), "s"(dst), "n"((1u << (NL - 32)) - 1u) : "memory");
}

constexpr int ST64 = 18432;

DI void retile_issue(const u16* __restrict__ wo, const u16* __restrict__ w1, const u16* __restrict__ w3, const u16* __restrict__ w2,
                     unsigned ldsu, int u, int lane) {
#pragma unroll
  for (int j = 0; j < 3; ++j) {
    const int ci = j * 64 + lane, rr = ci / 12, pos = ci - rr * 12;
    if (u < 640) {
      const int cg = u / 40, ks = u - cg * 40;
      glds16s(w1 + (size_t)ks * 16 * F + cg * 96, (rr * F + pos * 8) * 2, ldsu + j * 1024);
      glds16s(w3 + (size_t)ks * 16 * F + cg * 96, (rr * F + pos * 8) * 2, ldsu + 3072 + j * 1024);
    } else {
      const int i = u < 800 ? u - 640 : u - 800, nk = u < 800 ? 20 : 48, cg = i / nk, ks = i - cg * nk, c0 = cg * 80 - (cg & 1) * 16;
      const u16* src = (u < 800 ? wo : w2) + (size_t)ks * 32 * D + c0;
      const unsigned vo = (rr * D + (c0 + pos * 8 < D ? pos : 0) * 8) * 2;
      glds16s(src, vo, ldsu + j * 1024);
      glds16s(src + 16 * D, vo, ldsu + 3072 + j * 1024);
    }
  }
}
DI void st16sc1(void* p, u32x4 v) { asm volatile("global_store_dwordx4 %0, %1, off sc1\n\ts_nop 1" :: "v"(p), "v"(v) : "memory"); }
DI void retile_finish(char* __restrict__ ws, const LAS char* im, int u, int lane) {
  const int h = lane >> 5, q4 = (lane & 15) >> 2, p = lane & 3, blk = (lane >> 4) & 1;
  if (u < 640) {
    const LAS char* a = im + blk * 3072 + (8 * h + q4) * 192 + (((p & 1) << 1) | (p >> 1)) * 8;
    char* dst = ws + WS_W13 + (size_t)u * 6144 + lane * 16;
#pragma unroll
    for (int c = 0; c < 6; ++c) st16wt(dst + c * 1024, __builtin_bit_cast(u32x4, cat8(trread(a + c * 32), trread(a + c * 32 + 768))));
  } else {
    const int i = u < 800 ? u - 640 : u - 800, cg = i / (u < 800 ? 20 : 48);
    const LAS char* a = im + ((lane >> 4) * 8 + q4) * 192 + (cg & 1) * 32 + p * 8;
    char* dst = ws + (u < 800 ? WS_WO : WS_W2) + (size_t)i * 5120 + lane * 16;
#pragma unroll
    for (int c = 0; c < 5; ++c) st16sc1(dst + c * 1024, __builtin_bit_cast(u32x4, cat8(trread(a + c * 32), trread(a + c * 32 + 768))));
  }
}

DI GAS unsigned* beaconp(char* ws) { return (GAS unsigned*)(ws + WS_BEACON) + ((blockIdx.x >> 3) & 31) * 32; }
DI void beacon_sample(char* ws, unsigned& b0) { GAS unsigned* p = beaconp(ws); asm volatile("global_load_dword %0, %1, off sc1" : "=v"(b0) : "v"(p) : "memory"); }
DI void clean_wait(char* ws, unsigned b0) {
  GAS unsigned* p = beaconp(ws);
  for (unsigned spins = 0; spins < (1u << 22); ++spins) {
    if (__all(__hip_atomic_load(p, RLX_AGENT) != b0)) break;
    __builtin_amdgcn_s_sleep(4);
  }
  if ((threadIdx.x & 63) == 0) __hip_atomic_fetch_add((GAS unsigned*)(ws + WS_ACK) + (blockIdx.x & 31) * 16, 1u, RLX_AGENT);
}

constexpr int LN_ST = 25600, LN_RED = LN_ST + 512, LN_HALF = LN_RED + 8192;
DI void ln_block(const u16* __restrict__ x, const u16* __restrict__ wk, const u16* __restrict__ wv, char* __restrict__ ws, char* ldsb, int b) {
  const int tid = threadIdx.x, lane = tid & 63, w8 = __builtin_amdgcn_readfirstlane(tid >> 6), w = w8 & 3, kv = w8 >> 2;
  const int t16 = (b & 7) * 16 + (b >> 3);
  const int r16 = lane & 15, gq = lane >> 4;
  LAS char* lds = (LAS char*)ldsb + kv * LN_HALF;
  const unsigned lds0 = (unsigned)(uintptr_t)lds;
  const int token = t16 * 16 + r16;
  unsigned b0;
  beacon_sample(ws, b0);
  u32x4 xf[5];
  const u16* xp = x + (size_t)token * D + w * 160 + gq * 8;
#pragma unroll
  for (int s = 0; s < 5; ++s) asm volatile("global_load_dwordx4 %0, %1, off offset:%2" : "=v"(xf[s]) : "v"(xp), "i"(s * 64) : "memory");
  const u16* wsrc = kv ? wv : wk;
#pragma unroll
  for (int j = 0; j < 7; ++j)
    if (j < 6 || w == 0) glds16s(wsrc + (w + 4 * j) * 512, lane * 16, lds0 + (w + 4 * j) * 1024);
  asm volatile("s_waitcnt vmcnt(0)" : "+v"(xf[0]), "+v"(xf[1]), "+v"(xf[2]), "+v"(xf[3]), "+v"(xf[4]), "+v"(b0) :: "memory");
  float sum = 0.f, sq = 0.f, sum2 = 0.f, sq2 = 0.f;
#pragma unroll
  for (int s = 0; s < 5; ++s)
#pragma unroll
    for (int j = 0; j < 4; j += 2) {
      sum = dot2(xf[s][j], 0x3F803F80u, sum); sq = dot2(xf[s][j], xf[s][j], sq);
      sum2 = dot2(xf[s][j + 1], 0x3F803F80u, sum2); sq2 = dot2(xf[s][j + 1], xf[s][j + 1], sq2);
    }
  sum += sum2; sq += sq2;
  sum += __shfl_xor(sum, 16); sq += __shfl_xor(sq, 16);
  sum += __shfl_xor(sum, 32); sq += __shfl_xor(sq, 32);
  LAS f32x2* stp = (LAS f32x2*)(lds + LN_ST);
  if (gq == 0) stp[w * 16 + r16] = (f32x2){sum, sq};
  RAWBAR();
  { const f32x2 a = stp[r16], c = stp[16 + r16], d = stp[32 + r16], e = stp[48 + r16]; sum = (a[0] + c[0]) + (d[0] + e[0]); sq = (a[1] + c[1]) + (d[1] + e[1]); }
  float mean_b, rden;
  ln_scalars(sum, sq, mean_b, rden);
#pragma unroll
  for (int s = 0; s < 5; ++s)
#pragma unroll
    for (int j = 0; j < 4; ++j) {
      const unsigned d = cvtpk(bflo(xf[s][j]) - mean_b, bfhi(xf[s][j]) - mean_b);
      xf[s][j] = cvtpk(bflo(d) * rden, bfhi(d) * rden);
    }
  if ((w >> 1) == kv) {
    const int row = token & 127;
    char* xn = ws + WS_XN + ((size_t)((token >> 7) * 20 + w * 5) * 128 + row) * 64 + ((gq ^ ((row >> 2) & 3)) * 16);
#pragma unroll
    for (int s = 0; s < 5; ++s) st16s(xn + s * 8192, xf[s]);
  }
  if (w8 == 7 && gq == 0) st8wt(ws + WS_STAT + token * 8, (u32x2){__float_as_uint(mean_b), __float_as_uint(rden)});
  const int q4 = (lane & 15) >> 2, p = lane & 3;
  const LAS char* e0 = lds + (w * 160 + gq * 8 + q4) * 40 + p * 8;
  const LAS char* e1 = lds + (w * 160 + gq * 8 + q4) * 40 + 32;
  f32x4 acc[2] = {};
  if (kv == 0) {
#pragma unroll
    for (int s = 0; s < 5; ++s) {
      acc[0] = MFMA16(cat8(trread(e0 + s * 1280), trread(e0 + s * 1280 + 160)), __builtin_bit_cast(bf16x8, xf[s]), acc[0]);
      acc[1] = MFMA16(cat8(trread(e1 + s * 1280), trread(e1 + s * 1280 + 160)), __builtin_bit_cast(bf16x8, xf[s]), acc[1]);
    }
  } else {
#pragma unroll
    for (int s = 0; s < 5; ++s) {
      acc[0] = MFMA16(__builtin_bit_cast(bf16x8, xf[s]), cat8(trread(e0 + s * 1280), trread(e0 + s * 1280 + 160)), acc[0]);
      acc[1] = MFMA16(__builtin_bit_cast(bf16x8, xf[s]), cat8(trread(e1 + s * 1280), trread(e1 + s * 1280 + 160)), acc[1]);
    }
  }
  LAS f32x4* red = (LAS f32x4*)(lds + LN_RED) + lane;
  red[(w * 2) * 64] = acc[0]; red[(w * 2 + 1) * 64] = acc[1];
  RAWBAR();
  if (w8 == 3) clean_wait(ws, b0);
  if (w < 2) {
    const f32x4 a = (red[w * 64] + red[(2 + w) * 64]) + (red[(4 + w) * 64] + red[(6 + w) * 64]);
    const u32x2 v = {cvtpk(a[0], a[1]), cvtpk(a[2], a[3])};
    char* kvp = ws + WS_KV + (t16 >> 1) * KV_TILE;
    if (kv == 0) {
      const int r = token & 31;
      float n2 = dot2(v[0], v[0], dot2(v[1], v[1], 0.f));
      if (w == 0) {
        st8wt(kvp + (gq >> 1) * 512 + r * 16 + (gq & 1) * 8, v);
      } else {
        if (gq == 0) st8wt(kvp + KV_K2 + r * 8, v);
        else n2 = 0.f;
        if (gq == 1 && r == 0) st8wt(kvp + KV_C, (u32x2){0x00003F80u, 0u});
      }
      n2 += __shfl_xor(n2, 16); n2 += __shfl_xor(n2, 32);
#pragma unroll
      for (int o = 1; o < 16; o <<= 1) n2 = fmaxf(n2, __shfl_xor(n2, o));
      if (lane == 0) st4wt(ws + WS_KMAX + (w * 128 + t16) * 4, __float_as_uint(n2));
    } else {
      char* vp = kvp + KV_V + (((t16 & 1) * 2 + (gq & 1)) * VR) * 16 + (gq >> 1) * 8;
      if (w == 0) st8wt(vp + r16 * 16, v);
      else if (r16 < 6) {
        const u32x2 one = {0x3F803F80u, 0x3F803F80u}, zer = {0u, 0u};
        st8wt(vp + (16 + r16) * 16, r16 < 4 ? v : (r16 == 4 ? one : zer));
      }
    }
  }
  vmwait<0>();
  RAWBAR();
  if (w8 == 0) {
    unsigned old = 0;
    if (lane == 0) old = __hip_atomic_fetch_add((GAS unsigned*)(ws + WS_KVC) + (t16 >> 4) * 16, 1u, RLX_AGENT);
    if (__builtin_amdgcn_readfirstlane(old) == 15u && lane < 32) __hip_atomic_store((GAS unsigned*)(ws + WS_KVF) + lane * 32 + (t16 >> 4), 1u, RLX_AGENT);
  }
}

DI void arrive(char* ws, int i, unsigned want) {
  __hip_atomic_fetch_add((GAS unsigned*)(ws + WS_CNT) + i * 16, 1u, RLX_AGENT);
}
DI void arrive_relay(char* ws, int i, unsigned want) {
  if (__hip_atomic_fetch_add((GAS unsigned*)(ws + WS_CNT) + i * 16, 1u, RLX_AGENT) == want - 1u)
    __hip_atomic_store((GAS unsigned*)(ws + WS_CNT + 4096) + i * 32, 1u, RLX_AGENT);
}
DI GAS unsigned* relayp(char* ws, int i) { return (GAS unsigned*)(ws + WS_CNT + 4096) + i * 32; }
DI GAS unsigned* flagp(char* ws, int i) {
  return (GAS unsigned*)(ws + WS_CNT) + i * 16;
}
template <int SL = KSL>
DI void wait_kv(char* ws, int first, int n) {
  GAS unsigned* p = (GAS unsigned*)(ws + WS_KVF) + ((blockIdx.x >> 3) & 31) * 32 + first + min((int)(threadIdx.x & 63), n - 1);
  for (unsigned spins = 0; spins < (1u << 22); ++spins) {
    if (__all(__hip_atomic_load(p, RLX_AGENT) != 0u)) break;
    __builtin_amdgcn_s_sleep(SL);
  }
}
DI void wait_count(GAS unsigned* cnt, unsigned want) {
  for (unsigned spins = 0; spins < (1u << 22); ++spins) {
    if ((unsigned)__builtin_amdgcn_readfirstlane(__hip_atomic_load(cnt, RLX_AGENT)) >= want) break;
    __builtin_amdgcn_s_sleep(1);
  }
}

constexpr int KV_A = LDS_BYTES - KV_SLOT, KV_B = 0, KV_Cs = KV_SLOT, O_STG = 2 * KV_SLOT;

struct FragQ { bf16x8 x[4]; s16x4 w[8]; };
DI void attn_block(const u16* __restrict__ x, const u16* __restrict__ wq, const u16* __restrict__ w2, const u16* __restrict__ wo, char* __restrict__ ws, char* lds, int b) {
  const int tid = threadIdx.x, lane = tid & 63, w = __builtin_amdgcn_readfirstlane(tid >> 6);
  const int bi = b >> 3;
  const int tt = (b & 7) * 4 + (bi & 3), hg = bi >> 2;
  const int r = lane & 31, h = lane >> 5;
  const unsigned lds0 = (unsigned)(uintptr_t)(LAS char*)lds;
  const unsigned lane16 = lane * 16;
  ST_DECL ST();
  const int th = w >> 2, hd = w & 3, t0 = tt * 64;
  __builtin_amdgcn_s_setprio(APRIO);
  unsigned b0;
  beacon_sample(ws, b0);
  f32x16 qa = {};
  float cs0 = 0.f, cs1 = 0.f;
  {
    const int odd = hg & 1, c0 = hg * 80 - odd * 16;
    const int srow = w * 8 + (lane >> 3);
    const unsigned as = (srow * D + ((lane & 7) ^ ((srow >> 1) & 7)) * 8) * 2;
    const u16* ab = x + (size_t)t0 * D;
    const int rr = lane / 12, ch = lane - rr * 12;
    const unsigned bs = ((rr & 3) * D + (c0 + ch * 8 < D ? ch : 0) * 8) * 2;
    const u16* bb = wq + (size_t)w * 8 * D + c0;
    auto piece = [&](int kt, int j) {
      const int slot = kt & 3;
      if (j == 0) glds16s(ab + kt * 64, as, lds0 + slot * SLOT + w * 1024);
      else glds_lin<48>((const char*)(bb + (size_t)(kt * 64 + (j - 1) * 4) * D), bs, lds0 + slot * SLOT + 8192 + (2 * w + j - 1) * 768);
    };
#pragma unroll
    for (int i = 0; i < 4; ++i) { piece(i, 0); piece(i, 1); piece(i, 2); }
    const int xrow = th * 32 + r;
    const LAS char* xb[4];
#pragma unroll
    for (int s = 0; s < 4; ++s) xb[s] = (const LAS char*)lds + xrow * 128 + (((2 * s + h) ^ ((xrow >> 1) & 7)) * 16);
    const int gI = ((lane >> 4) & 1) * 4 + (lane & 3), q4 = (lane & 15) >> 2;
    const int dcol = gI < 4 ? ((gI & 1) * 8 + (gI >> 1) * 4) : 16;
    const LAS char* wb = (const LAS char*)lds + 8192 + (8 * h + q4) * 192 + odd * 32 + hd * 40 + dcol * 2;
    auto rd = [&](FragQ& f, int slot) {
#pragma unroll
      for (int s = 0; s < 4; ++s) {
        f.x[s] = *(const LAS bf16x8*)(xb[s] + slot * SLOT);
        f.w[2 * s] = trread(wb + slot * SLOT + s * 3072); f.w[2 * s + 1] = trread(wb + slot * SLOT + s * 3072 + 768);
      }
    };
    auto mm = [&](const FragQ& f, int dk) {
#pragma unroll
      for (int s = 0; s < 4; ++s) {
        qa = MFMA32(cat8(f.w[2 * s], f.w[2 * s + 1]), f.x[s], qa);
        SBAR(); if (dk >= 0 && s < 3) piece(dk, s);
        const u32x2 a = __builtin_bit_cast(u32x2, f.w[2 * s]), c = __builtin_bit_cast(u32x2, f.w[2 * s + 1]);
        if (w < 4) { cs0 = dot2(a[0], 0x3F803F80u, cs0); cs1 = dot2(a[1], 0x3F803F80u, cs1); cs0 = dot2(c[0], 0x3F803F80u, cs0); cs1 = dot2(c[1], 0x3F803F80u, cs1); }
        PIN(cs0); PIN(cs1); SBAR();
      }
    };
    FragQ fa, fb;
    ST();
    vmwait<9>(); PIN(b0);
    RAWBAR();
    rd(fa, 0);
#define IT(kt, FC, FN, VM, DMA) do { vmwait<VM>(); RAWBAR(); rd(FN, ((kt) + 1) & 3); SBAR(); mm(FC, DMA ? (kt) + 4 : -1); SBAR(); } while (0)
    IT(0, fa, fb, 6, true); IT(1, fb, fa, 6, true); IT(2, fa, fb, 6, true); IT(3, fb, fa, 6, true);
    IT(4, fa, fb, 6, true); IT(5, fb, fa, 6, true);
    if (w == 7) clean_wait(ws, b0);
    IT(6, fa, fb, 6, false);
    IT(7, fb, fa, 3, false); IT(8, fa, fb, 0, false);
    mm(fb, -1);
#undef IT
  }
  LAS float* csl = (LAS float*)((LAS char*)lds + 53248 + hd * 128);
  cs0 += cs1;
  { const auto sw = __builtin_amdgcn_permlane32_swap(__float_as_uint(cs0), __float_as_uint(cs0), false, false); cs0 = __uint_as_float(sw[0]) + __uint_as_float(sw[1]); }
  LAS f32x4* qx = (LAS f32x4*)((LAS char*)lds + 2 * SLOT) + hd * 192 + lane;
  if (w < 4) { if (h == 0) csl[r] = cs0; }
  else { qx[0] = (f32x4){qa[0], qa[1], qa[2], qa[3]}; qx[64] = (f32x4){qa[4], qa[5], qa[6], qa[7]}; qx[128] = (f32x4){qa[8], qa[9], qa[10], qa[11]}; }
  PIN(qa); ST();
  if (w == 7) wait_kv(ws, 0, 8);
  RAWBAR();
  ST();
  __builtin_amdgcn_s_setprio(3);
  if (w == 6) glds16s_sc1(ws + WS_KMAX, lane16, lds0 + 54272);
  if (w == 7) glds_lin_sc1<33>(ws + WS_STAT + t0 * 8, lane16, lds0 + 55296);
#pragma unroll
  for (int j = 0; j < 3; ++j)
    if (j < 2 || w < KV_PIECES - 16) glds16s_sc1(ws + WS_KV + (w + 8 * j) * 1024, lane16, lds0 + KV_A + (w + 8 * j) * 1024);
  if (w >= 4) {
    const int lw = w & 1;
    if (w < 6) {
      const char* kvg = ws + WS_KV + lw * 1024;
      auto dma = [&](int chunk, int slotoff) {
#pragma unroll
        for (int j = 0; j < 11; ++j) glds16s_sc1(kvg + chunk * KV_CHUNK + j * 2048, lane16, lds0 + slotoff + (lw + 2 * j) * 1024);
      };
      dma(1, KV_B);
      asm volatile("s_waitcnt vmcnt(11)\n\ts_barrier" ::: "memory");
      int fre = KV_Cs, nx = KV_A, nn = KV_B;
#pragma unroll 1
      for (int c = 0; c < 8; ++c) {
        asm volatile("s_waitcnt vmcnt(0)\n\ts_barrier" ::: "memory");
        if (c + 2 < 8) dma(c + 2, fre);
        const int t = fre; fre = nx; nx = nn; nn = t;
      }
    } else {
      asm volatile("s_waitcnt vmcnt(0)\n\ts_barrier" ::: "memory");
      const int s1 = b * 2 + lw, u1 = s1 < 384 ? 800 + s1 : 640 + (s1 - 384), u2 = 768 + b;
      const bool two = lw == 0 && b < 32;
      const unsigned stg = lds0 + O_STG + lw * 6144;
      const LAS char* stp = (const LAS char*)lds + O_STG + lw * 6144;
#define HB() asm volatile("s_barrier" ::: "memory")
      HB(); retile_issue(wo, wo, wo, w2, stg, u1, lane);
      HB(); vmwait<0>(); retile_finish(ws, stp, u1, lane);
      HB(); vmwait<0>();
      if (lane == 0) { if (u1 >= 800) arrive_relay(ws, 48 + (u1 - 800) / 48, 48u); else arrive_relay(ws, 56 + (u1 - 640) / 20, 20u); }
      if (two) retile_issue(wo, wo, wo, w2, stg, u2, lane);
      HB(); HB(); vmwait<0>();
      if (two) retile_finish(ws, stp, u2, lane);
      HB(); vmwait<0>();
      if (two && lane == 0) arrive_relay(ws, 56 + (u2 - 640) / 20, 20u);
      HB(); HB();
#undef HB
    }
  } else {
    const f32x4 b0 = qx[0], b1 = qx[64], b2 = qx[128];
    const f32x4 c0 = *(const LAS f32x4*)(csl + 4 * h), c1 = *(const LAS f32x4*)(csl + 8 + 4 * h), c2 = *(const LAS f32x4*)(csl + 16);
    asm volatile("s_waitcnt vmcnt(0) lgkmcnt(0)\n\ts_barrier" ::: "memory");
    __builtin_amdgcn_s_setprio(APRIO);
    u32x4 q1a, q1b; u32x2 q2a, q2b;
    {
      const f32x2 sa = *(const LAS f32x2*)((LAS char*)lds + 55296 + r * 8), sb = *(const LAS f32x2*)((LAS char*)lds + 55296 + 256 + r * 8);
      const float ka = sa[1] * QSCALE, kb = sb[1] * QSCALE;
#define QA(i, c) ((qa[i] - sa[0] * c[(i) & 3]) * ka)
#define QB(v, i, c) ((v[i] - sb[0] * c[i]) * kb)
      q1a = (u32x4){cvtpk(QA(0, c0), QA(1, c0)), cvtpk(QA(2, c0), QA(3, c0)), cvtpk(QA(4, c1), QA(5, c1)), cvtpk(QA(6, c1), QA(7, c1))};
      q2a = (u32x2){cvtpk(QA(8, c2), QA(9, c2)), cvtpk(QA(10, c2), QA(11, c2))};
      q1b = (u32x4){cvtpk(QB(b0, 0, c0), QB(b0, 1, c0)), cvtpk(QB(b0, 2, c0), QB(b0, 3, c0)), cvtpk(QB(b1, 0, c1), QB(b1, 1, c1)), cvtpk(QB(b1, 2, c1), QB(b1, 3, c1))};
      q2b = (u32x2){cvtpk(QB(b2, 0, c2), QB(b2, 1, c2)), cvtpk(QB(b2, 2, c2), QB(b2, 3, c2))};
#undef QA
#undef QB
    }
    float km;
    {
      const f32x4 km4 = *(const LAS f32x4*)((LAS char*)lds + 54272 + lane * 16);
      int k = __float_as_int(fmaxf(fmaxf(km4[0], km4[1]), fmaxf(km4[2], km4[3])));
      k = max(k, __builtin_amdgcn_update_dpp(0, k, 0x128, 0xf, 0xf, false)); k = max(k, __builtin_amdgcn_update_dpp(0, k, 0x124, 0xf, 0xf, false));
      k = max(k, __builtin_amdgcn_update_dpp(0, k, 0x122, 0xf, 0xf, false)); k = max(k, __builtin_amdgcn_update_dpp(0, k, 0x121, 0xf, 0xf, false));
      km = __int_as_float(max(__builtin_amdgcn_readlane(k, 0), __builtin_amdgcn_readlane(k, 16))) +
           __int_as_float(max(__builtin_amdgcn_readlane(k, 32), __builtin_amdgcn_readlane(k, 48)));
    }
    float qna = dot2(q1a[0], q1a[0], dot2(q1a[1], q1a[1], dot2(q1a[2], q1a[2], dot2(q1a[3], q1a[3], 0.f))));
    float qnb = dot2(q1b[0], q1b[0], dot2(q1b[1], q1b[1], dot2(q1b[2], q1b[2], dot2(q1b[3], q1b[3], 0.f))));
    if (h == 0) { qna = dot2(q2a[0], q2a[0], dot2(q2a[1], q2a[1], qna)); qnb = dot2(q2b[0], q2b[0], dot2(q2b[1], q2b[1], qnb)); }
    { const auto sw = __builtin_amdgcn_permlane32_swap(__float_as_uint(qna), __float_as_uint(qna), false, false); qna = __uint_as_float(sw[0]) + __uint_as_float(sw[1]); }
    { const auto sw = __builtin_amdgcn_permlane32_swap(__float_as_uint(qnb), __float_as_uint(qnb), false, false); qnb = __uint_as_float(sw[0]) + __uint_as_float(sw[1]); }
    const float bda = __builtin_amdgcn_sqrtf(qna * km) * 1.02f + 1e-3f, bdb = __builtin_amdgcn_sqrtf(qnb * km) * 1.02f + 1e-3f;
    const bool fast = __all(bda <= 48.f && bdb <= 48.f);
    if (h == 1) {
      q2a[0] = fast ? (cvtpk(-bda, 0.f) & 0xffffu) : 0u; q2a[1] = 0u;
      q2b[0] = fast ? (cvtpk(-bdb, 0.f) & 0xffffu) : 0u; q2b[1] = 0u;
    }
    const bf16x8 QA1 = __builtin_bit_cast(bf16x8, q1a), QB1 = __builtin_bit_cast(bf16x8, q1b);
    const s16x4 QA2 = __builtin_bit_cast(s16x4, q2a), QB2 = __builtin_bit_cast(s16x4, q2b);
    const int dv = r <= 20 ? r : 21;
    const LAS char* k1b = (const LAS char*)lds + lane * 16;
    const LAS char* k2b = (const LAS char*)lds + (h ? KV_C : KV_K2 + r * 8);
    const LAS char* vb = (const LAS char*)lds + KV_V + (h * VR + dv) * 16;
    f32x16 oa = {}, ob = {};
    const f32x16 zero16 = {};
    ST();
    if (fast) {
      f32x16 sA, sB;
      bf16x8 k1f, vf0, vg0, vf1; s16x4 k2f;
      u32x4 pa, pb;
      float e14, e15;
      k1f = *(const LAS bf16x8*)(k1b + KV_A); k2f = *(const LAS s16x4*)(k2b + KV_A); vf0 = *(const LAS bf16x8*)(vb + KV_A);
      sA = MFMA32(k1f, QA1, zero16); sA = MFMA32K8(k2f, QA2, sA);
      vf1 = (bf16x8){0, 0, 0, 0, 0, 0, 0, 0}; pb = (u32x4){0u, 0u, 0u, 0u}; e14 = 0.f; e15 = 0.f;
      int cur = KV_A, nxt = KV_B, fre = KV_Cs;
#define STEP(C, N, Q1, Q2, OC, OP, V0, L1, L2, L3, L4) do { \
        N = MFMA32(k1f, Q1, zero16); SBAR(); L1; SBAR(); \
        C[0] = EXP2(C[0]); C[1] = EXP2(C[1]); SBAR(); pb[3] = cvtpk(e14, e15); PIN(pb); SBAR(); \
        C[2] = EXP2(C[2]); C[3] = EXP2(C[3]); SBAR(); pa[0] = cvtpk(C[0], C[1]); PIN(pa); SBAR(); \
        OP = MFMA32(vf1, __builtin_bit_cast(bf16x8, pb), OP); SBAR(); L2; SBAR(); \
        C[4] = EXP2(C[4]); C[5] = EXP2(C[5]); SBAR(); pa[1] = cvtpk(C[2], C[3]); PIN(pa); SBAR(); \
        C[6] = EXP2(C[6]); C[7] = EXP2(C[7]); SBAR(); pa[2] = cvtpk(C[4], C[5]); PIN(pa); SBAR(); \
        N = MFMA32K8(k2f, Q2, N); SBAR(); L3; SBAR(); \
        C[8] = EXP2(C[8]); C[9] = EXP2(C[9]); SBAR(); pa[3] = cvtpk(C[6], C[7]); PIN(pa); SBAR(); \
        C[10] = EXP2(C[10]); C[11] = EXP2(C[11]); SBAR(); pb[0] = cvtpk(C[8], C[9]); PIN(pb); SBAR(); \
        OC = MFMA32(V0, __builtin_bit_cast(bf16x8, pa), OC); SBAR(); L4; SBAR(); \
        C[12] = EXP2(C[12]); C[13] = EXP2(C[13]); SBAR(); pb[1] = cvtpk(C[10], C[11]); PIN(pb); SBAR(); \
        e14 = EXP2(C[14]); e15 = EXP2(C[15]); SBAR(); pb[2] = cvtpk(C[12], C[13]); PIN(pb); PIN(e14); PIN(e15); SBAR(); \
      } while (0)
#define KT(i, V0C, V0N) do { \
        STEP(sA, sB, QB1, QB2, oa, ob, vf0, k1f = *(const LAS bf16x8*)((i) < 7 ? kc1 + ((i) + 1) * KV_TILE : k1b + nxt), \
             vf1 = *(const LAS bf16x8*)(vc + (i) * KV_TILE + 2 * VR * 16), k2f = *(const LAS s16x4*)((i) < 7 ? kc2 + ((i) + 1) * KV_TILE : k2b + nxt), (void)0); \
        STEP(sB, sA, QA1, QA2, ob, oa, vf0, (void)0, (void)0, (void)0, vf0 = *(const LAS bf16x8*)((i) < 7 ? vc + ((i) + 1) * KV_TILE : vb + nxt)); \
      } while (0)
#pragma unroll 1
      for (int c = 0; c < 8; ++c) {
        const LAS char* kc1 = k1b + cur; const LAS char* kc2 = k2b + cur; const LAS char* vc = vb + cur;
        KT(0, vf0, vg0); KT(1, vg0, vf0); KT(2, vf0, vg0);
        asm volatile("s_barrier" ::: "memory");
        KT(3, vg0, vf0); KT(4, vf0, vg0); KT(5, vg0, vf0); KT(6, vf0, vg0); KT(7, vg0, vf0);
        const int t = cur; cur = nxt; nxt = fre; fre = t;
      }
      pb[3] = cvtpk(e14, e15);
      ob = MFMA32(vf1, __builtin_bit_cast(bf16x8, pb), ob);
#undef KT
#undef STEP
    } else {
      float ma = -INFINITY, mb = -INFINITY;
      int cur = KV_A, nxt = KV_B, fre = KV_Cs;
#pragma unroll 1
      for (int c = 0; c < 8; ++c) {
#pragma unroll 1
        for (int i = 0; i < 8; ++i) {
          if (i == 3) RAWBAR();
          const int off = cur + i * KV_TILE;
          const bf16x8 k1f = *(const LAS bf16x8*)(k1b + off); const s16x4 k2f = *(const LAS s16x4*)(k2b + off);
          const bf16x8 vf0 = *(const LAS bf16x8*)(vb + off), vf1 = *(const LAS bf16x8*)(vb + off + 2 * VR * 16);
#pragma unroll
          for (int qs = 0; qs < 2; ++qs) {
            f32x16 sc = MFMA32(k1f, qs ? QB1 : QA1, zero16); sc = MFMA32K8(k2f, qs ? QB2 : QA2, sc);
            float& m = qs ? mb : ma; f32x16& o = qs ? ob : oa;
            float mx = sc[0];
#pragma unroll
            for (int j = 1; j < 16; ++j) mx = fmaxf(mx, sc[j]);
            mx = fmaxf(mx, __shfl_xor(mx, 32));
            const float mn = fmaxf(m, mx);
            const float al = EXP2(m - mn);
            m = mn;
#pragma unroll
            for (int j = 0; j < 16; ++j) { o[j] *= al; sc[j] = EXP2(sc[j] - mn); }
            const u32x4 pa = {cvtpk(sc[0], sc[1]), cvtpk(sc[2], sc[3]), cvtpk(sc[4], sc[5]), cvtpk(sc[6], sc[7])};
            const u32x4 pb = {cvtpk(sc[8], sc[9]), cvtpk(sc[10], sc[11]), cvtpk(sc[12], sc[13]), cvtpk(sc[14], sc[15])};
            o = MFMA32(vf0, __builtin_bit_cast(bf16x8, pa), o);
            o = MFMA32(vf1, __builtin_bit_cast(bf16x8, pb), o);
          }
        }
        const int t = cur; cur = nxt; nxt = fre; fre = t;
      }
    }
    PIN(oa); PIN(ob); ST();
#pragma unroll
    for (int qs = 0; qs < 2; ++qs) {
      const f32x16 o = qs ? ob : oa;
      const auto sw = __builtin_amdgcn_permlane32_swap(__float_as_uint(o[8]), __float_as_uint(o[8]), false, false);
      const float il = __builtin_amdgcn_rcpf(__uint_as_float(sw[1]));
      LAS char* os = (LAS char*)lds + O_STG + (qs * 32 + r) * 176 + w * 40 + 8 * h;
      *(LAS u32x2*)(os) = (u32x2){cvtpk(o[0] * il, o[1] * il), cvtpk(o[2] * il, o[3] * il)};
      *(LAS u32x2*)(os + 16) = (u32x2){cvtpk(o[4] * il, o[5] * il), cvtpk(o[6] * il, o[7] * il)};
      if (h == 0) *(LAS u32x2*)(os + 32) = (u32x2){cvtpk(o[8] * il, o[9] * il), cvtpk(o[10] * il, o[11] * il)};
    }
  }
  RAWBAR();
  const __amdgpu_buffer_rsrc_t orr = __builtin_amdgcn_make_buffer_rsrc(ws + WS_O, 0, S * D * 2, 0x00020000);
#pragma unroll
  for (int j = 0; j < 2; ++j) {
    const int ci = tid + 512 * j;
    if (ci < 640) {
      const int c = ci >> 6, cc = hg * 10 + c;
      const u32x4 v = *(const LAS u32x4*)((LAS char*)lds + O_STG + lane * 176 + c * 16);
      __builtin_amdgcn_raw_buffer_store_b128(v, orr, ((tt * 20 + (cc >> 2)) * 4 + (lane >> 4)) * 1024 + ((cc & 3) * 16 + (lane & 15)) * 16, 0, 16);
    }
  }
  vmwait<0>();
  RAWBAR();
  if (tid == 0) arrive(ws, 16 + tt, 8u);
}

struct Frag3 { bf16x8 x; s16x4 w[6]; };
struct Frag5 { bf16x8 x; bf16x8 w[5]; };
DI void ffn_block(const u16* __restrict__ x, const u16* __restrict__ wk, const u16* __restrict__ wv, const u16* __restrict__ w1, const u16* __restrict__ w3, char* __restrict__ ws, u16* __restrict__ out, char* lds, int b) {
  const int tid = threadIdx.x, lane = tid & 63, w = __builtin_amdgcn_readfirstlane(tid >> 6);
  const int bi = b >> 3;
  const unsigned lds0 = (unsigned)(uintptr_t)(LAS char*)lds, lane16 = lane * 16;
  ST_DECL ST();
  __builtin_amdgcn_s_setprio(3);
  if (b < 128) ln_block(x, wk, wv, ws, lds, b);
  else if (b == 128) {
    for (int i = tid; i < (int)(WS_ZEND / 16); i += 512) st16s(ws + i * 16, (u32x4){0u, 0u, 0u, 0u});
    vmwait<0>();
    RAWBAR();
    if (w == 1) {
      GAS unsigned* bp = (GAS unsigned*)(ws + WS_BEACON) + (lane & 31) * 32;
      GAS unsigned* ap = (GAS unsigned*)(ws + WS_ACK) + (lane & 31) * 16;
      unsigned v = __hip_atomic_load(bp, RLX_AGENT);
      for (unsigned spins = 0; spins < (1u << 20); ++spins) {
        if (lane < 32) __hip_atomic_store(bp, ++v, RLX_AGENT);
        __builtin_amdgcn_s_sleep(BSL);
        if (__all(__hip_atomic_load(ap, RLX_AGENT) >= ((lane & 31) == 0 ? 15u : 16u))) break;
      }
    }
  } else if (w == 1) clean_wait(ws, __hip_atomic_load(beaconp(ws), RLX_AGENT));
  __builtin_amdgcn_s_setprio(GPRIO);
  if (w == 1) wait_kv<GSL>(ws, (((b & 7) >> 2) * 8 + (bi & 7)) >> 1, 1);
  RAWBAR();
  {
    const int tp = ((b & 7) >> 2) * 8 + (bi & 7), cg = (b & 3) * 4 + (bi >> 3);
    const char* xsb = ws + WS_XN + (size_t)tp * 163840 + w * 1024;
    const int rr = lane / 12, pos = lane - rr * 12;
    const unsigned bs = ((rr & 3) * F + (pos ^ ((w >> 2) << 1)) * 8) * 2;
    const u16* bb = (w < 4 ? w1 : w3) + (size_t)(w & 3) * 8 * F + cg * 96;
    auto piece = [&](int kt, int slot, int j) {
      if (j == 0) glds16s_sc1(xsb + kt * 8192, lane16, lds0 + slot * SLOT + w * 1024);
      else glds_lin<48>((const char*)(bb + (size_t)(kt * 32 + (j - 1) * 4) * F), bs, lds0 + slot * SLOT + 8192 + (2 * w + j - 1) * 768);
    };
#pragma unroll
    for (int i = 0; i < 4; ++i) { piece(i, i, 0); piece(i, i, 1); piece(i, i, 2); }
    const int tq = w & 3, hc = w >> 2, r = lane & 31, h = lane >> 5;
    const int xrow = tq * 32 + r;
    const LAS char* xb0 = (const LAS char*)lds + xrow * 64 + ((h ^ ((xrow >> 2) & 3)) * 16);
    const LAS char* xb1 = (const LAS char*)lds + xrow * 64 + (((2 + h) ^ ((xrow >> 2) & 3)) * 16);
    const int gu = (lane >> 4) & 1, q4 = (lane & 15) >> 2, p = lane & 3, pp = ((p & 1) << 1) | (p >> 1);
    const LAS char* wb[3];
#pragma unroll
    for (int c = 0; c < 3; ++c) wb[c] = (const LAS char*)lds + 8192 + gu * 6144 + (8 * h + q4) * 192 + (((hc * 3 + c) ^ gu) * 32) + pp * 8;
    f32x16 acc[3] = {};
    auto rd = [&](Frag3& f, int slot, int s) {
      f.x = *(const LAS bf16x8*)((s ? xb1 : xb0) + slot * SLOT);
#pragma unroll
      for (int c = 0; c < 3; ++c) { f.w[2 * c] = trread(wb[c] + slot * SLOT + s * 3072); f.w[2 * c + 1] = trread(wb[c] + slot * SLOT + s * 3072 + 768); }
    };
    auto mm = [&](const Frag3& f, int s, int dk, int dsl) {
#pragma unroll
      for (int c = 0; c < 3; ++c) {
        acc[c] = MFMA32(cat8(f.w[2 * c], f.w[2 * c + 1]), f.x, acc[c]);
        SBAR();
        if (dk >= 0) { if (s == 1 && c == 0) piece(dk, dsl, 0); if (s == 1 && c == 2) piece(dk, dsl, 1); if (s == 0 && c == 0) piece(dk, dsl, 2); }
        SBAR();
      }
    };
    Frag3 f0, f1;
    vmwait<9>();
    RAWBAR();
    rd(f0, 0, 0);
#define IT(kt, sl, VM, PREV, DMA, NEXT) do { \
      rd(f1, sl, 1); SBAR(); mm(f0, 0, PREV ? (kt) + 3 : -1, ((sl) + 3) & 3); SBAR(); \
      if (NEXT) { vmwait<VM>(); RAWBAR(); rd(f0, ((sl) + 1) & 3, 0); } \
      SBAR(); mm(f1, 1, DMA ? (kt) + 4 : -1, sl); SBAR(); } while (0)
    IT(0, 0, 6, false, true, true); IT(1, 1, 6, true, true, true); IT(2, 2, 6, true, true, true); IT(3, 3, 6, true, true, true);
#pragma unroll 1
    for (int kt = 4; kt < 16; kt += 4) {
      if (kt == G1LATE) __builtin_amdgcn_s_setprio(3);
      IT(kt, 0, 6, true, true, true); IT(kt + 1, 1, 6, true, true, true); IT(kt + 2, 2, 6, true, true, true); IT(kt + 3, 3, 6, true, true, true);
    }
    IT(16, 0, 6, true, false, true); IT(17, 1, 3, false, false, true); IT(18, 2, 0, false, false, true); IT(19, 3, 0, false, false, false);
#undef IT
    PIN(acc[0]); PIN(acc[1]); PIN(acc[2]); ST();
    __builtin_amdgcn_s_setprio(3);
    u32x4 hv[3];
#pragma unroll
    for (int c = 0; c < 3; ++c) {
      float v[8];
#pragma unroll
      for (int j = 0; j < 8; ++j) {
        const float gt = acc[c][j], up = acc[c][j + 8];
        v[j] = gt * __builtin_amdgcn_rcpf(1.f + EXP2(-1.4426950408889634f * gt)) * up;
      }
      hv[c] = (u32x4){cvtpk(v[0], v[1]), cvtpk(v[2], v[3]), cvtpk(v[4], v[5]), cvtpk(v[6], v[7])};
    }
#pragma unroll
    for (int c = 0; c < 3; ++c) *(LAS u32x4*)((LAS char*)lds + xrow * 208 + hc * 96 + c * 32 + h * 16) = hv[c];
    RAWBAR();
    const __amdgpu_buffer_rsrc_t hr = __builtin_amdgcn_make_buffer_rsrc(ws + WS_H, 0, S * F * 2, 0x00020000);
#pragma unroll
    for (int j = 0; j < 3; ++j) {
      const int ci = tid + 512 * j, row = ci / 12, pos = ci - row * 12;
      const u32x4 v = *(const LAS u32x4*)((LAS char*)lds + row * 208 + pos * 16);
      __builtin_amdgcn_raw_buffer_store_b128(v, hr, ((tp * 128 + row) * F + cg * 96 + pos * 8) * 2, 0, 16);
    }
    if (w == 1) wait_count(relayp(ws, 48 + (bi >> 2)), 1u);
    vmwait<0>();
    RAWBAR();
    if (tid == 448) arrive(ws, tp, 16u);
  }
  ST();
  const int tt = (b & 7) * 4 + (bi & 3), t0 = tt * 64, cg = bi >> 2, c0 = cg * 80;
  const int ts = w & 3, kh = w >> 2, r16 = lane & 15, gq = lane >> 4, xrow = ts * 16 + r16;
  f32x4 acc[5] = {};
  const char* wog = ws + WS_WO + (size_t)cg * 102400;
  bf16x8 wr[10];
  auto wop = [&](int j) { glds16s_sc1(wog + (j >= 5 ? 10240 : 0) + (j * 8 + w) * 1024, lane16, lds0 + (j * 8 + w) * 1024); };
  {
    const int srow = w * 8 + (lane >> 3);
    const unsigned hs = (srow * F + ((lane & 7) ^ ((srow >> 1) & 7)) * 8) * 2;
    const u16* hb = (const u16*)(ws + WS_H) + (size_t)t0 * F;
    const char* wsrc = ws + WS_W2 + (size_t)cg * 245760 + w * 1280;
    auto piece = [&](int kt, int slot, int j) {
      if (j == 0) glds16s_sc1(hb + kt * 64, hs, lds0 + slot * ST64 + w * 1024);
      else glds_lin_sc1<40>(wsrc + kt * 10240 + (j - 1) * 640, lane16, lds0 + slot * ST64 + 8192 + w * 1280 + (j - 1) * 640);
    };
#pragma unroll
    for (int i = 0; i < 4; ++i) { piece(i, i, 1); piece(i, i, 2); }
    if (kh == 0) {
#pragma unroll
      for (int c = 0; c < 5; ++c) {
        const u32x2 xv = *(const u32x2*)(x + (size_t)(t0 + xrow) * D + c0 + c * 16 + gq * 4);
        acc[c] = (f32x4){bflo(xv[0]), bfhi(xv[0]), bflo(xv[1]), bfhi(xv[1])};
        PIN(acc[c]);
      }
    }
    if (w == 0) wait_count(flagp(ws, tt >> 1), 16u);
    RAWBAR();
    __builtin_amdgcn_s_setprio(P2PRIO);
    ST();
#pragma unroll
    for (int i = 0; i < 4; ++i) piece(i, i, 0);
    const LAS char* xb = (const LAS char*)lds + xrow * 128 + (((kh * 4 + gq) ^ ((xrow >> 1) & 7)) * 16);
    const LAS char* wb = (const LAS char*)lds + 8192 + kh * 5120 + lane * 16;
    auto rd = [&](Frag5& f, int slot) {
      f.x = *(const LAS bf16x8*)(xb + slot * ST64);
#pragma unroll
      for (int c = 0; c < 5; ++c) f.w[c] = *(const LAS bf16x8*)(wb + slot * ST64 + c * 1024);
    };
    auto mm = [&](const Frag5& f, int dk, int dsl) {
#pragma unroll
      for (int c = 0; c < 5; ++c) { acc[c] = MFMA16(f.w[c], f.x, acc[c]); SBAR(); if (dk >= 0 && !(c & 1)) piece(dk, dsl, c >> 1); SBAR(); }
    };
    Frag5 fa, fb;
    vmwait<3>();
    RAWBAR();
    rd(fa, 0);
#define IT(kt, sl, FC, FN, VM, DMA) do { vmwait<VM>(); RAWBAR(); rd(FN, ((sl) + 1) & 3); SBAR(); mm(FC, DMA ? (kt) + 4 : -1, sl); SBAR(); } while (0)
    IT(0, 0, fa, fb, 2, true); IT(1, 1, fb, fa, 4, true); IT(2, 2, fa, fb, 6, true); IT(3, 3, fb, fa, 6, true);
#pragma unroll 1
    for (int kt = 4; kt < 20; kt += 4) {
      IT(kt, 0, fa, fb, 6, true); IT(kt + 1, 1, fb, fa, 6, true); IT(kt + 2, 2, fa, fb, 6, true); IT(kt + 3, 3, fb, fa, 6, true);
    }
    vmwait<6>();
    if (w == 1) wait_count(relayp(ws, 56 + cg), 1u);
    IT(20, 0, fa, fb, 6, false); wop(0); wop(1); wop(9);
    {
      const char* wrp = wog + kh * 51200 + 40960;
#pragma unroll
      for (int j = 0; j < 10; ++j) asm volatile("global_load_dwordx4 %0, %1, %2 sc1" : "=v"(wr[j]) : "v"(lane16), "s"(wrp + j * 1024) : "memory");
    }
    IT(21, 1, fb, fa, 16, false); wop(2); wop(3);
    IT(22, 2, fa, fb, 15, false); wop(4); wop(5);
    mm(fb, -1, 0);
#undef IT
  }
  PIN(acc[0]); ST();
  RAWBAR();
  wop(6); wop(7); wop(8);
  asm volatile("s_waitcnt vmcnt(0)" : "+v"(wr[0]), "+v"(wr[1]), "+v"(wr[2]), "+v"(wr[3]), "+v"(wr[4]), "+v"(wr[5]), "+v"(wr[6]), "+v"(wr[7]), "+v"(wr[8]), "+v"(wr[9]) :: "memory");
  vmwait<0>();
  __builtin_amdgcn_s_setprio(3);
  ST();
  const LAS char* wbt = (const LAS char*)lds + kh * 40960 + lane * 16;
  if (w == 0) wait_count(flagp(ws, 16 + tt), 8u);
  RAWBAR();
  ST();
  {
    u32x4 of[10];
    const char* op = ws + WS_O + ((tt * 20 + kh * 10) * 4 + ts) * 1024;
#pragma unroll
    for (int s = 0; s < 10; ++s) asm volatile("global_load_dwordx4 %0, %1, %2 sc1" : "=v"(of[(s + 8) % 10]) : "v"(lane16), "s"(op + ((s + 8) % 10) * 4096) : "memory");
#define OSTEP(s, N, WC, WN) do { \
      if ((s) + 1 < 8) { _Pragma("unroll") for (int c = 0; c < 5; ++c) WN[c] = *(const LAS bf16x8*)(wbt + ((s) + 1) * 5120 + c * 1024); } \
      SBAR(); asm volatile("s_waitcnt vmcnt(" #N ")" : "+v"(of[s]) :: "memory"); SBAR(); \
      _Pragma("unroll") for (int c = 0; c < 5; ++c) acc[c] = MFMA16(WC[c], __builtin_bit_cast(bf16x8, of[s]), acc[c]); SBAR(); } while (0)
    asm volatile("s_waitcnt vmcnt(9)" : "+v"(of[8]) :: "memory");
#pragma unroll
    for (int c = 0; c < 5; ++c) acc[c] = MFMA16(wr[c], __builtin_bit_cast(bf16x8, of[8]), acc[c]);
    SBAR();
    bf16x8 w0[5];
#pragma unroll
    for (int c = 0; c < 5; ++c) w0[c] = *(const LAS bf16x8*)(wbt + c * 1024);
    SBAR(); asm volatile("s_waitcnt vmcnt(8)" : "+v"(of[9]) :: "memory"); SBAR();
#pragma unroll
    for (int c = 0; c < 5; ++c) acc[c] = MFMA16(wr[5 + c], __builtin_bit_cast(bf16x8, of[9]), acc[c]);
    SBAR();
    bf16x8 wn[5];
    OSTEP(0, 7, w0, wn); OSTEP(1, 6, wn, w0); OSTEP(2, 5, w0, wn); OSTEP(3, 4, wn, w0);
    OSTEP(4, 3, w0, wn); OSTEP(5, 2, wn, w0); OSTEP(6, 1, w0, wn); OSTEP(7, 0, wn, w0);
#undef OSTEP
  }
  RAWBAR();
#pragma unroll
  for (int c = 0; c < 5; ++c) *(LAS f32x4*)((LAS char*)lds + kh * 64 * 336 + xrow * 336 + c * 64 + gq * 16) = acc[c];
  RAWBAR();
#pragma unroll
  for (int j = 0; j < 2; ++j) {
    const int ci = tid + 512 * j;
    if (ci < 640) {
      const int row = ci / 10, c = ci - row * 10;
      const LAS f32x4* a0 = (const LAS f32x4*)((LAS char*)lds + row * 336 + c * 32);
      const LAS f32x4* a1 = (const LAS f32x4*)((LAS char*)lds + 64 * 336 + row * 336 + c * 32);
      const f32x4 lo = a0[0] + a1[0], hi = a0[1] + a1[1];
      const u32x4 res = {cvtpk(lo[0], lo[1]), cvtpk(lo[2], lo[3]), cvtpk(hi[0], hi[1]), cvtpk(hi[2], hi[3])};
      *(u32x4*)(out + (size_t)(t0 + row) * D + c0 + c * 8) = res;
    }
  }
  ST(); ST_OUT(ws, 2);
}

__global__ __launch_bounds__(512, 4) void k_main(const u16* __restrict__ x, const u16* __restrict__ wq, const u16* __restrict__ wk,
                                                  const u16* __restrict__ wv, const u16* __restrict__ wo, const u16* __restrict__ w1,
                                                  const u16* __restrict__ w3, const u16* __restrict__ w2, char* __restrict__ ws,
                                                  u16* __restrict__ out) {
  extern __shared__ __attribute__((aligned(16))) char lds[];
  if (blockIdx.x >= 256) attn_block(x, wq, w2, wo, ws, lds, blockIdx.x - 256);
  else ffn_block(x, wk, wv, w1, w3, ws, out, lds, blockIdx.x);
}

extern "C" void kernel_launch(void* const* d_in, const int* in_sizes, int n_in, void* d_out, int out_size, void* d_ws, size_t ws_size,
                              hipStream_t stream) {
  const u16* x = (const u16*)d_in[0];
  const u16* wq = (const u16*)d_in[1];
  const u16* wk = (const u16*)d_in[2];
  const u16* wv = (const u16*)d_in[3];
  const u16* wo = (const u16*)d_in[4];
  const u16* w1 = (const u16*)d_in[6];
  const u16* w3 = (const u16*)d_in[7];
  const u16* w2 = (const u16*)d_in[8];
  char* ws = (char*)d_ws;
  (void)hipFuncSetAttribute((const void*)k_main, hipFuncAttributeMaxDynamicSharedMemorySize, LDS_BYTES);
  k_main<<<512, 512, LDS_BYTES, stream>>>(x, wq, wk, wv, wo, w1, w3, w2, ws, (u16*)d_out);
}
```

```cpp
#include <hip/hip_runtime.h>
#include <hip/hip_bf16.h>
#include <stdint.h>

typedef short bf16x8 __attribute__((ext_vector_type(8)));
typedef short s16x4 __attribute__((ext_vector_type(4)));
typedef float f32x2 __attribute__((ext_vector_type(2)));
typedef float f32x4 __attribute__((ext_vector_type(4)));
typedef float f32x16 __attribute__((ext_vector_type(16)));
typedef unsigned u32x4 __attribute__((ext_vector_type(4)));
typedef unsigned u32x2 __attribute__((ext_vector_type(2)));
typedef __bf16 bf16x2_t __attribute__((ext_vector_type(2)));
typedef unsigned short u16;
#define LAS __attribute__((address_space(3)))
#define GAS __attribute__((address_space(1)))
#define RLX_AGENT __ATOMIC_RELAXED, __HIP_MEMORY_SCOPE_AGENT
#define DI __device__ __forceinline__
#define SBAR() __builtin_amdgcn_sched_barrier(0)
#define PIN(x) asm volatile("" : "+v"(x))

constexpr int GPRIO = 1, APRIO = 2;
constexpr int S = 2048, D = 640, H = 32, DH = 20, F = 1536;
constexpr int VR = 22;
constexpr int KV_K2 = 1024, KV_V = 1280, KV_VS = 768, KV_C = KV_V + 2 * VR * 16, KV_TILE = KV_V + 2 * KV_VS;
constexpr int KV_CHUNK = 8 * KV_TILE;
constexpr int KV_PIECES = (KV_CHUNK + 1023) / 1024, KV_SLOT = KV_PIECES * 1024;
constexpr size_t WS_CNT = 0;
constexpr size_t WS_KVF = 12288;
constexpr size_t WS_KVC = 16384;
constexpr size_t WS_ACK = 17408;
constexpr size_t WS_ZEND = 20480;
constexpr size_t WS_BEACON = 20480;
constexpr size_t WS_KMAX = 24576;
constexpr size_t WS_STAT = 28672;
constexpr size_t WS_KV = 49152;
constexpr size_t WS_XN = WS_KV + 192 * 1024;
constexpr size_t WS_O = WS_XN + (size_t)S * D * 2;
constexpr size_t WS_H = WS_O + (size_t)S * D * 2;
constexpr size_t WS_WO = WS_H + (size_t)S * F * 2;
constexpr size_t WS_W2 = WS_WO + (size_t)D * D * 2;
constexpr float QSCALE = 0.22360679774997896f * 1.4426950408889634f;
constexpr int LDS_BYTES = 81920, SLOT = 20480;
constexpr unsigned SPIN_MAX = 1u << 18;

DI unsigned cvtpk(float a, float b) { f32x2 v = {a, b}; bf16x2_t r = __builtin_convertvector(v, bf16x2_t); return __builtin_bit_cast(unsigned, r); }
DI float bflo(unsigned u) { return __uint_as_float(u << 16); }
DI float bfhi(unsigned u) { return __uint_as_float(u & 0xffff0000u); }
DI float rbf(float a) { return bflo(cvtpk(a, a)); }
DI float dot2(unsigned a, unsigned b, float c) { return __builtin_amdgcn_fdot2_f32_bf16(__builtin_bit_cast(bf16x2_t, a), __builtin_bit_cast(bf16x2_t, b), c, false); }
DI s16x4 trread(const LAS char* p) { return __builtin_bit_cast(s16x4, __builtin_amdgcn_ds_read_tr16_b64_v4i16((LAS s16x4*)p)); }
DI bf16x8 cat8(s16x4 a, s16x4 b) { return __builtin_shufflevector(a, b, 0, 1, 2, 3, 4, 5, 6, 7); }
DI void glds16s(const void* sbase, unsigned voff, unsigned lds_base) {
  unsigned sv;
  asm volatile("s_mov_b32 %0, m0\n\ts_mov_b32 m0, %3\n\ts_nop 0\n\tglobal_load_lds_dwordx4 %1, %2\n\ts_mov_b32 m0, %0" : "=&s"(sv) : "v"(voff), "s"(sbase), "s"(lds_base) : "memory");
}
DI void glds16s_sc1(const void* sbase, unsigned voff, unsigned lds_base) {
  unsigned sv;
  asm volatile("s_mov_b32 %0, m0\n\ts_mov_b32 m0, %3\n\ts_nop 0\n\tglobal_load_lds_dwordx4 %1, %2 sc1\n\ts_mov_b32 m0, %0" : "=&s"(sv) : "v"(voff), "s"(sbase), "s"(lds_base) : "memory");
}
#define MFMA32(a, b, c) __builtin_amdgcn_mfma_f32_32x32x16_bf16(a, b, c, 0, 0, 0)
#define MFMA32K8(a, b, c) __builtin_amdgcn_mfma_f32_32x32x8bf16_1k(a, b, c, 0, 0, 0)
#define MFMA16(a, b, c) __builtin_amdgcn_mfma_f32_16x16x32_bf16(a, b, c, 0, 0, 0)
#define EXP2(v) __builtin_amdgcn_exp2f(v)
#define RAWBAR() asm volatile("s_waitcnt lgkmcnt(0)\n\ts_barrier" ::: "memory")
DI void st8wt(void* p, u32x2 v) { asm volatile("global_store_dwordx2 %0, %1, off sc1" :: "v"(p), "v"(v) : "memory"); }
DI void st4wt(void* p, unsigned v) { asm volatile("global_store_dword %0, %1, off sc1" :: "v"(p), "v"(v) : "memory"); }
DI void st16s(void* p, u32x4 v) { asm volatile("global_store_dwordx4 %0, %1, off sc1\n\ts_nop 1" :: "v"(p), "v"(v) : "memory"); }
template <int N> DI void vmwait() { asm volatile("s_waitcnt vmcnt(%0)" :: "n"(N) : "memory"); }
DI void ln_scalars(float sum, float sq, float& mean_b, float& rden) {
  const float mean = sum * (1.f / D);
  const float var = fmaxf(sq * (1.f / D) - mean * mean, 0.f);
  mean_b = rbf(mean);
  rden = __builtin_amdgcn_rcpf(rbf(__builtin_amdgcn_sqrtf(rbf(rbf(var) + 1e-5f))));
}

template <int NL> DI void glds_lin_sc1(const char* src, unsigned lane16, unsigned dst) {
  unsigned sv;
  asm volatile("s_mov_b32 %0, m0\n\ts_mov_b32 m0, %3\n\ts_mov_b32 exec_hi, %4\n\tglobal_load_lds_dwordx4 %1, %2 sc1\n\ts_mov_b32 exec_hi, -1\n\ts_mov_b32 m0, %0"
               : "=&s"(sv) : "v"(lane16), "s"(src), "s"(dst), "n"((1u << (NL - 32)) - 1u) : "memory");
}
template <int NL> DI void glds_lin(const char* src, unsigned lane16, unsigned dst) {
  unsigned sv;
  asm volatile("s_mov_b32 %0, m0\n\ts_mov_b32 m0, %3\n\ts_mov_b32 exec_hi, %4\n\tglobal_load_lds_dwordx4 %1, %2\n\ts_mov_b32 exec_hi, -1\n\ts_mov_b32 m0, %0"
               : "=&s"(sv) : "v"(lane16), "s"(src), "s"(dst), "n"((1u << (NL - 32)) - 1u) : "memory");
}

constexpr int ST64 = 18432;

DI void retile_issue(const u16* __restrict__ wo, const u16* __restrict__ w2, unsigned ldsu, int u, int lane) {
  const int i = u < 160 ? u : u - 160, nk = u < 160 ? 20 : 48, cg = i / nk, ks = i - cg * nk, c0 = cg * 80 - (cg & 1) * 16;
  const u16* src = (u < 160 ? wo : w2) + (size_t)ks * 32 * D + c0;
#pragma unroll
  for (int j = 0; j < 3; ++j) {
    const int ci = j * 64 + lane, rr = ci / 12, pos = ci - rr * 12;
    const unsigned vo = (rr * D + (c0 + pos * 8 < D ? pos : 0) * 8) * 2;
    glds16s(src, vo, ldsu + j * 1024);
    glds16s(src + 16 * D, vo, ldsu + 3072 + j * 1024);
  }
}
DI void retile_finish(char* __restrict__ ws, const LAS char* im, int u, int lane) {
  const int i = u < 160 ? u : u - 160, cg = i / (u < 160 ? 20 : 48);
  const LAS char* a = im + ((lane >> 4) * 8 + ((lane & 15) >> 2)) * 192 + (cg & 1) * 32 + (lane & 3) * 8;
  char* dst = ws + (u < 160 ? WS_WO : WS_W2) + (size_t)i * 5120 + lane * 16;
#pragma unroll
  for (int c = 0; c < 5; ++c) st16s(dst + c * 1024, __builtin_bit_cast(u32x4, cat8(trread(a + c * 32), trread(a + c * 32 + 768))));
}

DI GAS unsigned* beaconp(char* ws) { return (GAS unsigned*)(ws + WS_BEACON) + ((blockIdx.x >> 3) & 31) * 32; }
DI void beacon_sample(char* ws, unsigned& b0) { GAS unsigned* p = beaconp(ws); asm volatile("global_load_dword %0, %1, off sc1" : "=v"(b0) : "v"(p) : "memory"); }
DI void clean_wait(char* ws, unsigned b0) {
  GAS unsigned* p = beaconp(ws);
  for (unsigned spins = 0; spins < SPIN_MAX; ++spins) {
    if (__all(__hip_atomic_load(p, RLX_AGENT) != b0)) break;
    __builtin_amdgcn_s_sleep(4);
  }
  if ((threadIdx.x & 63) == 0) __hip_atomic_fetch_add((GAS unsigned*)(ws + WS_ACK) + (blockIdx.x & 31) * 16, 1u, RLX_AGENT);
}

constexpr int LN_ST = 25600, LN_RED = LN_ST + 512;
DI float xsum16(float v) { const auto a = __builtin_amdgcn_permlane16_swap(__float_as_uint(v), __float_as_uint(v), false, false); return __uint_as_float(a[0]) + __uint_as_float(a[1]); }
DI float xsum32(float v) { const auto a = __builtin_amdgcn_permlane32_swap(__float_as_uint(v), __float_as_uint(v), false, false); return __uint_as_float(a[0]) + __uint_as_float(a[1]); }
DI void ln_block(const u16* __restrict__ x, const u16* __restrict__ wk, const u16* __restrict__ wv, char* __restrict__ ws, char* ldsb, int b) {
  const int tid = threadIdx.x, lane = tid & 63, w = __builtin_amdgcn_readfirstlane(tid >> 6), kv = b >> 7;
  const int t16 = (b & 7) * 16 + ((b >> 3) & 15);
  LAS char* lds = (LAS char*)ldsb;
  if (w >= 4) {
    if (w == 7) {
      unsigned b0 = __hip_atomic_load(beaconp(ws), RLX_AGENT);
      PIN(b0);
      RAWBAR(); RAWBAR();
      clean_wait(ws, b0);
      RAWBAR();
    } else if (w == 6 && b == 0) {
      GAS unsigned* bp = (GAS unsigned*)(ws + WS_BEACON) + (lane & 31) * 32;
      GAS unsigned* ap = (GAS unsigned*)(ws + WS_ACK) + (lane & 31) * 16;
      unsigned v = __hip_atomic_load(bp, RLX_AGENT);
      for (int i = lane; i < (int)(WS_ZEND / 16); i += 64) st16s(ws + i * 16, (u32x4){0u, 0u, 0u, 0u});
      vmwait<0>();
      if (lane < 32) __hip_atomic_store(bp, ++v, RLX_AGENT);
      RAWBAR();
      if (lane < 32) __hip_atomic_store(bp, ++v, RLX_AGENT);
      RAWBAR();
      RAWBAR();
      for (unsigned spins = 0; spins < SPIN_MAX; ++spins) {
        if (lane < 32) __hip_atomic_store(bp, ++v, RLX_AGENT);
        __builtin_amdgcn_s_sleep(12);
        if (__all(__hip_atomic_load(ap, RLX_AGENT) >= 16u)) break;
      }
    } else {
      if (w < 6) {
        const u16* wsrc = kv ? wv : wk;
#pragma unroll
        for (int j = 0; j < 13; ++j)
          if (j < 12 || w == 4) glds16s(wsrc + (w - 4 + 2 * j) * 512, lane * 16, (unsigned)(uintptr_t)lds + (w - 4 + 2 * j) * 1024);
        vmwait<0>();
      }
      RAWBAR(); RAWBAR(); RAWBAR();
    }
    return;
  }
  const int r16 = lane & 15, gq = lane >> 4;
  const int token = t16 * 16 + r16;
  u32x4 xf[5];
  const u16* xp = x + (size_t)token * D + w * 160 + gq * 8;
#pragma unroll
  for (int s = 0; s < 5; ++s) xf[s] = *(const u32x4*)(xp + s * 32);
  float sum = 0.f, sq = 0.f, sum2 = 0.f, sq2 = 0.f;
#pragma unroll
  for (int s = 0; s < 5; ++s)
#pragma unroll
    for (int j = 0; j < 4; j += 2) {
      sum = dot2(xf[s][j], 0x3F803F80u, sum); sq = dot2(xf[s][j], xf[s][j], sq);
      sum2 = dot2(xf[s][j + 1], 0x3F803F80u, sum2); sq2 = dot2(xf[s][j + 1], xf[s][j + 1], sq2);
    }
  sum = xsum32(xsum16(sum + sum2)); sq = xsum32(xsum16(sq + sq2));
  LAS f32x2* stp = (LAS f32x2*)(lds + LN_ST);
  if (gq == 0) stp[w * 16 + r16] = (f32x2){sum, sq};
  RAWBAR();
  { const f32x2 a = stp[r16], c = stp[16 + r16], d = stp[32 + r16], e = stp[48 + r16]; sum = (a[0] + c[0]) + (d[0] + e[0]); sq = (a[1] + c[1]) + (d[1] + e[1]); }
  float mean_b, rden;
  ln_scalars(sum, sq, mean_b, rden);
#pragma unroll
  for (int s = 0; s < 5; ++s)
#pragma unroll
    for (int j = 0; j < 4; ++j) {
      const unsigned d = cvtpk(bflo(xf[s][j]) - mean_b, bfhi(xf[s][j]) - mean_b);
      xf[s][j] = cvtpk(bflo(d) * rden, bfhi(d) * rden);
    }
  const int q4 = (lane & 15) >> 2, p = lane & 3;
  const LAS char* e0 = lds + (w * 160 + gq * 8 + q4) * 40 + p * 8;
  const LAS char* e1 = lds + (w * 160 + gq * 8 + q4) * 40 + 32;
  f32x4 acc[2] = {};
  if (kv == 0) {
#pragma unroll
    for (int s = 0; s < 5; ++s) {
      acc[0] = MFMA16(cat8(trread(e0 + s * 1280), trread(e0 + s * 1280 + 160)), __builtin_bit_cast(bf16x8, xf[s]), acc[0]);
      acc[1] = MFMA16(cat8(trread(e1 + s * 1280), trread(e1 + s * 1280 + 160)), __builtin_bit_cast(bf16x8, xf[s]), acc[1]);
    }
  } else {
#pragma unroll
    for (int s = 0; s < 5; ++s) {
      acc[0] = MFMA16(__builtin_bit_cast(bf16x8, xf[s]), cat8(trread(e0 + s * 1280), trread(e0 + s * 1280 + 160)), acc[0]);
      acc[1] = MFMA16(__builtin_bit_cast(bf16x8, xf[s]), cat8(trread(e1 + s * 1280), trread(e1 + s * 1280 + 160)), acc[1]);
    }
  }
  if ((w >> 1) == kv) {
    const int row = token & 127;
    char* xn = ws + WS_XN + ((size_t)((token >> 7) * 20 + w * 5) * 128 + row) * 64 + ((gq ^ ((row >> 2) & 3)) * 16);
#pragma unroll
    for (int s = 0; s < 5; ++s) st16s(xn + s * 8192, xf[s]);
  }
  if (kv == 1 && w == 1 && gq == 0) st8wt(ws + WS_STAT + token * 8, (u32x2){__float_as_uint(mean_b), __float_as_uint(rden)});
  LAS f32x4* red = (LAS f32x4*)(lds + LN_RED) + lane;
  red[(w * 2) * 64] = acc[0]; red[(w * 2 + 1) * 64] = acc[1];
  RAWBAR();
  if (w == 0) {
    const f32x4 a = (red[0] + red[2 * 64]) + (red[4 * 64] + red[6 * 64]), c = (red[64] + red[3 * 64]) + (red[5 * 64] + red[7 * 64]);
    const u32x2 v = {cvtpk(a[0], a[1]), cvtpk(a[2], a[3])}, v2 = {cvtpk(c[0], c[1]), cvtpk(c[2], c[3])};
    char* kvp = ws + WS_KV + (t16 >> 1) * KV_TILE;
    LAS char* stg = lds;
    if (kv == 0) {
      *(LAS u32x2*)(stg + (gq >> 1) * 256 + r16 * 16 + (gq & 1) * 8) = v;
      if (gq == 0) *(LAS u32x2*)(stg + 512 + r16 * 8) = v2;
      const float n2 = dot2(v[0], v[0], dot2(v[1], v[1], gq == 0 ? dot2(v2[0], v2[0], dot2(v2[1], v2[1], 0.f)) : 0.f));
      int k = __float_as_int(xsum32(xsum16(n2)));
      k = max(k, __builtin_amdgcn_update_dpp(0, k, 0x128, 0xf, 0xf, false)); k = max(k, __builtin_amdgcn_update_dpp(0, k, 0x124, 0xf, 0xf, false));
      k = max(k, __builtin_amdgcn_update_dpp(0, k, 0x122, 0xf, 0xf, false)); k = max(k, __builtin_amdgcn_update_dpp(0, k, 0x121, 0xf, 0xf, false));
      if (lane == 0) st4wt(ws + WS_KMAX + t16 * 4, (unsigned)k);
      const u32x4 o = *(const LAS u32x4*)(stg + lane * 16);
      const int hh = lane >> 4;
      if (lane < 40) st16s(kvp + (hh < 2 ? hh * 512 + (t16 & 1) * 256 + r16 * 16 : KV_K2 + (t16 & 1) * 128 + r16 * 16), o);
    } else {
      LAS char* vp = stg + ((gq & 1) * VR) * 16 + (gq >> 1) * 8;
      *(LAS u32x2*)(vp + r16 * 16) = v;
      const u32x2 one = {0x3F803F80u, 0x3F803F80u}, zer = {0u, 0u};
      if (r16 < 6) *(LAS u32x2*)(vp + (16 + r16) * 16) = r16 < 4 ? v2 : (r16 == 4 ? one : zer);
      else if (lane >= 56) *(LAS u32x2*)(stg + 2 * VR * 16 + (lane - 56) * 8) = lane == 56 ? (u32x2){0x00003F80u, 0u} : zer;
      const u32x4 o = *(const LAS u32x4*)(stg + lane * 16);
      if (lane < 48) st16s(kvp + KV_V + (t16 & 1) * KV_VS + lane * 16, o);
    }
  }
  vmwait<0>();
  RAWBAR();
  if (w == 0) {
    unsigned old = 0;
    if (lane == 0) old = __hip_atomic_fetch_add((GAS unsigned*)(ws + WS_KVC) + (t16 >> 4) * 16, 1u, RLX_AGENT);
    if (__builtin_amdgcn_readfirstlane(old) == 31u && lane < 32) __hip_atomic_store((GAS unsigned*)(ws + WS_KVF) + lane * 32 + (t16 >> 4), 1u, RLX_AGENT);
  }
}

DI void arrive(char* ws, int i) {
  __hip_atomic_fetch_add((GAS unsigned*)(ws + WS_CNT) + i * 16, 1u, RLX_AGENT);
}
DI void arrive_relay(char* ws, int i, unsigned want) {
  if (__hip_atomic_fetch_add((GAS unsigned*)(ws + WS_CNT) + i * 16, 1u, RLX_AGENT) == want - 1u)
    __hip_atomic_store((GAS unsigned*)(ws + WS_CNT + 4096) + i * 32, 1u, RLX_AGENT);
}
DI GAS unsigned* relayp(char* ws, int i) { return (GAS unsigned*)(ws + WS_CNT + 4096) + i * 32; }
DI GAS unsigned* flagp(char* ws, int i) {
  return (GAS unsigned*)(ws + WS_CNT) + i * 16;
}
template <int SL = 1>
DI void wait_kv(char* ws, int first, int n) {
  GAS unsigned* p = (GAS unsigned*)(ws + WS_KVF) + ((blockIdx.x >> 3) & 31) * 32 + first + min((int)(threadIdx.x & 63), n - 1);
  for (unsigned spins = 0; spins < SPIN_MAX; ++spins) {
    if (__all(__hip_atomic_load(p, RLX_AGENT) != 0u)) break;
    __builtin_amdgcn_s_sleep(SL);
  }
}
DI void wait_count(GAS unsigned* cnt, unsigned want) {
  for (unsigned spins = 0; spins < SPIN_MAX; ++spins) {
    if ((unsigned)__builtin_amdgcn_readfirstlane(__hip_atomic_load(cnt, RLX_AGENT)) >= want) break;
    __builtin_amdgcn_s_sleep(1);
  }
}

constexpr int KV_A = LDS_BYTES - KV_SLOT, KV_B = 0, KV_Cs = KV_SLOT, O_STG = 2 * KV_SLOT;

struct FragQ { bf16x8 x[4]; s16x4 w[8]; };
DI void attn_block(const u16* __restrict__ x, const u16* __restrict__ wq, const u16* __restrict__ w2, const u16* __restrict__ wo, char* __restrict__ ws, char* lds, int b) {
  const int tid = threadIdx.x, lane = tid & 63, w = __builtin_amdgcn_readfirstlane(tid >> 6);
  const int bi = b >> 3;
  const int tt = (b & 7) * 4 + (bi & 3), hg = bi >> 2;
  const int r = lane & 31, h = lane >> 5;
  const unsigned lds0 = (unsigned)(uintptr_t)(LAS char*)lds;
  const unsigned lane16 = lane * 16;
  const int th = w >> 2, hd = w & 3, t0 = tt * 64;
  __builtin_amdgcn_s_setprio(APRIO);
  unsigned b0;
  beacon_sample(ws, b0);
  f32x16 qa = {};
  float cs0 = 0.f, cs1 = 0.f;
  {
    const int odd = hg & 1, c0 = hg * 80 - odd * 16;
    const int srow = w * 8 + (lane >> 3);
    const unsigned as = (srow * D + ((lane & 7) ^ ((srow >> 1) & 7)) * 8) * 2;
    const u16* ab = x + (size_t)t0 * D;
    const int rr = lane / 12, ch = lane - rr * 12;
    const unsigned bs = ((rr & 3) * D + (c0 + ch * 8 < D ? ch : 0) * 8) * 2;
    const u16* bb = wq + (size_t)w * 8 * D + c0;
    auto piece = [&](int kt, int j) {
      const int slot = kt & 3;
      if (j == 0) glds16s(ab + kt * 64, as, lds0 + slot * SLOT + w * 1024);
      else glds_lin<48>((const char*)(bb + (size_t)(kt * 64 + (j - 1) * 4) * D), bs, lds0 + slot * SLOT + 8192 + (2 * w + j - 1) * 768);
    };
#pragma unroll
    for (int i = 0; i < 4; ++i) { piece(i, 0); piece(i, 1); piece(i, 2); }
    const int xrow = th * 32 + r;
    const LAS char* xb[4];
#pragma unroll
    for (int s = 0; s < 4; ++s) xb[s] = (const LAS char*)lds + xrow * 128 + (((2 * s + h) ^ ((xrow >> 1) & 7)) * 16);
    const int gI = ((lane >> 4) & 1) * 4 + (lane & 3), q4 = (lane & 15) >> 2;
    const int dcol = gI < 4 ? ((gI & 1) * 8 + (gI >> 1) * 4) : 16;
    const LAS char* wb = (const LAS char*)lds + 8192 + (8 * h + q4) * 192 + odd * 32 + hd * 40 + dcol * 2;
    auto rd = [&](FragQ& f, int slot) {
#pragma unroll
      for (int s = 0; s < 4; ++s) {
        f.x[s] = *(const LAS bf16x8*)(xb[s] + slot * SLOT);
        f.w[2 * s] = trread(wb + slot * SLOT + s * 3072); f.w[2 * s + 1] = trread(wb + slot * SLOT + s * 3072 + 768);
      }
    };
    auto mm = [&](const FragQ& f, int dk) {
#pragma unroll
      for (int s = 0; s < 4; ++s) {
        qa = MFMA32(cat8(f.w[2 * s], f.w[2 * s + 1]), f.x[s], qa);
        SBAR(); if (dk >= 0 && s < 3) piece(dk, s);
        const u32x2 a = __builtin_bit_cast(u32x2, f.w[2 * s]), c = __builtin_bit_cast(u32x2, f.w[2 * s + 1]);
        if (w < 4) { cs0 = dot2(a[0], 0x3F803F80u, cs0); cs1 = dot2(a[1], 0x3F803F80u, cs1); cs0 = dot2(c[0], 0x3F803F80u, cs0); cs1 = dot2(c[1], 0x3F803F80u, cs1); }
        PIN(cs0); PIN(cs1); SBAR();
      }
    };
    FragQ fa, fb;
    vmwait<9>(); PIN(b0);
    RAWBAR();
    rd(fa, 0);
#define IT(kt, FC, FN, VM, DMA) do { vmwait<VM>(); RAWBAR(); rd(FN, ((kt) + 1) & 3); SBAR(); mm(FC, DMA ? (kt) + 4 : -1); SBAR(); } while (0)
    IT(0, fa, fb, 6, true); IT(1, fb, fa, 6, true); IT(2, fa, fb, 6, true); IT(3, fb, fa, 6, true);
    IT(4, fa, fb, 6, true); IT(5, fb, fa, 6, true);
    if (w == 7) clean_wait(ws, b0);
    IT(6, fa, fb, 6, false);
    vmwait<3>();
    if (w == 7) wait_kv(ws, 0, 8);
    RAWBAR();
    if (w == 6) glds16s_sc1(ws + WS_KMAX, lane16, lds0 + 54272);
    if (w == 7) glds_lin_sc1<33>(ws + WS_STAT + t0 * 8, lane16, lds0 + 55296);
#pragma unroll
    for (int j = 0; j < 3; ++j)
      if (j < 2 || w < KV_PIECES - 16) glds16s_sc1(ws + WS_KV + (w + 8 * j) * 1024, lane16, lds0 + KV_A + (w + 8 * j) * 1024);
    rd(fa, 0); SBAR(); mm(fb, -1); SBAR();
    IT(8, fa, fb, 3, false);
    mm(fb, -1);
#undef IT
  }
  LAS float* csl = (LAS float*)((LAS char*)lds + 53248 + hd * 128);
  cs0 += cs1;
  { const auto sw = __builtin_amdgcn_permlane32_swap(__float_as_uint(cs0), __float_as_uint(cs0), false, false); cs0 = __uint_as_float(sw[0]) + __uint_as_float(sw[1]); }
  LAS f32x4* qx = (LAS f32x4*)((LAS char*)lds + 2 * SLOT) + hd * 192 + lane;
  if (w < 4) { if (h == 0) csl[r] = cs0; }
  else { qx[0] = (f32x4){qa[0], qa[1], qa[2], qa[3]}; qx[64] = (f32x4){qa[4], qa[5], qa[6], qa[7]}; qx[128] = (f32x4){qa[8], qa[9], qa[10], qa[11]}; }
  PIN(qa);
  vmwait<0>();
  RAWBAR();
  __builtin_amdgcn_s_setprio(3);
  if (w >= 4) {
    const int lw = w & 1;
    if (w < 6) {
      const char* kvg = ws + WS_KV + lw * 1024;
      auto dma = [&](int chunk, int slotoff) {
#pragma unroll
        for (int j = 0; j < 11; ++j) glds16s_sc1(kvg + chunk * KV_CHUNK + j * 2048, lane16, lds0 + slotoff + (lw + 2 * j) * 1024);
      };
      dma(1, KV_B);
      int fre = KV_Cs, nx = KV_A, nn = KV_B;
#pragma unroll 1
      for (int c = 0; c < 8; ++c) {
        asm volatile("s_waitcnt vmcnt(0)\n\ts_barrier" ::: "memory");
        if (c + 2 < 8) dma(c + 2, fre);
        const int t = fre; fre = nx; nx = nn; nn = t;
      }
    } else {
      const int s1 = b * 2 + lw, u1 = s1 < 384 ? 160 + s1 : s1 - 384, u2 = 128 + b;
      const bool two = lw == 0 && b < 32;
      const unsigned stg = lds0 + O_STG + lw * 6144;
      const LAS char* stp = (const LAS char*)lds + O_STG + lw * 6144;
#define HB() asm volatile("s_barrier" ::: "memory")
      HB(); retile_issue(wo, w2, stg, u1, lane);
      HB(); vmwait<0>(); retile_finish(ws, stp, u1, lane);
      HB(); vmwait<0>();
      if (lane == 0) { if (u1 >= 160) arrive_relay(ws, 48 + (u1 - 160) / 48, 48u); else arrive_relay(ws, 56 + u1 / 20, 20u); }
      if (two) retile_issue(wo, w2, stg, u2, lane);
      HB(); HB(); vmwait<0>();
      if (two) retile_finish(ws, stp, u2, lane);
      HB(); vmwait<0>();
      if (two && lane == 0) arrive_relay(ws, 56 + u2 / 20, 20u);
      HB(); HB();
#undef HB
    }
  } else {
    const f32x4 b0 = qx[0], b1 = qx[64], b2 = qx[128];
    const f32x4 c0 = *(const LAS f32x4*)(csl + 4 * h), c1 = *(const LAS f32x4*)(csl + 8 + 4 * h), c2 = *(const LAS f32x4*)(csl + 16);
    __builtin_amdgcn_s_setprio(APRIO);
    u32x4 q1a, q1b; u32x2 q2a, q2b;
    {
      const f32x2 sa = *(const LAS f32x2*)((LAS char*)lds + 55296 + r * 8), sb = *(const LAS f32x2*)((LAS char*)lds + 55296 + 256 + r * 8);
      const float ka = sa[1] * QSCALE, kb = sb[1] * QSCALE;
#define QA(i, c) ((qa[i] - sa[0] * c[(i) & 3]) * ka)
#define QB(v, i, c) ((v[i] - sb[0] * c[i]) * kb)
      q1a = (u32x4){cvtpk(QA(0, c0), QA(1, c0)), cvtpk(QA(2, c0), QA(3, c0)), cvtpk(QA(4, c1), QA(5, c1)), cvtpk(QA(6, c1), QA(7, c1))};
      q2a = (u32x2){cvtpk(QA(8, c2), QA(9, c2)), cvtpk(QA(10, c2), QA(11, c2))};
      q1b = (u32x4){cvtpk(QB(b0, 0, c0), QB(b0, 1, c0)), cvtpk(QB(b0, 2, c0), QB(b0, 3, c0)), cvtpk(QB(b1, 0, c1), QB(b1, 1, c1)), cvtpk(QB(b1, 2, c1), QB(b1, 3, c1))};
      q2b = (u32x2){cvtpk(QB(b2, 0, c2), QB(b2, 1, c2)), cvtpk(QB(b2, 2, c2), QB(b2, 3, c2))};
#undef QA
#undef QB
    }
    float km;
    {
      const f32x2 km2 = *(const LAS f32x2*)((LAS char*)lds + 54272 + lane * 8);
      int k = max(__float_as_int(km2[0]), __float_as_int(km2[1]));
      k = max(k, __builtin_amdgcn_update_dpp(0, k, 0x128, 0xf, 0xf, false)); k = max(k, __builtin_amdgcn_update_dpp(0, k, 0x124, 0xf, 0xf, false));
      k = max(k, __builtin_amdgcn_update_dpp(0, k, 0x122, 0xf, 0xf, false)); k = max(k, __builtin_amdgcn_update_dpp(0, k, 0x121, 0xf, 0xf, false));
      km = __int_as_float(max(max(__builtin_amdgcn_readlane(k, 0), __builtin_amdgcn_readlane(k, 16)), max(__builtin_amdgcn_readlane(k, 32), __builtin_amdgcn_readlane(k, 48))));
    }
    float qna = dot2(q1a[0], q1a[0], dot2(q1a[1], q1a[1], dot2(q1a[2], q1a[2], dot2(q1a[3], q1a[3], 0.f))));
    float qnb = dot2(q1b[0], q1b[0], dot2(q1b[1], q1b[1], dot2(q1b[2], q1b[2], dot2(q1b[3], q1b[3], 0.f))));
    if (h == 0) { qna = dot2(q2a[0], q2a[0], dot2(q2a[1], q2a[1], qna)); qnb = dot2(q2b[0], q2b[0], dot2(q2b[1], q2b[1], qnb)); }
    { const auto sw = __builtin_amdgcn_permlane32_swap(__float_as_uint(qna), __float_as_uint(qna), false, false); qna = __uint_as_float(sw[0]) + __uint_as_float(sw[1]); }
    { const auto sw = __builtin_amdgcn_permlane32_swap(__float_as_uint(qnb), __float_as_uint(qnb), false, false); qnb = __uint_as_float(sw[0]) + __uint_as_float(sw[1]); }
    const float bda = __builtin_amdgcn_sqrtf(qna * km) * 1.02f + 1e-3f, bdb = __builtin_amdgcn_sqrtf(qnb * km) * 1.02f + 1e-3f;
    const bool fast = __all(bda <= 48.f && bdb <= 48.f);
    if (h == 1) {
      q2a[0] = fast ? (cvtpk(-bda, 0.f) & 0xffffu) : 0u; q2a[1] = 0u;
      q2b[0] = fast ? (cvtpk(-bdb, 0.f) & 0xffffu) : 0u; q2b[1] = 0u;
    }
    const bf16x8 QA1 = __builtin_bit_cast(bf16x8, q1a), QB1 = __builtin_bit_cast(bf16x8, q1b);
    const s16x4 QA2 = __builtin_bit_cast(s16x4, q2a), QB2 = __builtin_bit_cast(s16x4, q2b);
    const int dv = r <= 20 ? r : 21;
    const LAS char* k1b = (const LAS char*)lds + lane * 16;
    const LAS char* k2b = (const LAS char*)lds + (h ? KV_C : KV_K2 + r * 8);
    const LAS char* vb = (const LAS char*)lds + KV_V + (h * VR + dv) * 16;
    f32x16 oa = {}, ob = {};
    const f32x16 zero16 = {};
    if (fast) {
      f32x16 sA, sB;
      bf16x8 k1f, vf0, vf1; s16x4 k2f;
      u32x4 pa, pb;
      float e14, e15;
      k1f = *(const LAS bf16x8*)(k1b + KV_A); k2f = *(const LAS s16x4*)(k2b + KV_A); vf0 = *(const LAS bf16x8*)(vb + KV_A);
      sA = MFMA32(k1f, QA1, zero16); sA = MFMA32K8(k2f, QA2, sA);
      vf1 = (bf16x8){0, 0, 0, 0, 0, 0, 0, 0}; pb = (u32x4){0u, 0u, 0u, 0u}; e14 = 0.f; e15 = 0.f;
      int cur = KV_A, nxt = KV_B, fre = KV_Cs;
#define STEP(C, N, Q1, Q2, OC, OP, V0, L1, L2, L3, L4) do { \
        N = MFMA32(k1f, Q1, zero16); SBAR(); L1; SBAR(); \
        C[0] = EXP2(C[0]); C[1] = EXP2(C[1]); SBAR(); pb[3] = cvtpk(e14, e15); PIN(pb); SBAR(); \
        C[2] = EXP2(C[2]); C[3] = EXP2(C[3]); SBAR(); pa[0] = cvtpk(C[0], C[1]); PIN(pa); SBAR(); \
        OP = MFMA32(vf1, __builtin_bit_cast(bf16x8, pb), OP); SBAR(); L2; SBAR(); \
        C[4] = EXP2(C[4]); C[5] = EXP2(C[5]); SBAR(); pa[1] = cvtpk(C[2], C[3]); PIN(pa); SBAR(); \
        C[6] = EXP2(C[6]); C[7] = EXP2(C[7]); SBAR(); pa[2] = cvtpk(C[4], C[5]); PIN(pa); SBAR(); \
        N = MFMA32K8(k2f, Q2, N); SBAR(); L3; SBAR(); \
        C[8] = EXP2(C[8]); C[9] = EXP2(C[9]); SBAR(); pa[3] = cvtpk(C[6], C[7]); PIN(pa); SBAR(); \
        C[10] = EXP2(C[10]); C[11] = EXP2(C[11]); SBAR(); pb[0] = cvtpk(C[8], C[9]); PIN(pb); SBAR(); \
        OC = MFMA32(V0, __builtin_bit_cast(bf16x8, pa), OC); SBAR(); L4; SBAR(); \
        C[12] = EXP2(C[12]); C[13] = EXP2(C[13]); SBAR(); pb[1] = cvtpk(C[10], C[11]); PIN(pb); SBAR(); \
        e14 = EXP2(C[14]); e15 = EXP2(C[15]); SBAR(); pb[2] = cvtpk(C[12], C[13]); PIN(pb); PIN(e14); PIN(e15); SBAR(); \
      } while (0)
#define KT(i) do { \
        STEP(sA, sB, QB1, QB2, oa, ob, vf0, k1f = *(const LAS bf16x8*)((i) < 7 ? kc1 + ((i) + 1) * KV_TILE : k1b + nxt), \
             vf1 = *(const LAS bf16x8*)(vc + (i) * KV_TILE + KV_VS), k2f = *(const LAS s16x4*)((i) < 7 ? kc2 + ((i) + 1) * KV_TILE : k2b + nxt), (void)0); \
        STEP(sB, sA, QA1, QA2, ob, oa, vf0, (void)0, (void)0, (void)0, vf0 = *(const LAS bf16x8*)((i) < 7 ? vc + ((i) + 1) * KV_TILE : vb + nxt)); \
      } while (0)
#pragma unroll 1
      for (int c = 0; c < 8; ++c) {
        const LAS char* kc1 = k1b + cur; const LAS char* kc2 = k2b + cur; const LAS char* vc = vb + cur;
        KT(0); KT(1); KT(2);
        asm volatile("s_barrier" ::: "memory");
        KT(3); KT(4); KT(5); KT(6); KT(7);
        const int t = cur; cur = nxt; nxt = fre; fre = t;
      }
      pb[3] = cvtpk(e14, e15);
      ob = MFMA32(vf1, __builtin_bit_cast(bf16x8, pb), ob);
#undef KT
#undef STEP
    } else {
      float ma = -INFINITY, mb = -INFINITY;
      int cur = KV_A, nxt = KV_B, fre = KV_Cs;
#pragma unroll 1
      for (int c = 0; c < 8; ++c) {
#pragma unroll 1
        for (int i = 0; i < 8; ++i) {
          if (i == 3) RAWBAR();
          const int off = cur + i * KV_TILE;
          const bf16x8 k1f = *(const LAS bf16x8*)(k1b + off); const s16x4 k2f = *(const LAS s16x4*)(k2b + off);
          const bf16x8 vf0 = *(const LAS bf16x8*)(vb + off), vf1 = *(const LAS bf16x8*)(vb + off + KV_VS);
#pragma unroll
          for (int qs = 0; qs < 2; ++qs) {
            f32x16 sc = MFMA32(k1f, qs ? QB1 : QA1, zero16); sc = MFMA32K8(k2f, qs ? QB2 : QA2, sc);
            float& m = qs ? mb : ma; f32x16& o = qs ? ob : oa;
            float mx = sc[0];
#pragma unroll
            for (int j = 1; j < 16; ++j) mx = fmaxf(mx, sc[j]);
            mx = fmaxf(mx, __shfl_xor(mx, 32));
            const float mn = fmaxf(m, mx);
            const float al = EXP2(m - mn);
            m = mn;
#pragma unroll
            for (int j = 0; j < 16; ++j) { o[j] *= al; sc[j] = EXP2(sc[j] - mn); }
            const u32x4 pa = {cvtpk(sc[0], sc[1]), cvtpk(sc[2], sc[3]), cvtpk(sc[4], sc[5]), cvtpk(sc[6], sc[7])};
            const u32x4 pb = {cvtpk(sc[8], sc[9]), cvtpk(sc[10], sc[11]), cvtpk(sc[12], sc[13]), cvtpk(sc[14], sc[15])};
            o = MFMA32(vf0, __builtin_bit_cast(bf16x8, pa), o);
            o = MFMA32(vf1, __builtin_bit_cast(bf16x8, pb), o);
          }
        }
        const int t = cur; cur = nxt; nxt = fre; fre = t;
      }
    }
    PIN(oa); PIN(ob);
#pragma unroll
    for (int qs = 0; qs < 2; ++qs) {
      const f32x16 o = qs ? ob : oa;
      const auto sw = __builtin_amdgcn_permlane32_swap(__float_as_uint(o[8]), __float_as_uint(o[8]), false, false);
      const float il = __builtin_amdgcn_rcpf(__uint_as_float(sw[1]));
      LAS char* os = (LAS char*)lds + O_STG + (qs * 32 + r) * 176 + w * 40 + 8 * h;
      *(LAS u32x2*)(os) = (u32x2){cvtpk(o[0] * il, o[1] * il), cvtpk(o[2] * il, o[3] * il)};
      *(LAS u32x2*)(os + 16) = (u32x2){cvtpk(o[4] * il, o[5] * il), cvtpk(o[6] * il, o[7] * il)};
      if (h == 0) *(LAS u32x2*)(os + 32) = (u32x2){cvtpk(o[8] * il, o[9] * il), cvtpk(o[10] * il, o[11] * il)};
    }
  }
  RAWBAR();
  const __amdgpu_buffer_rsrc_t orr = __builtin_amdgcn_make_buffer_rsrc(ws + WS_O, 0, S * D * 2, 0x00020000);
#pragma unroll
  for (int j = 0; j < 2; ++j) {
    const int ci = tid + 512 * j;
    if (ci < 640) {
      const int c = ci >> 6, cc = hg * 10 + c;
      const u32x4 v = *(const LAS u32x4*)((LAS char*)lds + O_STG + lane * 176 + c * 16);
      __builtin_amdgcn_raw_buffer_store_b128(v, orr, ((tt * 20 + (cc >> 2)) * 4 + (lane >> 4)) * 1024 + ((cc & 3) * 16 + (lane & 15)) * 16, 0, 16);
    }
  }
  vmwait<0>();
  RAWBAR();
  if (tid == 0) arrive(ws, 16 + tt);
}

struct Frag3 { bf16x8 x; s16x4 w[6]; };
struct Frag5 { bf16x8 x; bf16x8 w[5]; };
DI void ffn_block(const u16* __restrict__ x, const u16* __restrict__ wk, const u16* __restrict__ wv, const u16* __restrict__ w1, const u16* __restrict__ w3, char* __restrict__ ws, u16* __restrict__ out, char* lds, int b) {
  const int tid = threadIdx.x, lane = tid & 63, w = __builtin_amdgcn_readfirstlane(tid >> 6);
  const int bi = b >> 3;
  const unsigned lds0 = (unsigned)(uintptr_t)(LAS char*)lds, lane16 = lane * 16;
  __builtin_amdgcn_s_setprio(3);
  ln_block(x, wk, wv, ws, lds, b);
  __builtin_amdgcn_s_setprio(GPRIO);
  if (w == 1) wait_kv<16>(ws, (((b & 7) >> 2) * 8 + (bi & 7)) >> 1, 1);
  __builtin_amdgcn_s_sleep(24);
  RAWBAR();
  {
    const int tp = ((b & 7) >> 2) * 8 + (bi & 7), cg = (b & 3) * 4 + (bi >> 3);
    const char* xsb = ws + WS_XN + (size_t)tp * 163840 + w * 1024;
    const int rr = lane / 12, pos = lane - rr * 12;
    const unsigned bs = ((rr & 3) * F + (pos ^ ((w >> 2) << 1)) * 8) * 2;
    const u16* bb = (w < 4 ? w1 : w3) + (size_t)(w & 3) * 8 * F + cg * 96;
    auto piece = [&](int kt, int slot, int j) {
      if (j == 0) glds16s_sc1(xsb + kt * 8192, lane16, lds0 + slot * SLOT + w * 1024);
      else glds_lin<48>((const char*)(bb + (size_t)(kt * 32 + (j - 1) * 4) * F), bs, lds0 + slot * SLOT + 8192 + (2 * w + j - 1) * 768);
    };
#pragma unroll
    for (int i = 0; i < 4; ++i) { piece(i, i, 0); piece(i, i, 1); piece(i, i, 2); }
    const int tq = w & 3, hc = w >> 2, r = lane & 31, h = lane >> 5;
    const int xrow = tq * 32 + r;
    const LAS char* xb0 = (const LAS char*)lds + xrow * 64 + ((h ^ ((xrow >> 2) & 3)) * 16);
    const LAS char* xb1 = (const LAS char*)lds + xrow * 64 + (((2 + h) ^ ((xrow >> 2) & 3)) * 16);
    const int gu = (lane >> 4) & 1, q4 = (lane & 15) >> 2, p = lane & 3, pp = ((p & 1) << 1) | (p >> 1);
    const LAS char* wb[3];
#pragma unroll
    for (int c = 0; c < 3; ++c) wb[c] = (const LAS char*)lds + 8192 + gu * 6144 + (8 * h + q4) * 192 + (((hc * 3 + c) ^ gu) * 32) + pp * 8;
    f32x16 acc[3] = {};
    auto rd = [&](Frag3& f, int slot, int s) {
      f.x = *(const LAS bf16x8*)((s ? xb1 : xb0) + slot * SLOT);
#pragma unroll
      for (int c = 0; c < 3; ++c) { f.w[2 * c] = trread(wb[c] + slot * SLOT + s * 3072); f.w[2 * c + 1] = trread(wb[c] + slot * SLOT + s * 3072 + 768); }
    };
    auto mm = [&](const Frag3& f, int s, int dk, int dsl) {
#pragma unroll
      for (int c = 0; c < 3; ++c) {
        acc[c] = MFMA32(cat8(f.w[2 * c], f.w[2 * c + 1]), f.x, acc[c]);
        SBAR();
        if (dk >= 0) { if (s == 1 && c == 0) piece(dk, dsl, 0); if (s == 1 && c == 2) piece(dk, dsl, 1); if (s == 0 && c == 0) piece(dk, dsl, 2); }
        SBAR();
      }
    };
    Frag3 f0, f1;
    vmwait<9>();
    RAWBAR();
    rd(f0, 0, 0);
#define IT(kt, sl, VM, PREV, DMA, NEXT) do { \
      rd(f1, sl, 1); SBAR(); mm(f0, 0, PREV ? (kt) + 3 : -1, ((sl) + 3) & 3); SBAR(); \
      if (NEXT) { vmwait<VM>(); RAWBAR(); rd(f0, ((sl) + 1) & 3, 0); } \
      SBAR(); mm(f1, 1, DMA ? (kt) + 4 : -1, sl); SBAR(); } while (0)
    IT(0, 0, 6, false, true, true); IT(1, 1, 6, true, true, true); IT(2, 2, 6, true, true, true); IT(3, 3, 6, true, true, true);
#pragma unroll 1
    for (int kt = 4; kt < 16; kt += 4) {
      IT(kt, 0, 6, true, true, true); IT(kt + 1, 1, 6, true, true, true); IT(kt + 2, 2, 6, true, true, true); IT(kt + 3, 3, 6, true, true, true);
    }
    IT(16, 0, 6, true, false, true); IT(17, 1, 3, false, false, true); IT(18, 2, 0, false, false, true); IT(19, 3, 0, false, false, false);
#undef IT
    PIN(acc[0]); PIN(acc[1]); PIN(acc[2]);
    __builtin_amdgcn_s_setprio(3);
    u32x4 hv[3];
#pragma unroll
    for (int c = 0; c < 3; ++c) {
      float v[8];
#pragma unroll
      for (int j = 0; j < 8; ++j) {
        const float gt = acc[c][j], up = acc[c][j + 8];
        v[j] = gt * __builtin_amdgcn_rcpf(1.f + EXP2(-1.4426950408889634f * gt)) * up;
      }
      hv[c] = (u32x4){cvtpk(v[0], v[1]), cvtpk(v[2], v[3]), cvtpk(v[4], v[5]), cvtpk(v[6], v[7])};
    }
#pragma unroll
    for (int c = 0; c < 3; ++c) *(LAS u32x4*)((LAS char*)lds + xrow * 208 + hc * 96 + c * 32 + h * 16) = hv[c];
    RAWBAR();
    const __amdgpu_buffer_rsrc_t hr = __builtin_amdgcn_make_buffer_rsrc(ws + WS_H, 0, S * F * 2, 0x00020000);
#pragma unroll
    for (int j = 0; j < 3; ++j) {
      const int ci = tid + 512 * j, row = ci / 12, pos = ci - row * 12;
      const u32x4 v = *(const LAS u32x4*)((LAS char*)lds + row * 208 + pos * 16);
      __builtin_amdgcn_raw_buffer_store_b128(v, hr, ((tp * 128 + row) * F + cg * 96 + pos * 8) * 2, 0, 16);
    }
    if (w == 1) wait_count(relayp(ws, 48 + (bi >> 2)), 1u);
    vmwait<0>();
    RAWBAR();
    if (tid == 448) arrive(ws, tp);
  }

  const int tt = (b & 7) * 4 + (bi & 3), t0 = tt * 64, cg = bi >> 2, c0 = cg * 80;
  const int ts = w & 3, kh = w >> 2, r16 = lane & 15, gq = lane >> 4, xrow = ts * 16 + r16;
  f32x4 acc[5] = {};
  const char* wog = ws + WS_WO + (size_t)cg * 102400;
  bf16x8 wr[10];
  auto wop = [&](int j) { glds16s_sc1(wog + (j >= 5 ? 10240 : 0) + (j * 8 + w) * 1024, lane16, lds0 + (j * 8 + w) * 1024); };
  {
    const int srow = w * 8 + (lane >> 3);
    const unsigned hs = (srow * F + ((lane & 7) ^ ((srow >> 1) & 7)) * 8) * 2;
    const u16* hb = (const u16*)(ws + WS_H) + (size_t)t0 * F;
    const char* wsrc = ws + WS_W2 + (size_t)cg * 245760 + w * 1280;
    auto piece = [&](int kt, int slot, int j) {
      if (j == 0) glds16s_sc1(hb + kt * 64, hs, lds0 + slot * ST64 + w * 1024);
      else glds_lin_sc1<40>(wsrc + kt * 10240 + (j - 1) * 640, lane16, lds0 + slot * ST64 + 8192 + w * 1280 + (j - 1) * 640);
    };
#pragma unroll
    for (int i = 0; i < 4; ++i) { piece(i, i, 1); piece(i, i, 2); }
    if (kh == 0) {
#pragma unroll
      for (int c = 0; c < 5; ++c) {
        const u32x2 xv = *(const u32x2*)(x + (size_t)(t0 + xrow) * D + c0 + c * 16 + gq * 4);
        acc[c] = (f32x4){bflo(xv[0]), bfhi(xv[0]), bflo(xv[1]), bfhi(xv[1])};
        PIN(acc[c]);
      }
    }
    if (w == 0) wait_count(flagp(ws, tt >> 1), 16u);
    RAWBAR();
    __builtin_amdgcn_s_setprio(GPRIO);

#pragma unroll
    for (int i = 0; i < 4; ++i) piece(i, i, 0);
    const LAS char* xb = (const LAS char*)lds + xrow * 128 + (((kh * 4 + gq) ^ ((xrow >> 1) & 7)) * 16);
    const LAS char* wb = (const LAS char*)lds + 8192 + kh * 5120 + lane * 16;
    auto rd = [&](Frag5& f, int slot) {
      f.x = *(const LAS bf16x8*)(xb + slot * ST64);
#pragma unroll
      for (int c = 0; c < 5; ++c) f.w[c] = *(const LAS bf16x8*)(wb + slot * ST64 + c * 1024);
    };
    auto mm = [&](const Frag5& f, int dk, int dsl) {
#pragma unroll
      for (int c = 0; c < 5; ++c) { acc[c] = MFMA16(f.w[c], f.x, acc[c]); SBAR(); if (dk >= 0 && !(c & 1)) piece(dk, dsl, c >> 1); SBAR(); }
    };
    Frag5 fa, fb;
    vmwait<3>();
    RAWBAR();
    rd(fa, 0);
#define IT(kt, sl, FC, FN, VM, DMA) do { vmwait<VM>(); RAWBAR(); rd(FN, ((sl) + 1) & 3); SBAR(); mm(FC, DMA ? (kt) + 4 : -1, sl); SBAR(); } while (0)
    IT(0, 0, fa, fb, 2, true); IT(1, 1, fb, fa, 4, true); IT(2, 2, fa, fb, 6, true); IT(3, 3, fb, fa, 6, true);
#pragma unroll 1
    for (int kt = 4; kt < 20; kt += 4) {
      IT(kt, 0, fa, fb, 6, true); IT(kt + 1, 1, fb, fa, 6, true); IT(kt + 2, 2, fa, fb, 6, true); IT(kt + 3, 3, fb, fa, 6, true);
    }
    vmwait<6>();
    if (w == 1) wait_count(relayp(ws, 56 + cg), 1u);
    IT(20, 0, fa, fb, 6, false); wop(0); wop(1); wop(9);
    {
      const char* wrp = wog + kh * 51200 + 40960;
#pragma unroll
      for (int j = 0; j < 10; ++j) asm volatile("global_load_dwordx4 %0, %1, %2 sc1" : "=v"(wr[j]) : "v"(lane16), "s"(wrp + j * 1024) : "memory");
    }
    IT(21, 1, fb, fa, 16, false); wop(2); wop(3);
    IT(22, 2, fa, fb, 15, false); wop(4); wop(5);
    mm(fb, -1, 0);
#undef IT
  }
  PIN(acc[0]);
  RAWBAR();
  wop(6); wop(7); wop(8);
  asm volatile("s_waitcnt vmcnt(0)" : "+v"(wr[0]), "+v"(wr[1]), "+v"(wr[2]), "+v"(wr[3]), "+v"(wr[4]), "+v"(wr[5]), "+v"(wr[6]), "+v"(wr[7]), "+v"(wr[8]), "+v"(wr[9]) :: "memory");
  vmwait<0>();
  __builtin_amdgcn_s_setprio(3);
  const LAS char* wbt = (const LAS char*)lds + kh * 40960 + lane * 16;
  if (w == 0) wait_count(flagp(ws, 16 + tt), 8u);
  RAWBAR();

  {
    u32x4 of[10];
    const char* op = ws + WS_O + ((tt * 20 + kh * 10) * 4 + ts) * 1024;
#pragma unroll
    for (int s = 0; s < 10; ++s) asm volatile("global_load_dwordx4 %0, %1, %2 sc1" : "=v"(of[(s + 8) % 10]) : "v"(lane16), "s"(op + ((s + 8) % 10) * 4096) : "memory");
#define OSTEP(s, N, WC, WN) do { \
      if ((s) + 1 < 8) { _Pragma("unroll") for (int c = 0; c < 5; ++c) WN[c] = *(const LAS bf16x8*)(wbt + ((s) + 1) * 5120 + c * 1024); } \
      SBAR(); asm volatile("s_waitcnt vmcnt(" #N ")" : "+v"(of[s]) :: "memory"); SBAR(); \
      _Pragma("unroll") for (int c = 0; c < 5; ++c) acc[c] = MFMA16(WC[c], __builtin_bit_cast(bf16x8, of[s]), acc[c]); SBAR(); } while (0)
    asm volatile("s_waitcnt vmcnt(9)" : "+v"(of[8]) :: "memory");
#pragma unroll
    for (int c = 0; c < 5; ++c) acc[c] = MFMA16(wr[c], __builtin_bit_cast(bf16x8, of[8]), acc[c]);
    SBAR();
    bf16x8 w0[5];
#pragma unroll
    for (int c = 0; c < 5; ++c) w0[c] = *(const LAS bf16x8*)(wbt + c * 1024);
    SBAR(); asm volatile("s_waitcnt vmcnt(8)" : "+v"(of[9]) :: "memory"); SBAR();
#pragma unroll
    for (int c = 0; c < 5; ++c) acc[c] = MFMA16(wr[5 + c], __builtin_bit_cast(bf16x8, of[9]), acc[c]);
    SBAR();
    bf16x8 wn[5];
    OSTEP(0, 7, w0, wn); OSTEP(1, 6, wn, w0); OSTEP(2, 5, w0, wn); OSTEP(3, 4, wn, w0);
    OSTEP(4, 3, w0, wn); OSTEP(5, 2, wn, w0); OSTEP(6, 1, w0, wn); OSTEP(7, 0, wn, w0);
#undef OSTEP
  }
  RAWBAR();
#pragma unroll
  for (int c = 0; c < 5; ++c) *(LAS f32x4*)((LAS char*)lds + kh * 64 * 336 + xrow * 336 + c * 64 + gq * 16) = acc[c];
  RAWBAR();
#pragma unroll
  for (int j = 0; j < 2; ++j) {
    const int ci = tid + 512 * j;
    if (ci < 640) {
      const int row = ci / 10, c = ci - row * 10;
      const LAS f32x4* a0 = (const LAS f32x4*)((LAS char*)lds + row * 336 + c * 32);
      const LAS f32x4* a1 = (const LAS f32x4*)((LAS char*)lds + 64 * 336 + row * 336 + c * 32);
      const f32x4 lo = a0[0] + a1[0], hi = a0[1] + a1[1];
      const u32x4 res = {cvtpk(lo[0], lo[1]), cvtpk(lo[2], lo[3]), cvtpk(hi[0], hi[1]), cvtpk(hi[2], hi[3])};
      *(u32x4*)(out + (size_t)(t0 + row) * D + c0 + c * 8) = res;
    }
  }
}

__global__ __launch_bounds__(512, 4) void k_main(const u16* __restrict__ x, const u16* __restrict__ wq, const u16* __restrict__ wk,
                                                  const u16* __restrict__ wv, const u16* __restrict__ wo, const u16* __restrict__ w1,
                                                  const u16* __restrict__ w3, const u16* __restrict__ w2, char* __restrict__ ws,
                                                  u16* __restrict__ out) {
  extern __shared__ __attribute__((aligned(16))) char lds[];
  if (blockIdx.x >= 256) attn_block(x, wq, w2, wo, ws, lds, blockIdx.x - 256);
  else ffn_block(x, wk, wv, w1, w3, ws, out, lds, blockIdx.x);
}

extern "C" void kernel_launch(void* const* d_in, const int* in_sizes, int n_in, void* d_out, int out_size, void* d_ws, size_t ws_size,
                              hipStream_t stream) {
  const u16* x = (const u16*)d_in[0];
  const u16* wq = (const u16*)d_in[1];
  const u16* wk = (const u16*)d_in[2];
  const u16* wv = (const u16*)d_in[3];
  const u16* wo = (const u16*)d_in[4];
  const u16* w1 = (const u16*)d_in[6];
  const u16* w3 = (const u16*)d_in[7];
  const u16* w2 = (const u16*)d_in[8];
  char* ws = (char*)d_ws;
  (void)hipFuncSetAttribute((const void*)k_main, hipFuncAttributeMaxDynamicSharedMemorySize, LDS_BYTES);
  k_main<<<512, 512, LDS_BYTES, stream>>>(x, wq, wk, wv, wo, w1, w3, w2, ws, (u16*)d_out);
}
```
